```python
import torch

def _softmax(x, axis=-1):
    return torch.softmax(x, dim=axis)
def _gelu(x):
    return torch.nn.functional.gelu(x, approximate="tanh")
def _silu(x):
    return torch.nn.functional.silu(x)
def _sigmoid(x):
    return torch.sigmoid(x)
def _layernorm(x, axis=-1, eps=1e-5):
    m = x.mean(dim=axis, keepdim=True)
    v = x.var(dim=axis, keepdim=True, unbiased=False)
    return (x - m) / torch.sqrt(v + eps)
def _scatter_add(tgt, idx, src, axis=0):
    return tgt.clone().index_add_(axis, idx, src)
def _segment_sum(src, seg, n):
    out = torch.zeros((n,) + src.shape[1:], dtype=src.dtype)
    return out.index_add_(0, seg, src)
def _pad(x, pw):
    flat = tuple(v for pair in reversed(pw) for v in pair)
    return torch.nn.functional.pad(x, flat)

kernel_name = 'concat_heads__gqa_block_cached__gqa_block_cached__near_ridge'

def setup_inputs():

    _w = (lambda a: (lambda t: t.to(torch.bfloat16) if t.is_floating_point() else t)(torch.as_tensor(a)))
    def _A_setup():
        B, Sq, D = 16, 96, 512
        Hq, Hkv, Sk, Dh = 32, 32, 96, 128
        assert Hq % Hkv == 0, (Hq, Hkv)
        return {
            "x": _w((lambda *s: torch.randn(*s, dtype=torch.bfloat16) * 0.8)(B, Sq, D)),
            "k_cache": _w((lambda *s: torch.randn(*s, dtype=torch.bfloat16) * 0.8)(B, Hkv, Sk, Dh)),
            "v_cache": _w((lambda *s: torch.randn(*s, dtype=torch.bfloat16) * 0.05)(B, Hkv, Sk, Dh)),
            "wq": _w((lambda *s: torch.randn(*s, dtype=torch.bfloat16) * 0.8)(D, Hq * Dh) * (D ** -0.5)),
            "wo": _w((lambda *s: torch.randn(*s, dtype=torch.bfloat16) * 0.05)(Hq * Dh, D) * ((Hq * Dh) ** -0.5)),
        }
    _A_in = _A_setup()

    def _B_setup():
        B, Sq, D = 16, 96, 512
        Hq, Hkv, Sk, Dh = 32, 32, 96, 128
        assert Hq % Hkv == 0, (Hq, Hkv)
        return {
            "x": _w((lambda *s: torch.randn(*s, dtype=torch.bfloat16) * 0.8)(B, Sq, D)),
            "k_cache": _w((lambda *s: torch.randn(*s, dtype=torch.bfloat16) * 0.8)(B, Hkv, Sk, Dh)),
            "v_cache": _w((lambda *s: torch.randn(*s, dtype=torch.bfloat16) * 0.05)(B, Hkv, Sk, Dh)),
            "wq": _w((lambda *s: torch.randn(*s, dtype=torch.bfloat16) * 0.8)(D, Hq * Dh) * (D ** -0.5)),
            "wo": _w((lambda *s: torch.randn(*s, dtype=torch.bfloat16) * 0.05)(Hq * Dh, D) * ((Hq * Dh) ** -0.5)),
        }
    _B_in = _B_setup()

    return {
        "A_x": _A_in["x"],
        "A_k_cache": _A_in["k_cache"],
        "A_v_cache": _A_in["v_cache"],
        "A_wq": _A_in["wq"],
        "A_wo": _A_in["wo"],
        "B_x": _B_in["x"],
        "B_k_cache": _B_in["k_cache"],
        "B_v_cache": _B_in["v_cache"],
        "B_wq": _B_in["wq"],
        "B_wo": _B_in["wo"],
    }

def reference(A_x, A_k_cache, A_v_cache, A_wq, A_wo, B_x, B_k_cache, B_v_cache, B_wq, B_wo):
    def _A_ref(x, k_cache, v_cache, wq, wo):
        B, Sq, D = x.shape
        Hq, Dh = 32, 128
        Hkv = k_cache.shape[1]
        rep = Hq // Hkv
        q = (lambda x, *d: x.permute(*d))((x @ wq).reshape(B, Sq, Hq, Dh), 0, 2, 1, 3)
        k = (lambda x, n, axis: torch.repeat_interleave(x, n, dim=axis))(k_cache, rep, axis=1)
        v = (lambda x, n, axis: torch.repeat_interleave(x, n, dim=axis))(v_cache, rep, axis=1)
        scores = torch.einsum("bhqd,bhkd->bhqk", q, k) * (Dh ** -0.5)
        p = _softmax(scores, axis=-1)
        o = torch.einsum("bhqk,bhkd->bhqd", p, v)
        return (lambda x, *d: x.permute(*d))(o, 0, 2, 1, 3).reshape(B, Sq, Hq * Dh) @ wo

    def _B_ref(x, k_cache, v_cache, wq, wo):
        B, Sq, D = x.shape
        Hq, Dh = 32, 128
        Hkv = k_cache.shape[1]
        rep = Hq // Hkv
        q = (lambda x, *d: x.permute(*d))((x @ wq).reshape(B, Sq, Hq, Dh), 0, 2, 1, 3)
        k = (lambda x, n, axis: torch.repeat_interleave(x, n, dim=axis))(k_cache, rep, axis=1)
        v = (lambda x, n, axis: torch.repeat_interleave(x, n, dim=axis))(v_cache, rep, axis=1)
        scores = torch.einsum("bhqd,bhkd->bhqk", q, k) * (Dh ** -0.5)
        p = _softmax(scores, axis=-1)
        o = torch.einsum("bhqk,bhkd->bhqd", p, v)
        return (lambda x, *d: x.permute(*d))(o, 0, 2, 1, 3).reshape(B, Sq, Hq * Dh) @ wo

    a = _A_ref(A_x, A_k_cache, A_v_cache, A_wq, A_wo)
    b = _B_ref(B_x, B_k_cache, B_v_cache, B_wq, B_wo)
    return torch.concatenate([a, b], axis=-1)
```

```cpp
#include <hip/hip_runtime.h>
#include <hip/hip_bf16.h>


typedef unsigned u32x2 __attribute__((ext_vector_type(2)));
typedef unsigned u32x4 __attribute__((ext_vector_type(4)));
typedef float f32x4 __attribute__((ext_vector_type(4)));
typedef short bf16x8 __attribute__((ext_vector_type(8)));

#define DI __device__ __forceinline__

constexpr int SQ = 96, DM = 512, NH = 32, DH = 128, NBATCH = 16;
constexpr int RSRC_W3 = 0x00020000;

DI f32x4 mfma(u32x4 a, u32x4 b, f32x4 c) {
  return __builtin_amdgcn_mfma_f32_16x16x32_bf16(__builtin_bit_cast(bf16x8, a), __builtin_bit_cast(bf16x8, b), c, 0, 0, 0);
}
DI unsigned pk_bf16(float lo, float hi) {
  __hip_bfloat16 a = __float2bfloat16(lo), b = __float2bfloat16(hi);
  return (unsigned)__builtin_bit_cast(unsigned short, a) | ((unsigned)__builtin_bit_cast(unsigned short, b) << 16);
}
DI unsigned perm_lo(unsigned e, unsigned o) { return __builtin_amdgcn_perm(o, e, 0x05040100u); }
DI unsigned perm_hi(unsigned e, unsigned o) { return __builtin_amdgcn_perm(o, e, 0x07060302u); }
#define SCHED __builtin_amdgcn_sched_barrier(0)

struct Ptrs {
  const unsigned short* x[2];
  const unsigned short* k[2];
  const unsigned short* v[2];
  const unsigned short* wq[2];
  const unsigned short* wo[2];
};

DI int img_off(int row, int chunk) { return row * 1024 + ((chunk ^ (row & 15)) << 4); }

template <int RS, int PF>
DI void stream_gemm(__amdgpu_buffer_rsrc_t rs, int voff, const char* lds, int lane, f32x4 (&acc)[4][6]) {
  const int q = lane & 15, g = lane >> 4;
  u32x2 W[PF][8];
#pragma unroll
  for (int s = 0; s < PF; ++s)
#pragma unroll
    for (int i = 0; i < 8; ++i) W[s][i] = __builtin_amdgcn_raw_buffer_load_b64(rs, voff, (32 * s + i) * RS, 0);
  SCHED;
#pragma unroll
  for (int s = 0; s < 16; ++s) {
    u32x4 B[6];
#pragma unroll
    for (int t = 0; t < 6; ++t) B[t] = *(const u32x4*)(lds + img_off(16 * t + q, 4 * s + g));
    u32x4 A[4];
#pragma unroll
    for (int p = 0; p < 4; ++p) {
      A[0][p] = perm_lo(W[s % PF][2 * p][0], W[s % PF][2 * p + 1][0]);
      A[1][p] = perm_hi(W[s % PF][2 * p][0], W[s % PF][2 * p + 1][0]);
      A[2][p] = perm_lo(W[s % PF][2 * p][1], W[s % PF][2 * p + 1][1]);
      A[3][p] = perm_hi(W[s % PF][2 * p][1], W[s % PF][2 * p + 1][1]);
    }
    if (s + PF < 16) {
#pragma unroll
      for (int i = 0; i < 8; ++i)
        W[s % PF][i] = __builtin_amdgcn_raw_buffer_load_b64(rs, voff, (32 * (s + PF) + i) * RS, 0);
    }
#pragma unroll
    for (int t = 0; t < 6; ++t)
#pragma unroll
      for (int f = 0; f < 4; ++f) acc[f][t] = mfma(A[f], B[t], acc[f][t]);
    SCHED;
  }
}

__global__ __launch_bounds__(512, 2) void k1_qproj_attn(Ptrs p, unsigned short* __restrict__ ows) {
  extern __shared__ __attribute__((aligned(16))) char lds[];
  const int bid = blockIdx.x;
  const int xcd = bid & 7, idx = bid >> 3;
  const int br = xcd >> 2, hg = (xcd & 3) * 2 + (idx & 1), b = idx >> 1;
  const int tid = threadIdx.x, lane = tid & 63;
  const int w = __builtin_amdgcn_readfirstlane(tid >> 6);
  const int q = lane & 15, g = lane >> 4;

  {
    const char* xb = (const char*)(p.x[br] + (size_t)b * SQ * DM);
#pragma unroll
    for (int i = 0; i < 12; ++i) {
      const int row = w * 12 + i;
      __builtin_amdgcn_global_load_lds((const unsigned*)(xb + row * 1024 + ((lane ^ (row & 15)) << 4)),
                                       (unsigned*)(lds + row * 1024), 16, 0, 0);
    }
  }
  asm volatile("s_waitcnt vmcnt(0)" ::: "memory");
  __syncthreads();

  f32x4 acc[4][6];
#pragma unroll
  for (int f = 0; f < 4; ++f)
#pragma unroll
    for (int t = 0; t < 6; ++t) acc[f][t] = f32x4{0.f, 0.f, 0.f, 0.f};
  {
    __amdgpu_buffer_rsrc_t rs = __builtin_amdgcn_make_buffer_rsrc((void*)p.wq[br], 0, DM * NH * DH * 2, RSRC_W3);
    const int voff = (8 * g) * 8192 + (hg * 512 + w * 64 + 4 * q) * 2;
    stream_gemm<8192, 4>(rs, voff, lds, lane, acc);
  }
  __syncthreads();
#pragma unroll
  for (int t = 0; t < 6; ++t) {
    u32x4 c0, c1;
    c0[0] = pk_bf16(acc[0][t][0], acc[1][t][0]);
    c0[1] = pk_bf16(acc[2][t][0], acc[3][t][0]);
    c0[2] = pk_bf16(acc[0][t][1], acc[1][t][1]);
    c0[3] = pk_bf16(acc[2][t][1], acc[3][t][1]);
    c1[0] = pk_bf16(acc[0][t][2], acc[1][t][2]);
    c1[1] = pk_bf16(acc[2][t][2], acc[3][t][2]);
    c1[2] = pk_bf16(acc[0][t][3], acc[1][t][3]);
    c1[3] = pk_bf16(acc[2][t][3], acc[3][t][3]);
    *(u32x4*)(lds + img_off(16 * t + q, 8 * w + 2 * g)) = c0;
    *(u32x4*)(lds + img_off(16 * t + q, 8 * w + 2 * g + 1)) = c1;
  }
  __syncthreads();

  const int hl = w >> 1, qh = w & 1;
  const int head = hg * 4 + hl;
  const size_t kvoff = ((size_t)b * NH + head) * SQ * DH;
  __amdgpu_buffer_rsrc_t rk = __builtin_amdgcn_make_buffer_rsrc((void*)(p.k[br] + kvoff), 0, SQ * DH * 2, RSRC_W3);
  __amdgpu_buffer_rsrc_t rv = __builtin_amdgcn_make_buffer_rsrc((void*)(p.v[br] + kvoff), 0, SQ * DH * 2, RSRC_W3);

  f32x4 sacc[6][3];
  u32x4 Vr[3][8];
  {
    u32x4 Kf[6][4];
#pragma unroll
    for (int kt = 0; kt < 6; ++kt)
#pragma unroll
      for (int s = 0; s < 4; ++s)
        Kf[kt][s] = __builtin_amdgcn_raw_buffer_load_b128(rk, q * 256 + g * 16, kt * 4096 + s * 64, 0);
    u32x4 Qf[3][4];
#pragma unroll
    for (int t = 0; t < 3; ++t)
#pragma unroll
      for (int s = 0; s < 4; ++s) Qf[t][s] = *(const u32x4*)(lds + img_off(16 * (3 * qh + t) + q, 16 * hl + 4 * s + g));
    SCHED;
#pragma unroll
    for (int kt = 0; kt < 6; ++kt) {
#pragma unroll
      for (int t = 0; t < 3; ++t) {
        f32x4 a = f32x4{0.f, 0.f, 0.f, 0.f};
#pragma unroll
        for (int s = 0; s < 4; ++s) a = mfma(Kf[kt][s], Qf[t][s], a);
        sacc[kt][t] = a;
      }
      SCHED;
#pragma unroll
      for (int jj = 0; jj < 4; ++jj) {
        const int t3 = kt >> 1, j = (kt & 1) * 4 + jj;
        Vr[t3][j] = __builtin_amdgcn_raw_buffer_load_b128(rv, g * 1024 + q * 16, (32 * t3 + 16 * (j >> 2) + (j & 3)) * 256, 0);
      }
      SCHED;
    }
  }

  const float c = 0.08838834764831845f * 1.4426950408889634f;
  u32x4 Pf[3][3];
  float linv[3];
#pragma unroll
  for (int t = 0; t < 3; ++t) {
    float m = sacc[0][t][0];
#pragma unroll
    for (int kt = 0; kt < 6; ++kt)
#pragma unroll
      for (int r = 0; r < 4; ++r) m = fmaxf(m, sacc[kt][t][r]);
    m = fmaxf(m, __shfl_xor(m, 16));
    m = fmaxf(m, __shfl_xor(m, 32));
    const float mc = m * c;
    float l = 0.f;
    float e[6][4];
#pragma unroll
    for (int kt = 0; kt < 6; ++kt)
#pragma unroll
      for (int r = 0; r < 4; ++r) {
        e[kt][r] = __builtin_amdgcn_exp2f(sacc[kt][t][r] * c - mc);
        l += e[kt][r];
      }
    l += __shfl_xor(l, 16);
    l += __shfl_xor(l, 32);
    linv[t] = 1.0f / l;
#pragma unroll
    for (int t3 = 0; t3 < 3; ++t3) {
      Pf[t3][t][0] = pk_bf16(e[2 * t3][0], e[2 * t3][1]);
      Pf[t3][t][1] = pk_bf16(e[2 * t3][2], e[2 * t3][3]);
      Pf[t3][t][2] = pk_bf16(e[2 * t3 + 1][0], e[2 * t3 + 1][1]);
      Pf[t3][t][3] = pk_bf16(e[2 * t3 + 1][2], e[2 * t3 + 1][3]);
    }
    SCHED;
  }
  f32x4 oacc[8][3];
#pragma unroll
  for (int f = 0; f < 8; ++f)
#pragma unroll
    for (int t = 0; t < 3; ++t) oacc[f][t] = f32x4{0.f, 0.f, 0.f, 0.f};
#pragma unroll
  for (int t3 = 0; t3 < 3; ++t3) {
#pragma unroll
    for (int f = 0; f < 8; ++f) {
      u32x4 A;
#pragma unroll
      for (int pp = 0; pp < 4; ++pp)
        A[pp] = (f & 1) ? perm_hi(Vr[t3][2 * pp][f >> 1], Vr[t3][2 * pp + 1][f >> 1])
                        : perm_lo(Vr[t3][2 * pp][f >> 1], Vr[t3][2 * pp + 1][f >> 1]);
#pragma unroll
      for (int t = 0; t < 3; ++t) oacc[f][t] = mfma(A, Pf[t3][t], oacc[f][t]);
    }
    SCHED;
  }
  {
    unsigned short* ob = ows + ((size_t)(br * NBATCH + b) * 128 + head * 4 + g) * (6 * 512);
#pragma unroll
    for (int t = 0; t < 3; ++t) {
      const float li = linv[t];
#pragma unroll
      for (int r = 0; r < 4; ++r) {
        u32x4 o;
        o[0] = pk_bf16(oacc[0][t][r] * li, oacc[1][t][r] * li);
        o[1] = pk_bf16(oacc[2][t][r] * li, oacc[3][t][r] * li);
        o[2] = pk_bf16(oacc[4][t][r] * li, oacc[5][t][r] * li);
        o[3] = pk_bf16(oacc[6][t][r] * li, oacc[7][t][r] * li);
        *(u32x4*)(ob + (3 * qh + t) * 512 + (q + 16 * r) * 8) = o;
      }
    }
  }
}

__global__ __launch_bounds__(512, 2) void k2_oproj(Ptrs p, const unsigned short* __restrict__ ows,
                                                   unsigned short* __restrict__ out) {
  extern __shared__ __attribute__((aligned(16))) char lds[];
  const int bid = blockIdx.x;
  const int xcd = bid & 7, idx = bid >> 3;
  const int br = xcd >> 2, b = (xcd & 3) * 4 + (idx >> 3), j = idx & 7;
  const int tid = threadIdx.x, lane = tid & 63;
  const int w = __builtin_amdgcn_readfirstlane(tid >> 6);
  const int q = lane & 15, g = lane >> 4;

  __amdgpu_buffer_rsrc_t rs = __builtin_amdgcn_make_buffer_rsrc((void*)(p.wo[br] + (size_t)w * 512 * DM), 0, 512 * DM * 2, RSRC_W3);
  __amdgpu_buffer_rsrc_t ro = __builtin_amdgcn_make_buffer_rsrc(
      (void*)(ows + ((size_t)(br * NBATCH + b) * 128 + w * 16) * (6 * 512)), 0, 16 * 6 * 1024, RSRC_W3);
  const int voff = (8 * g) * 1024 + (64 * j + 4 * q) * 2;

  f32x4 acc[4][6];
#pragma unroll
  for (int f = 0; f < 4; ++f)
#pragma unroll
    for (int t = 0; t < 6; ++t) acc[f][t] = f32x4{0.f, 0.f, 0.f, 0.f};

  constexpr int PF = 3;
  u32x2 W[PF][8];
  u32x4 Bq[PF][6];
#pragma unroll
  for (int s = 0; s < PF; ++s) {
#pragma unroll
    for (int i = 0; i < 8; ++i) W[s][i] = __builtin_amdgcn_raw_buffer_load_b64(rs, voff, (32 * s + i) * 1024, 0);
#pragma unroll
    for (int t = 0; t < 6; ++t) Bq[s][t] = __builtin_amdgcn_raw_buffer_load_b128(ro, lane * 16, (s * 6 + t) * 1024, 0);
  }
  SCHED;
#pragma unroll
  for (int s = 0; s < 16; ++s) {
    u32x4 A[4];
#pragma unroll
    for (int pp = 0; pp < 4; ++pp) {
      A[0][pp] = perm_lo(W[s % PF][2 * pp][0], W[s % PF][2 * pp + 1][0]);
      A[1][pp] = perm_hi(W[s % PF][2 * pp][0], W[s % PF][2 * pp + 1][0]);
      A[2][pp] = perm_lo(W[s % PF][2 * pp][1], W[s % PF][2 * pp + 1][1]);
      A[3][pp] = perm_hi(W[s % PF][2 * pp][1], W[s % PF][2 * pp + 1][1]);
    }
    if (s + PF < 16) {
#pragma unroll
      for (int i = 0; i < 8; ++i)
        W[s % PF][i] = __builtin_amdgcn_raw_buffer_load_b64(rs, voff, (32 * (s + PF) + i) * 1024, 0);
    }
#pragma unroll
    for (int t = 0; t < 6; ++t)
#pragma unroll
      for (int f = 0; f < 4; ++f) acc[f][t] = mfma(A[f], Bq[s % PF][t], acc[f][t]);
    if (s + PF < 16) {
#pragma unroll
      for (int t = 0; t < 6; ++t)
        Bq[s % PF][t] = __builtin_amdgcn_raw_buffer_load_b128(ro, lane * 16, ((s + PF) * 6 + t) * 1024, 0);
    }
    SCHED;
  }
  unsigned short* ob = out + (size_t)b * SQ * 1024 + br * 512 + 64 * j;
#pragma unroll
  for (int rd = 0; rd < 2; ++rd) {
    if (rd) __syncthreads();
#pragma unroll
    for (int t = 0; t < 3; ++t)
#pragma unroll
      for (int r = 0; r < 4; ++r) {
        f32x4 v = f32x4{acc[0][3 * rd + t][r], acc[1][3 * rd + t][r], acc[2][3 * rd + t][r], acc[3][3 * rd + t][r]};
        *(f32x4*)(lds + (((w * 3 + t) * 4 + r) * 64 + lane) * 16) = v;
      }
    __syncthreads();
    for (int it = tid; it < 768; it += 512) {
      f32x4 s = *(const f32x4*)(lds + it * 16);
#pragma unroll
      for (int ww = 1; ww < 8; ++ww) s += *(const f32x4*)(lds + (ww * 768 + it) * 16);
      const int t = it >> 8, r = (it >> 6) & 3, ln = it & 63;
      const int row = 16 * (3 * rd + t) + (ln & 15), col = 16 * (ln >> 4) + 4 * r;
      u32x2 o;
      o[0] = pk_bf16(s[0], s[1]);
      o[1] = pk_bf16(s[2], s[3]);
      *(u32x2*)(ob + (size_t)row * 1024 + col) = o;
    }
  }
}

extern "C" void kernel_launch(void* const* d_in, const int* in_sizes, int n_in, void* d_out, int out_size, void* d_ws,
                              size_t ws_size, hipStream_t stream) {
  Ptrs p;
  for (int br = 0; br < 2; ++br) {
    p.x[br] = (const unsigned short*)d_in[5 * br + 0];
    p.k[br] = (const unsigned short*)d_in[5 * br + 1];
    p.v[br] = (const unsigned short*)d_in[5 * br + 2];
    p.wq[br] = (const unsigned short*)d_in[5 * br + 3];
    p.wo[br] = (const unsigned short*)d_in[5 * br + 4];
  }
  unsigned short* ows = (unsigned short*)d_ws;
  hipFuncSetAttribute((const void*)k1_qproj_attn, hipFuncAttributeMaxDynamicSharedMemorySize, 98304);
  hipFuncSetAttribute((const void*)k2_oproj, hipFuncAttributeMaxDynamicSharedMemorySize, 98304);
  k1_qproj_attn<<<256, 512, 98304, stream>>>(p, ows);
  k2_oproj<<<256, 512, 98304, stream>>>(p, ows, (unsigned short*)d_out);
}
```

```cpp
#include <hip/hip_runtime.h>
#include <hip/hip_bf16.h>
#include <utility>

typedef unsigned u32x2 __attribute__((ext_vector_type(2)));
typedef unsigned u32x4 __attribute__((ext_vector_type(4)));
typedef float f32x4 __attribute__((ext_vector_type(4)));
#define LDSP __attribute__((address_space(3)))
#define DI __device__ __forceinline__
typedef __amdgpu_buffer_rsrc_t rsrc_t;

constexpr int SQ = 96, DM = 512, NH = 32, DH = 128, NBATCH = 16;
constexpr int RSRC_W3 = 0x00020000;
#define POL_KV " nt"
#define POL_W ""
#define POL_X ""
#define AUX_PST 0
#define AUX_PLD 0

template <class F, int... I>
DI void sfor_impl(F&& f, std::integer_sequence<int, I...>) {
  (f(std::integral_constant<int, I>{}), ...);
}
template <int N, class F>
DI void sfor(F&& f) {
  sfor_impl(f, std::make_integer_sequence<int, N>{});
}
template <int N>
using IC = std::integral_constant<int, N>;

typedef float f32x2 __attribute__((ext_vector_type(2)));
typedef __bf16 bf16x2 __attribute__((ext_vector_type(2)));
DI unsigned pk_bf16(float lo, float hi) {
  return __builtin_bit_cast(unsigned, __builtin_convertvector(f32x2{lo, hi}, bf16x2));
}
DI unsigned perm_lo(unsigned e, unsigned o) { return __builtin_amdgcn_perm(o, e, 0x05040100u); }
DI unsigned perm_hi(unsigned e, unsigned o) { return __builtin_amdgcn_perm(o, e, 0x07060302u); }
#define SCHED __builtin_amdgcn_sched_barrier(0)

#define MF "v_mfma_f32_16x16x32_bf16 "
template <int D, int Bq, bool Z>
DI void mf_a_va(const u32x4& A) {
  if constexpr (Z) asm volatile(MF "a[%c0:%c1], %2, a[%c3:%c4], 0" ::"i"(D), "i"(D + 3), "v"(A), "i"(Bq), "i"(Bq + 3));
  else asm volatile(MF "a[%c0:%c1], %2, a[%c3:%c4], a[%c0:%c1]" ::"i"(D), "i"(D + 3), "v"(A), "i"(Bq), "i"(Bq + 3));
}
template <int Bq, bool Z>
DI void mf_v_va(f32x4& C, const u32x4& A) {
  if constexpr (Z) asm volatile(MF "%0, %1, a[%c2:%c3], 0" : "=&v"(C) : "v"(A), "i"(Bq), "i"(Bq + 3));
  else asm volatile(MF "%0, %1, a[%c2:%c3], %0" : "+v"(C) : "v"(A), "i"(Bq), "i"(Bq + 3));
}
template <int Aq, bool Z>
DI void mf_v_av(f32x4& C, const u32x4& Bv) {
  if constexpr (Z) asm volatile(MF "%0, a[%c2:%c3], %1, 0" : "=&v"(C) : "v"(Bv), "i"(Aq), "i"(Aq + 3));
  else asm volatile(MF "%0, a[%c2:%c3], %1, %0" : "+v"(C) : "v"(Bv), "i"(Aq), "i"(Aq + 3));
}
template <bool Z>
DI void mf_v_vv(f32x4& C, const u32x4& A, const u32x4& Bv) {
  if constexpr (Z) asm volatile(MF "%0, %1, %2, 0" : "=&v"(C) : "v"(A), "v"(Bv));
  else asm volatile(MF "%0, %1, %2, %0" : "+v"(C) : "v"(A), "v"(Bv));
}
template <int X, int Y>
DI unsigned pk_acc() {
  unsigned r, t0, t1;
  asm volatile("v_accvgpr_read_b32 %1, a[%c3]\n\tv_accvgpr_read_b32 %2, a[%c4]\n\tv_cvt_pk_bf16_f32 %0, %1, %2"
               : "=v"(r), "=&v"(t0), "=&v"(t1)
               : "i"(X), "i"(Y));
  return r;
}
template <int IMM>
DI void ld_w(u32x4& d, rsrc_t r, int voff, int soff) {
  asm volatile("buffer_load_dwordx4 %0, %1, %2, %3 offen offset:%c4" POL_W : "=v"(d) : "v"(voff), "s"(r), "s"(soff), "i"(IMM));
}
template <int D, int IMM>
DI void ld_k(rsrc_t r, int voff, int soff) {
  asm volatile("buffer_load_dwordx4 a[%c0:%c1], %2, %3, %4 offen offset:%c5" POL_KV ::"i"(D), "i"(D + 3), "v"(voff), "s"(r),
               "s"(soff), "i"(IMM));
}
template <int D>
DI void ld_pf(rsrc_t r, int voff, int soff) {
  asm volatile("buffer_load_dwordx4 a[%c0:%c1], %2, %3, %4 offen" POL_W ::"i"(D), "i"(D + 3), "v"(voff), "s"(r), "s"(soff));
}
DI void dma_x(int m0v, rsrc_t r, int voff, int soff) {
  asm volatile("s_mov_b32 m0, %0\n\ts_nop 0\n\tbuffer_load_dwordx4 %1, %2, %3 offen" POL_X " lds" ::"s"(m0v), "v"(voff), "s"(r),
               "s"(soff)
               : "memory");
}
DI void dma_v(int m0v, rsrc_t r, int voff, int soff) {
  asm volatile("s_mov_b32 m0, %0\n\ts_nop 0\n\tbuffer_load_dwordx4 %1, %2, %3 offen" POL_KV " lds" ::"s"(m0v), "v"(voff), "s"(r),
               "s"(soff)
               : "memory");
}
template <int D, int IMM>
DI void lds_rd_a(int addr) {
  asm volatile("ds_read_b128 a[%c0:%c1], %2 offset:%c3" ::"i"(D), "i"(D + 3), "v"(addr), "i"(IMM) : "memory");
}
template <int D, int IMM>
DI void lds_rdtr_a(int addr) {
  asm volatile("ds_read_b64_tr_b16 a[%c0:%c1], %2 offset:%c3" ::"i"(D), "i"(D + 1), "v"(addr), "i"(IMM) : "memory");
}
template <int D, int IMM>
DI void lds_wr_a(int addr) {
  asm volatile("ds_write_b128 %2, a[%c0:%c1] offset:%c3" ::"i"(D), "i"(D + 3), "v"(addr), "i"(IMM) : "memory");
}
template <int IMM>
DI void lds_wr(int addr, const u32x4& d) {
  asm volatile("ds_write_b128 %0, %1 offset:%c2" ::"v"(addr), "v"(d), "i"(IMM) : "memory");
}
template <int N>
DI void wait_vm() {
  static_assert(N >= 0 && N < 64, "vmcnt");
  asm volatile("s_waitcnt vmcnt(%c0)" ::"i"(N) : "memory");
}
DI void wait_lgkm0() { asm volatile("s_waitcnt lgkmcnt(0)" ::: "memory"); }
DI void barrier() { asm volatile("s_barrier" ::: "memory"); }
template <int N>
DI void nop() {
  asm volatile("s_nop %c0" ::"i"(N));
}
DI float max3(float a, float b, float c) {
  float r;
  asm volatile("v_max3_f32 %0, %1, %2, %3" : "=v"(r) : "v"(a), "v"(b), "v"(c));
  return r;
}
DI float max2(float a, float b) {
  float r;
  asm volatile("v_max_f32 %0, %1, %2" : "=v"(r) : "v"(a), "v"(b));
  return r;
}
DI float xmax16(float m) {
  float t;
  asm volatile("v_mov_b32 %1, %0\n\ts_nop 1\n\tv_permlane16_swap_b32 %0, %1\n\tv_max_f32 %0, %0, %1" : "+v"(m), "=&v"(t));
  return m;
}
DI float xmax32(float m) {
  float t;
  asm volatile("v_mov_b32 %1, %0\n\ts_nop 1\n\tv_permlane32_swap_b32 %0, %1\n\tv_max_f32 %0, %0, %1" : "+v"(m), "=&v"(t));
  return m;
}

#define STAMP(i)

struct Ptrs {
  const unsigned short* x[2];
  const unsigned short* k[2];
  const unsigned short* v[2];
  const unsigned short* wq[2];
  const unsigned short* wo[2];
};

constexpr int LDS_BYTES = 163840;
constexpr int XQ_BYTES = 24576, V1_BASE = 98304;

constexpr int A_B = 112, A_K = 160, A_V = 0;
constexpr bool tile_v(int f, int t) { return t < 2 || (t == 2 && f < 4); }
constexpr int tile_vi(int f, int t) { return t * 8 + f; }
constexpr int tile_a(int f, int t) { return 4 * (t >= 3 ? (t - 3) * 8 + f : 24 + (f - 4)); }

#define PF_WQ 8
#define PF_WO 8
constexpr int A_NP = 6 + 8 + 8 + 18 + 8 + PF_WQ + 6;
constexpr int a_cnt(int s) { return (s <= 12 ? 8 : 0) + (s <= 8 ? 2 : 0) + (s >= 13 ? 8 : 0); }
constexpr int a_base(int s) {
  int c = A_NP;
  for (int i = 0; i < s; ++i) c += a_cnt(i);
  return c;
}
constexpr int a_lastw(int step) { return step == 0 ? 13 : step == 1 ? 21 : step == 2 ? 47 : a_base(step - 3) + 7; }
constexpr int a_wait(int s) { return a_base(s) + (s <= 12 ? a_cnt(s) : 7) - a_lastw(s + 1) - 1; }

DI unsigned aperm(const u32x4 (&W)[8], int f, int p) {
  return (f & 1) ? perm_hi(W[2 * p][f >> 1], W[2 * p + 1][f >> 1]) : perm_lo(W[2 * p][f >> 1], W[2 * p + 1][f >> 1]);
}

__global__ __launch_bounds__(256, 1) void k1_fused(Ptrs p, u32x4* __restrict__ part
) {
  extern __shared__ __attribute__((aligned(16))) char lds[];
  asm volatile("" ::: "a0", "a255");
  const int bid = blockIdx.x;
  const int xcd = bid & 7, idx = bid >> 3;
  const int br = xcd >> 2, hg = (xcd & 3) * 2 + (idx & 1), b = idx >> 1;
  const int tid = threadIdx.x, lane = tid & 63;
  const int w = __builtin_amdgcn_readfirstlane(tid >> 6);
  const int q = lane & 15, g = lane >> 4;
  const int cchunk = q;
  const int lb = (int)(unsigned)(size_t)(LDSP char*)lds;
  STAMP(0);

  const int head = hg * 4 + w;
  const size_t kvoff = ((size_t)b * NH + head) * SQ * DH;
  const rsrc_t rx = __builtin_amdgcn_make_buffer_rsrc((void*)(p.x[br] + (size_t)b * SQ * DM), 0, SQ * DM * 2, RSRC_W3);
  const rsrc_t rk = __builtin_amdgcn_make_buffer_rsrc((void*)(p.k[br] + kvoff), 0, SQ * DH * 2, RSRC_W3);
  const rsrc_t rv = __builtin_amdgcn_make_buffer_rsrc((void*)(p.v[br] + kvoff), 0, SQ * DH * 2, RSRC_W3);
  const rsrc_t rq = __builtin_amdgcn_make_buffer_rsrc((void*)p.wq[br], 0, DM * NH * DH * 2, RSRC_W3);
  const rsrc_t ro = __builtin_amdgcn_make_buffer_rsrc((void*)(p.wo[br] + (size_t)hg * 512 * DM), 0, 512 * DM * 2, RSRC_W3);

  f32x4 accv[20];
  u32x4 W[4][8];
  u32x4 A[2];

  auto gmf = [&](auto FI, auto TI, auto BI, auto ZI, const u32x4& Af) {
    constexpr int f = FI, t = TI, bq = A_B + 24 * BI + 4 * t;
    constexpr bool z = ZI;
    if constexpr (tile_v(f, t)) mf_v_va<bq, z>(accv[tile_vi(f, t)], Af);
    else mf_a_va<tile_a(f, t), bq, z>(Af);
  };
  auto gpk = [&](auto PI, auto TI, auto RI) -> unsigned {
    constexpr int pp = PI, t = TI, r = RI;
    if constexpr (tile_v(2 * pp, t)) return pk_bf16(accv[tile_vi(2 * pp, t)][r], accv[tile_vi(2 * pp + 1, t)][r]);
    else return pk_acc<tile_a(2 * pp, t) + r, tile_a(2 * pp + 1, t) + r>();
  };

  const int vx = g * 1024 + ((q ^ g ^ (4 * w)) << 4);
  const int vwq = (8 * g) * 8192 + (hg * 512 + w * 128 + 8 * cchunk) * 2;
  nop<4>();
  const int vk = lane * 16;
  sfor<6>([&](auto I) { dma_x(lb + (4 * I + w) * 1024, rx, vx, (4 * I + w) * 4096); });
  sfor<16>([&](auto I) { ld_w<0>(W[I >> 3][I & 7], rq, vwq, (32 * (I >> 3) + (I & 7)) * 8192); });
  sfor<18>([&](auto I) {
    constexpr int qt = 1 + I / 6, i = I % 6;
    dma_x(lb + qt * XQ_BYTES + (4 * i + w) * 1024, rx, vx, (4 * i + w) * 4096 + qt * 256);
  });
  sfor<8>([&](auto I) { ld_w<0>(W[2][I], rq, vwq, (64 + I) * 8192); });
  sfor<PF_WQ>([&](auto I) { ld_pf<A_K + 64 + 4 * I>(rq, vk, (32 * b + 8 * w + I) * 8192 + hg * 1024); });
  sfor<6>([&](auto I) { ld_k<A_K + 4 * I, (I & 3) * 1024>(rk, vk, (I >> 2) * 4096); });
  int xa[2][4];
#pragma unroll
  for (int h = 0; h < 2; ++h)
#pragma unroll
    for (int j = 0; j < 4; ++j) xa[h][j] = lb + h * 2 * XQ_BYTES + q * 256 + (((4 * j + g) ^ q) << 4);
  const int kst = lb + V1_BASE + w * 16384 + 16384 - 16 * 272;
  const int kwa = kst + g * 272 + q * 16, kra = kst + q * 272 + g * 64;
  const int vv0 = g * 256 + ((q ^ (((g >> 1) << 2) | (g & 1))) << 4);
  const int vv1 = vv0 ^ 128;
  SCHED;
  wait_vm<A_NP - 6>();
  barrier();
  STAMP(1);

  auto xrd = [&](auto SI, auto TI) {
    constexpr int s = SI, t = TI;
    lds_rd_a<A_B + 24 * (s & 1) + 4 * t, ((s >> 2) & 1) * XQ_BYTES + t * 4096>(xa[s >> 3][s & 3]);
  };
  sfor<6>([&](auto T) { xrd(IC<0>{}, T); });
  wait_vm<A_NP - 14>();
  SCHED;
#pragma unroll
  for (int pp = 0; pp < 4; ++pp) A[0][pp] = aperm(W[0], 0, pp);
  SCHED;
  wait_lgkm0();
  nop<0>();
  SCHED;
  sfor<128>([&](auto NI) {
    constexpr int n = NI, s = n >> 3, f = n & 7;
    constexpr int kt = s == 3 ? 0 : (s >= 4 && s <= 12 && !(s & 1)) ? s / 2 - 1 : -1;
    constexpr int ktc = kt < 0 ? 0 : kt;
    if constexpr (n == 24 || n == 64) {
      barrier();
      SCHED;
    }
    sfor<6>([&](auto TI) {
      constexpr int t = TI;
      gmf(IC<f>{}, TI, IC<(s & 1)>{}, std::bool_constant<s == 0>{}, A[n & 1]);
      SCHED;
      if constexpr ((t == 0 || t == 2) && f < 4 && s <= 12) {
        constexpr int i = 2 * f + (t >> 1);
        ld_w<0>(W[(s + 3) & 3][i], rq, vwq, (32 * (s + 3) + i) * 8192);
      }
      if constexpr (t == 0 && (f == 4 || f == 5) && s <= 8) {
        constexpr int j = 6 + 2 * s + (f - 4);
        ld_k<A_K + 4 * j, (j & 3) * 1024>(rk, vk, (j >> 2) * 4096);
      }
      if constexpr (t == 0 && f == 7 && s < 15) wait_vm<a_wait(s)>();
      if constexpr (t == 0 && s >= 13) {
        constexpr int j = 8 * (s - 13) + f;
        dma_v(j < 16 ? lb + V1_BASE + w * 16384 + j * 1024 : lb + w * 8192 + (j - 16) * 1024, rv, (j & 1) ? vv1 : vv0, j * 1024);
      }
      if constexpr (t == 5) {
        if constexpr (s + 1 < 16 && f < 6) xrd(IC<s + 1>{}, IC<f>{});
        else if constexpr (s + 1 < 16 && f == 7) wait_lgkm0();
        else if constexpr (kt >= 0 && f == 6) lds_rd_a<A_K + 16 * ktc + 12, 48>(kra);
        else nop<0>();
      }
      if constexpr (n + 1 < 128 && t >= 1 && t <= 4) A[(n + 1) & 1][t - 1] = aperm(W[((n + 1) >> 3) & 3], (n + 1) & 7, t - 1);
      if constexpr (t == 3 && kt >= 0) {
        if constexpr (f < 4) lds_wr_a<A_K + 16 * ktc + 4 * f, f * 4 * 272>(kwa);
        else if constexpr (f < 7) lds_rd_a<A_K + 16 * ktc + 4 * (f - 4), (f - 4) * 16>(kra);
      }
      SCHED;
    });
  });
  sfor<PF_WO>([&](auto I) { ld_pf<A_B + 4 * I>(ro, vk, (32 * b + 8 * w + I) * 1024); });
  STAMP(2);

  u32x4 Qf[2][4];
  f32x4 sacc[2][6];
  u32x4 Pf[6][3];
  f32x4 oacc[2][8];
  f32x4 lacc[2];
  float mA = 0.f, mB = 0.f, mc = 0.f, li = 0.f;
  float ev[24];
  const float c = 0.08838834764831845f * 1.4426950408889634f;

  auto qp = [&](auto TI, auto JI) {
    constexpr int t = TI, j = JI, pp = j >> 2, s = j & 3;
    Qf[t & 1][s][pp] = gpk(IC<pp>{}, TI, IC<s>{});
  };
  auto sm = [&](auto TI, auto JI) {
    constexpr int t = TI, j = JI;
    const f32x4(&S)[6] = sacc[t & 1];
    auto v = [&](int i) -> float { return S[i >> 2][i & 3]; };
    if constexpr (j == 0) mA = max3(v(0), v(1), v(2));
    else if constexpr (j == 1) mB = max3(v(3), v(4), v(5));
    else if constexpr (j < 11) {
      if constexpr (j & 1) mB = max3(mB, v(2 * j + 2), v(2 * j + 3));
      else mA = max3(mA, v(2 * j + 2), v(2 * j + 3));
    } else if constexpr (j == 11) mA = max2(mA, mB);
    else if constexpr (j == 12) mA = xmax16(mA);
    else if constexpr (j == 13) mA = xmax32(mA);
    else if constexpr (j == 14) mc = -mA * c;
    else if constexpr (j < 15 + 24) {
      constexpr int i = j - 15;
      ev[i] = __builtin_amdgcn_exp2f(__builtin_fmaf(v(i), c, mc));
    } else {
      constexpr int i = j - 39;
      Pf[t][i >> 2][i & 3] = pk_bf16(ev[2 * i], ev[2 * i + 1]);
    }
  };
  constexpr int SM_N = 51;
  const int vo = lane * 16;
  const int oa1 = lb + V1_BASE + w * 16384 + vo, oa2 = lb + w * 8192 + vo;
  u32x4 ot;
  auto oe = [&](auto TI, auto JI) {
    constexpr int t = TI, j = JI;
    if constexpr (j == 0) {
      asm volatile("" : "+v"(lacc[t & 1]));
      li = __builtin_amdgcn_rcpf(lacc[t & 1][0]);
    }
    else {
      constexpr int i = j - 1, sp = i / 5, e = i % 5;
      if constexpr (e < 4) {
        const f32x4& o = oacc[t & 1][2 * sp + (e >> 1)];
        ot[e] = pk_bf16(o[2 * (e & 1)] * li, o[2 * (e & 1) + 1] * li);
      } else {
        constexpr int fi = sp * 6 + t;
        if constexpr (fi < 16) lds_wr<fi * 1024>(oa1, ot);
        else lds_wr<(fi - 16) * 1024>(oa2, ot);
      }
    }
  };
  constexpr int OE_N = 21;
  const int vtr = (4 * g + ((lane >> 2) & 3)) * 256 +
                  (((lane & 3) ^ (((g & 1) << 3) | (((lane >> 3) & 1) << 2) | ((lane >> 2) & 1))) << 4) + 8 * (lane & 1);
  const int vt1 = lb + V1_BASE + w * 16384 + vtr, vt2 = lb + w * 8192 + vtr;
  auto vrd = [&](auto JI) {
    constexpr int j = JI, f = j / 6, t3 = (j % 6) >> 1, hh = j & 1, sp = f >> 1, h = f & 1;
    constexpr int x = (sp << 6) ^ (h << 3), d = A_V + 4 * (t3 * 8 + f) + 2 * hh;
    if constexpr (t3 < 2) lds_rdtr_a<d, (32 * t3 + 16 * hh) * 256>(vt1 ^ x);
    else lds_rdtr_a<d, 16 * hh * 256>(vt2 ^ x);
  };
  const int vwoA = (8 * g + 4 * (g & 1)) * 1024 + (w * 128 + 8 * cchunk) * 2;
  const int vwoB = (8 * g - 4 * (g & 1) + 4) * 1024 + (w * 128 + 8 * cchunk) * 2;
  auto wold = [&](auto SI, auto II) {
    constexpr int S = SI, i = II;
    const int so = (((w + (S >> 2)) & 3) * 128 + 32 * (S & 3)) * 1024;
    ld_w<(i & 3) * 1024>(W[S & 3][i], ro, i < 4 ? vwoA : vwoB, so);
  };

  sfor<16>([&](auto J) { qp(IC<0>{}, J); });
  SCHED;
  nop<1>();
  sfor<6>([&](auto TI) {
    constexpr int t = TI;
    constexpr int NQ = t < 5 ? 16 : 0, NS = t > 0 ? SM_N : 0, NV = t == 5 ? 48 : 0;
    constexpr int NF = NQ + NS + NV;
    if constexpr (t == 5) wait_vm<PF_WO>();
    sfor<24>([&](auto MI) {
      constexpr int m = MI, s = m / 6, kt = m % 6;
      mf_v_av<A_K + 4 * (kt * 4 + s), s == 0>(sacc[t & 1][kt], Qf[t & 1][s]);
      SCHED;
      constexpr int lo = m * NF / 22 < NF ? m * NF / 22 : NF, hi = (m + 1) * NF / 22 < NF ? (m + 1) * NF / 22 : NF;
      sfor<hi - lo>([&](auto KI) {
        constexpr int k = lo + KI;
        if constexpr (k < NS) sm(IC<t - 1>{}, IC<k>{});
        else if constexpr (k < NS + NQ) qp(IC<t + 1>{}, IC<k - NS>{});
        else vrd(IC<k - NS - NQ>{});
      });
      SCHED;
    });
  });
  wait_lgkm0();
  STAMP(3);
  u32x4 ones = u32x4{0x3F803F80u, 0x3F803F80u, 0x3F803F80u, 0x3F803F80u};
  asm volatile("" : "+v"(ones));
  nop<1>();
  sfor<6>([&](auto TI) {
    constexpr int t = TI;
    constexpr int NS = t == 0 ? SM_N : 0, NO = t > 0 ? OE_N : 0, NW = t >= 3 ? 8 : 0;
    constexpr int NF = NS + NO + NW;
    sfor<27>([&](auto MI) {
      constexpr int m = MI, t3 = m / 9, f = m % 9;
      if constexpr (f > 0) mf_v_av<A_V + 4 * (t3 * 8 + f - 1), t3 == 0>(oacc[t & 1][f - 1], Pf[t][t3]);
      else mf_v_vv<t3 == 0>(lacc[t & 1], ones, Pf[t][t3]);
      SCHED;
      constexpr int lo = m * NF / 27, hi = (m + 1) * NF / 27;
      sfor<hi - lo>([&](auto KI) {
        constexpr int k = lo + KI;
        if constexpr (k < NS) sm(IC<5>{}, IC<k>{});
        else if constexpr (k < NS + NO) oe(IC<t - 1>{}, IC<k - NS>{});
        else wold(IC<t - 3>{}, IC<k - NS - NO>{});
      });
      SCHED;
    });
  });
  sfor<OE_N>([&](auto J) { oe(IC<5>{}, J); });
  SCHED;
  STAMP(4);

  int ob[4][2];
#pragma unroll
  for (int h = 0; h < 4; ++h) {
    const int wp = (w + h) & 3;
    ob[h][0] = lb + V1_BASE + wp * 16384 + vo;
    ob[h][1] = lb + wp * 8192 + vo;
  }
  auto ord = [&](auto SI, auto TI) {
    constexpr int S = SI, t = TI, fi = (S & 3) * 6 + t;
    if constexpr (fi < 16) lds_rd_a<A_B + 24 * (S & 1) + 4 * t, fi * 1024>(ob[S >> 2][0]);
    else lds_rd_a<A_B + 24 * (S & 1) + 4 * t, (fi - 16) * 1024>(ob[S >> 2][1]);
  };
  wait_vm<16>();
  sfor<6>([&](auto T) { ord(IC<0>{}, T); });
  SCHED;
#pragma unroll
  for (int pp = 0; pp < 4; ++pp) A[0][pp] = aperm(W[0], 0, pp);
  SCHED;
  wait_lgkm0();
  nop<0>();
  SCHED;
  sfor<128>([&](auto NI) {
    constexpr int n = NI, s = n >> 3, f = n & 7;
    if constexpr (n == 24) {
      barrier();
      SCHED;
    }
    sfor<6>([&](auto TI) {
      constexpr int t = TI;
      gmf(IC<f>{}, TI, IC<(s & 1)>{}, std::bool_constant<s == 0>{}, A[n & 1]);
      SCHED;
      if constexpr ((t == 0 || t == 2) && f < 4 && s + 3 < 16) wold(IC<s + 3>{}, IC<(2 * f + (t >> 1)) & 7>{});
      if constexpr (t == 0 && f == 7 && s < 15) wait_vm<(s <= 12 ? 16 : s == 13 ? 8 : 0)>();
      if constexpr (t == 5) {
        if constexpr (s + 1 < 16 && f < 6) ord(IC<s + 1>{}, IC<f>{});
        else if constexpr (s + 1 < 16 && f == 7) wait_lgkm0();
        else nop<0>();
      }
      if constexpr (n + 1 < 128 && t >= 1 && t <= 4) A[(n + 1) & 1][t - 1] = aperm(W[((n + 1) >> 3) & 3], (n + 1) & 7, t - 1);
      SCHED;
    });
  });
  STAMP(5);
  {
    const rsrc_t rp = __builtin_amdgcn_make_buffer_rsrc((void*)(part + (size_t)(((br * NBATCH + b) * 8 + hg) * 4 + w) * 24 * 64), 0,
                                                        24 * 1024, RSRC_W3);
    nop<7>();
    sfor<24>([&](auto I) {
      constexpr int t = I >> 2, r = I & 3;
      u32x4 o;
      sfor<4>([&](auto P) { o[(int)P] = gpk(P, IC<t>{}, IC<r>{}); });
      __builtin_amdgcn_raw_buffer_store_b128(o, rp, vo + (I & 3) * 1024, (I >> 2) * 4096, AUX_PST);
      SCHED;
      nop<1>();
      SCHED;
    });
  }
  STAMP(6);
}

__global__ __launch_bounds__(256) void k2_reduce(const u32x4* __restrict__ part, unsigned short* __restrict__ out) {
  const int i = blockIdx.x * 256 + threadIdx.x;
  const int bb = i / 6144, e = i % 6144;
  const rsrc_t rp = __builtin_amdgcn_make_buffer_rsrc((void*)(part + (size_t)bb * 8 * 6144), 0, 8 * 6144 * 16, RSRC_W3);
  u32x4 v[8];
#pragma unroll
  for (int h = 0; h < 8; ++h) v[h] = __builtin_amdgcn_raw_buffer_load_b128(rp, e * 16, h * 6144 * 16, AUX_PLD);
  float s[8];
#pragma unroll
  for (int j = 0; j < 8; ++j) s[j] = 0.f;
#pragma unroll
  for (int h = 0; h < 8; ++h)
#pragma unroll
    for (int j = 0; j < 4; ++j) {
      s[2 * j] += __builtin_bit_cast(float, v[h][j] << 16);
      s[2 * j + 1] += __builtin_bit_cast(float, v[h][j] & 0xffff0000u);
    }
  u32x4 o;
#pragma unroll
  for (int j = 0; j < 4; ++j) o[j] = pk_bf16(s[2 * j], s[2 * j + 1]);
  const int lane = e & 63, pc = e >> 6, r = pc & 3, t = (pc >> 2) % 6, w = pc / 24;
  const int q = lane & 15, g = lane >> 4;
  const int br = bb >> 4, b = bb & 15;
  *(u32x4*)(out + ((size_t)b * SQ + 16 * t + q) * 1024 + br * 512 + 128 * w + 32 * g + 8 * r) = o;
}

extern "C" void kernel_launch(void* const* d_in, const int* in_sizes, int n_in, void* d_out, int out_size, void* d_ws,
                              size_t ws_size, hipStream_t stream) {
  Ptrs p;
  for (int br = 0; br < 2; ++br) {
    p.x[br] = (const unsigned short*)d_in[5 * br + 0];
    p.k[br] = (const unsigned short*)d_in[5 * br + 1];
    p.v[br] = (const unsigned short*)d_in[5 * br + 2];
    p.wq[br] = (const unsigned short*)d_in[5 * br + 3];
    p.wo[br] = (const unsigned short*)d_in[5 * br + 4];
  }
  u32x4* part = (u32x4*)d_ws;
  (void)hipFuncSetAttribute((const void*)k1_fused, hipFuncAttributeMaxDynamicSharedMemorySize, LDS_BYTES);
  k1_fused<<<256, 256, LDS_BYTES, stream>>>(p, part
  );
  k2_reduce<<<2 * 16 * 96 * 64 / 256, 256, 0, stream>>>(part, (unsigned short*)d_out);
}
```

```cpp
#include <hip/hip_runtime.h>
#include <hip/hip_bf16.h>
#include <utility>

typedef unsigned u32x2 __attribute__((ext_vector_type(2)));
typedef unsigned u32x4 __attribute__((ext_vector_type(4)));
typedef float f32x4 __attribute__((ext_vector_type(4)));
#define LDSP __attribute__((address_space(3)))
#define DI __device__ __forceinline__
typedef __amdgpu_buffer_rsrc_t rsrc_t;

constexpr int SQ = 96, DM = 512, NH = 32, DH = 128, NBATCH = 16;
constexpr int RSRC_W3 = 0x00020000;
#define POL_KV " nt"
#define POL_W ""
#define POL_PF ""
#define POL_X ""
#define AUX_PST 16
#define AUX_PLD 0

template <class F, int... I>
DI void sfor_impl(F&& f, std::integer_sequence<int, I...>) {
  (f(std::integral_constant<int, I>{}), ...);
}
template <int N, class F>
DI void sfor(F&& f) {
  sfor_impl(f, std::make_integer_sequence<int, N>{});
}
template <int N>
using IC = std::integral_constant<int, N>;

typedef float f32x2 __attribute__((ext_vector_type(2)));
typedef __bf16 bf16x2 __attribute__((ext_vector_type(2)));
DI unsigned pk_bf16(float lo, float hi) {
  return __builtin_bit_cast(unsigned, __builtin_convertvector(f32x2{lo, hi}, bf16x2));
}
DI unsigned perm_lo(unsigned e, unsigned o) { return __builtin_amdgcn_perm(o, e, 0x05040100u); }
DI unsigned perm_hi(unsigned e, unsigned o) { return __builtin_amdgcn_perm(o, e, 0x07060302u); }
#define SCHED __builtin_amdgcn_sched_barrier(0)

#define MF "v_mfma_f32_16x16x32_bf16 "
template <int D, int Bq, bool Z>
DI void mf_a_va(const u32x4& A) {
  if constexpr (Z) asm volatile(MF "a[%c0:%c1], %2, a[%c3:%c4], 0" ::"i"(D), "i"(D + 3), "v"(A), "i"(Bq), "i"(Bq + 3));
  else asm volatile(MF "a[%c0:%c1], %2, a[%c3:%c4], a[%c0:%c1]" ::"i"(D), "i"(D + 3), "v"(A), "i"(Bq), "i"(Bq + 3));
}
template <int Bq, bool Z>
DI void mf_v_va(f32x4& C, const u32x4& A) {
  if constexpr (Z) asm volatile(MF "%0, %1, a[%c2:%c3], 0" : "=&v"(C) : "v"(A), "i"(Bq), "i"(Bq + 3));
  else asm volatile(MF "%0, %1, a[%c2:%c3], %0" : "+v"(C) : "v"(A), "i"(Bq), "i"(Bq + 3));
}
template <int Aq, bool Z>
DI void mf_v_av(f32x4& C, const u32x4& Bv) {
  if constexpr (Z) asm volatile(MF "%0, a[%c2:%c3], %1, 0" : "=&v"(C) : "v"(Bv), "i"(Aq), "i"(Aq + 3));
  else asm volatile(MF "%0, a[%c2:%c3], %1, %0" : "+v"(C) : "v"(Bv), "i"(Aq), "i"(Aq + 3));
}
template <bool Z>
DI void mf_v_vv(f32x4& C, const u32x4& A, const u32x4& Bv) {
  if constexpr (Z) asm volatile(MF "%0, %1, %2, 0" : "=&v"(C) : "v"(A), "v"(Bv));
  else asm volatile(MF "%0, %1, %2, %0" : "+v"(C) : "v"(A), "v"(Bv));
}
template <int X, int Y>
DI unsigned pk_acc() {
  unsigned r, t0, t1;
  asm volatile("v_accvgpr_read_b32 %1, a[%c3]\n\tv_accvgpr_read_b32 %2, a[%c4]\n\tv_cvt_pk_bf16_f32 %0, %1, %2"
               : "=v"(r), "=&v"(t0), "=&v"(t1)
               : "i"(X), "i"(Y));
  return r;
}
template <int IMM>
DI void ld_w(u32x4& d, rsrc_t r, int voff, int soff) {
  asm volatile("buffer_load_dwordx4 %0, %1, %2, %3 offen offset:%c4" POL_W : "=v"(d) : "v"(voff), "s"(r), "s"(soff), "i"(IMM));
}
template <int D, int IMM>
DI void ld_k(rsrc_t r, int voff, int soff) {
  asm volatile("buffer_load_dwordx4 a[%c0:%c1], %2, %3, %4 offen offset:%c5" POL_KV ::"i"(D), "i"(D + 3), "v"(voff), "s"(r),
               "s"(soff), "i"(IMM));
}
template <int D>
DI void ld_pf(rsrc_t r, int voff, int soff) {
  asm volatile("buffer_load_dwordx4 a[%c0:%c1], %2, %3, %4 offen" POL_PF ::"i"(D), "i"(D + 3), "v"(voff), "s"(r), "s"(soff));
}
DI void ld_pfv(u32x4& d, rsrc_t r, int voff, int soff) {
  asm volatile("buffer_load_dwordx4 %0, %1, %2, %3 offen" POL_PF : "+v"(d) : "v"(voff), "s"(r), "s"(soff));
}
DI void dma_x(int m0v, rsrc_t r, int voff, int soff) {
  asm volatile("s_mov_b32 m0, %0\n\ts_nop 0\n\tbuffer_load_dwordx4 %1, %2, %3 offen" POL_X " lds" ::"s"(m0v), "v"(voff), "s"(r),
               "s"(soff)
               : "memory");
}
DI void dma_v(int m0v, rsrc_t r, int voff, int soff) {
  asm volatile("s_mov_b32 m0, %0\n\ts_nop 0\n\tbuffer_load_dwordx4 %1, %2, %3 offen" POL_KV " lds" ::"s"(m0v), "v"(voff), "s"(r),
               "s"(soff)
               : "memory");
}
template <int D, int IMM>
DI void lds_rd_a(int addr) {
  asm volatile("ds_read_b128 a[%c0:%c1], %2 offset:%c3" ::"i"(D), "i"(D + 3), "v"(addr), "i"(IMM) : "memory");
}
template <int D, int IMM>
DI void lds_rdtr_a(int addr) {
  asm volatile("ds_read_b64_tr_b16 a[%c0:%c1], %2 offset:%c3" ::"i"(D), "i"(D + 1), "v"(addr), "i"(IMM) : "memory");
}
template <int D, int IMM>
DI void lds_wr_a(int addr) {
  asm volatile("ds_write_b128 %2, a[%c0:%c1] offset:%c3" ::"i"(D), "i"(D + 3), "v"(addr), "i"(IMM) : "memory");
}
template <int IMM>
DI void lds_wr(int addr, const u32x4& d) {
  asm volatile("ds_write_b128 %0, %1 offset:%c2" ::"v"(addr), "v"(d), "i"(IMM) : "memory");
}
template <int N>
DI void wait_vm() {
  static_assert(N >= 0 && N < 64, "vmcnt");
  asm volatile("s_waitcnt vmcnt(%c0)" ::"i"(N) : "memory");
}
DI void wait_lgkm0() { asm volatile("s_waitcnt lgkmcnt(0)" ::: "memory"); }
DI void barrier() { asm volatile("s_barrier" ::: "memory"); }
template <int N>
DI void nop() {
  asm volatile("s_nop %c0" ::"i"(N));
}
DI float max3(float a, float b, float c) {
  float r;
  asm volatile("v_max3_f32 %0, %1, %2, %3" : "=v"(r) : "v"(a), "v"(b), "v"(c));
  return r;
}
DI float max2(float a, float b) {
  float r;
  asm volatile("v_max_f32 %0, %1, %2" : "=v"(r) : "v"(a), "v"(b));
  return r;
}
DI float xmax16(float m) {
  float t;
  asm volatile("v_mov_b32 %1, %0\n\ts_nop 1\n\tv_permlane16_swap_b32 %0, %1\n\tv_max_f32 %0, %0, %1" : "+v"(m), "=&v"(t));
  return m;
}
DI float xmax32(float m) {
  float t;
  asm volatile("v_mov_b32 %1, %0\n\ts_nop 1\n\tv_permlane32_swap_b32 %0, %1\n\tv_max_f32 %0, %0, %1" : "+v"(m), "=&v"(t));
  return m;
}

#define STAMP(i)

struct Ptrs {
  const unsigned short* x[2];
  const unsigned short* k[2];
  const unsigned short* v[2];
  const unsigned short* wq[2];
  const unsigned short* wo[2];
};

constexpr int LDS_BYTES = 163840;
constexpr int XQ_BYTES = 24576, V1_BASE = 98304;

constexpr int A_B = 112, A_K = 160, A_V = 0;
constexpr bool tile_v(int f, int t) { return t < 2 || (t == 2 && f < 4); }
constexpr int tile_vi(int f, int t) { return t * 8 + f; }
constexpr int tile_a(int f, int t) { return 4 * (t >= 3 ? (t - 3) * 8 + f : 24 + (f - 4)); }

#define PF_WO 8
enum { OX = 1000, OK_ = 2000, OP = 3000, OV = 4000, OW = 5000 };
struct ASched {
  int op[16][24];
  bool bar[16];
};
constexpr ASched a_make() {
  ASched a{};
  const int burst[6][8] = {{OX + 6, OX + 7, OX + 8, OX + 9, OX + 10, OX + 11, OP + 2, OP + 3},
                           {OX + 12, OX + 13, OX + 14, OX + 15, OX + 16, OX + 17, OP + 4, OP + 5},
                           {OK_ + 0, OK_ + 1, OK_ + 2, OK_ + 3, OK_ + 4, OK_ + 5, OK_ + 6, OK_ + 7},
                           {OX + 18, OX + 19, OX + 20, OX + 21, OX + 22, OX + 23, OP + 6, OP + 7},
                           {OK_ + 8, OK_ + 9, OK_ + 10, OK_ + 11, OK_ + 12, OK_ + 13, OK_ + 14, OK_ + 15},
                           {OK_ + 16, OK_ + 17, OK_ + 18, OK_ + 19, OK_ + 20, OK_ + 21, OK_ + 22, OK_ + 23}};
  for (int s = 0; s <= 12; s += 2)
    for (int i = 0; i < 16; ++i) a.op[s][(i >> 1) * 3 + (i & 1)] = OW + 8 * (s + 2) + i;
  for (int s = 1; s <= 11; s += 2) {
    a.bar[s] = true;
    for (int i = 0; i < 8; ++i) a.op[s][i] = burst[s >> 1][i];
  }
  for (int i = 0; i < 8; ++i) a.op[13][3 * i] = OV + i;
  for (int i = 0; i < 3; ++i) a.op[14][9 * i] = OV + 8 + i;
  for (int i = 0; i < 3; ++i) a.op[15][9 * i] = OV + 16 + i;
  return a;
}
constexpr ASched A_S = a_make();
constexpr int A_KST[6] = {8, 9, 12, 13, 14, 15};
constexpr int B_V[] = {19, 20, 21, 22, 23, 11, 12, 13, 14, 15};
constexpr int B_NV = sizeof(B_V) / sizeof(int);
constexpr int A_NP = 2 + 6 + 16;
constexpr int a_seq(int op) {
  if (op >= OW && op < OW + 16) return 8 + op - OW;
  int c = A_NP;
  for (int s = 0; s < 16; ++s)
    for (int k = 0; k < 24; ++k)
      if (A_S.op[s][k]) {
        if (A_S.op[s][k] == op) return c;
        ++c;
      }
  return -1;
}
constexpr int a_issued(int s, int k) {
  int c = A_NP;
  for (int i = 0; i < 16; ++i)
    for (int j = 0; j < 24; ++j)
      if (A_S.op[i][j] && (i < s || (i == s && j < k))) ++c;
  return c;
}
constexpr int a_kt(int s) {
  for (int k = 0; k < 6; ++k)
    if (A_KST[k] == s) return k;
  return -1;
}
constexpr int imax(int a, int b) { return a > b ? a : b; }
constexpr int a_need(int s) {
  int n = 0;
  for (int i = 0; i < 8; ++i) n = imax(n, a_seq(OW + 8 * (s + 1) + i));
  if (s == 2 || s == 6 || s == 10)
    for (int i = 0; i < 6; ++i) n = imax(n, a_seq(OX + 6 * ((s + 2) / 4) + i));
  if (a_kt(s + 1) >= 0)
    for (int i = 0; i < 4; ++i) n = imax(n, a_seq(OK_ + 4 * a_kt(s + 1) + i));
  if (s == 14)
    for (int i = 2; i < 8; ++i) n = imax(n, a_seq(OP + i));
  return n;
}
constexpr int a_wait(int s) { return a_issued(s, 21) - a_need(s) - 1; }
constexpr bool a_check() {
  for (int i = 0; i < 24; ++i)
    if (a_seq(OK_ + i) < 0) return false;
  for (int i = 6; i < 24; ++i)
    if (a_seq(OX + i) < 0) return false;
  for (int i = 0; i < 128; ++i)
    if (a_seq(OW + i) < 0 || (i >= 32 && a_seq(OW + i) < a_issued(i / 8 - 3, 0))) return false;
  int nv = B_NV;
  for (int i = 0; i < 24; ++i)
    if (a_seq(OV + i) >= 0) {
      ++nv;
      if (i >= 11 && i < 16) return false;
      if (i >= 16 && a_seq(OV + i) < a_issued(7, 0)) return false;
    }
  for (int s = 0; s < 15; ++s)
    if (a_wait(s) < 0 || a_wait(s) > 63) return false;
  return nv == 24 && A_S.bar[3] && A_S.bar[7] && A_S.bar[11];
}
static_assert(a_check(), "phase A schedule");

DI unsigned aperm(const u32x4 (&W)[8], int f, int p) {
  return (f & 1) ? perm_hi(W[2 * p][f >> 1], W[2 * p + 1][f >> 1]) : perm_lo(W[2 * p][f >> 1], W[2 * p + 1][f >> 1]);
}

__global__ __launch_bounds__(256, 1) void k1_fused(Ptrs p, u32x4* __restrict__ part
) {
  extern __shared__ __attribute__((aligned(16))) char lds[];
  asm volatile("" ::: "a0", "a255");
  const int bid = blockIdx.x;
  const int xcd = bid & 7, idx = bid >> 3;
  const int br = xcd >> 2, hg = (xcd & 3) * 2 + (idx & 1), b = idx >> 1;
  const int tid = threadIdx.x, lane = tid & 63;
  const int w = __builtin_amdgcn_readfirstlane(tid >> 6);
  const int q = lane & 15, g = lane >> 4;
  const int cchunk = q;
  const int lb = (int)(unsigned)(size_t)(LDSP char*)lds;
  STAMP(0);

  const int head = hg * 4 + w;
  const size_t kvoff = ((size_t)b * NH + head) * SQ * DH;
  const rsrc_t rx = __builtin_amdgcn_make_buffer_rsrc((void*)(p.x[br] + (size_t)b * SQ * DM), 0, SQ * DM * 2, RSRC_W3);
  const rsrc_t rk = __builtin_amdgcn_make_buffer_rsrc((void*)(p.k[br] + kvoff), 0, SQ * DH * 2, RSRC_W3);
  const rsrc_t rv = __builtin_amdgcn_make_buffer_rsrc((void*)(p.v[br] + kvoff), 0, SQ * DH * 2, RSRC_W3);
  const rsrc_t rq = __builtin_amdgcn_make_buffer_rsrc((void*)p.wq[br], 0, DM * NH * DH * 2, RSRC_W3);
  const rsrc_t ro = __builtin_amdgcn_make_buffer_rsrc((void*)(p.wo[br] + (size_t)hg * 512 * DM), 0, 512 * DM * 2, RSRC_W3);

  f32x4 accv[20];
  u32x4 W[4][8];
  u32x4 A[2];

  auto gmf = [&](auto FI, auto TI, auto BI, auto ZI, const u32x4& Af) {
    constexpr int f = FI, t = TI, bq = A_B + 24 * BI + 4 * t;
    constexpr bool z = ZI;
    if constexpr (tile_v(f, t)) mf_v_va<bq, z>(accv[tile_vi(f, t)], Af);
    else mf_a_va<tile_a(f, t), bq, z>(Af);
  };
  auto gpk = [&](auto PI, auto TI, auto RI) -> unsigned {
    constexpr int pp = PI, t = TI, r = RI;
    if constexpr (tile_v(2 * pp, t)) return pk_bf16(accv[tile_vi(2 * pp, t)][r], accv[tile_vi(2 * pp + 1, t)][r]);
    else return pk_acc<tile_a(2 * pp, t) + r, tile_a(2 * pp + 1, t) + r>();
  };

  const int vx = g * 1024 + ((q ^ g ^ (4 * w)) << 4);
  const int vwq = (8 * g) * 8192 + (hg * 512 + w * 128 + 8 * cchunk) * 2;
  nop<4>();
  const int vk = lane * 16;
  auto xdma = [&](auto II) {
    constexpr int qt = II / 6, i = II % 6;
    dma_x(lb + qt * XQ_BYTES + (4 * i + w) * 1024, rx, vx, (4 * i + w) * 4096 + qt * 256);
  };
  const int vpf = ((lane >> 5) * 32 + 2 * b + (w >> 1)) * 8192 + hg * 1024 + (w & 1) * 512 + (lane & 31) * 16;
  u32x4 pfd = u32x4{0, 0, 0, 0};
  nop<4>();
  STAMP(8);
  ld_pfv(pfd, rq, vpf, 0);
  ld_pfv(pfd, rq, vpf, 64 * 8192);
  sfor<6>([&](auto I) { xdma(I); });
  STAMP(9);
  wait_vm<7>();
  STAMP(10);
  sfor<16>([&](auto I) { ld_w<0>(W[I >> 3][I & 7], rq, vwq, (32 * (I >> 3) + (I & 7)) * 8192); });
  int xa[2][4];
#pragma unroll
  for (int h = 0; h < 2; ++h)
#pragma unroll
    for (int j = 0; j < 4; ++j) xa[h][j] = lb + h * 2 * XQ_BYTES + q * 256 + (((4 * j + g) ^ q) << 4);
  const int kst = lb + V1_BASE + w * 16384 + 16384 - 16 * 272;
  const int kwa = kst + g * 272 + q * 16, kra = kst + q * 272 + g * 64;
  const int vv0 = g * 256 + ((q ^ (((g >> 1) << 2) | (g & 1))) << 4);
  const int vv1 = vv0 ^ 128;
  auto vdma = [&](auto JI) {
    constexpr int j = JI;
    dma_v(j < 16 ? lb + V1_BASE + w * 16384 + j * 1024 : lb + w * 8192 + (j - 16) * 1024, rv, (j & 1) ? vv1 : vv0, j * 1024);
  };
  STAMP(11);
  SCHED;
  wait_vm<A_NP - 8>();
  barrier();
  STAMP(1);

  auto xrd = [&](auto SI, auto TI) {
    constexpr int s = SI, t = TI;
    lds_rd_a<A_B + 24 * (s & 1) + 4 * t, ((s >> 2) & 1) * XQ_BYTES + t * 4096>(xa[s >> 3][s & 3]);
  };
  sfor<6>([&](auto T) { xrd(IC<0>{}, T); });
  wait_vm<8>();
  SCHED;
#pragma unroll
  for (int pp = 0; pp < 4; ++pp) A[0][pp] = aperm(W[0], 0, pp);
  SCHED;
  wait_lgkm0();
  nop<0>();
  SCHED;
  sfor<128>([&](auto NI) {
    constexpr int n = NI, s = n >> 3, f = n & 7;
    constexpr int kt = a_kt(s), ktc = kt < 0 ? 0 : kt;
    if constexpr (f == 0 && A_S.bar[s]) {
      barrier();
      SCHED;
    }
    sfor<6>([&](auto TI) {
      constexpr int t = TI;
      gmf(IC<f>{}, TI, IC<(s & 1)>{}, std::bool_constant<s == 0>{}, A[n & 1]);
      SCHED;
      if constexpr (t == 0 && f == 7 && s < 15) wait_vm<a_wait(s)>();
      constexpr int op = (t & 1) ? 0 : A_S.op[s][3 * f + t / 2], j = op % 1000;
      if constexpr (op >= OW) ld_w<0>(W[(j >> 3) & 3][j & 7], rq, vwq, (32 * (j >> 3) + (j & 7)) * 8192);
      else if constexpr (op >= OV) vdma(IC<j>{});
      else if constexpr (op >= OP) ld_pfv(pfd, rq, vpf, j * 64 * 8192);
      else if constexpr (op >= OK_) ld_k<A_K + 4 * j, (j & 3) * 1024>(rk, vk, (j >> 2) * 4096);
      else if constexpr (op >= OX) xdma(IC<j>{});
      if constexpr (t == 5) {
        if constexpr (s + 1 < 16 && f < 6) xrd(IC<s + 1>{}, IC<f>{});
        else if constexpr (f == 7) wait_lgkm0();
        else if constexpr (kt >= 0 && f == 6) lds_rd_a<A_K + 16 * ktc + 12, 48>(kra);
        else nop<0>();
      }
      if constexpr (n + 1 < 128 && t >= 1 && t <= 4) A[(n + 1) & 1][t - 1] = aperm(W[((n + 1) >> 3) & 3], (n + 1) & 7, t - 1);
      if constexpr (t == 3 && kt >= 0) {
        if constexpr (f < 4) lds_wr_a<A_K + 16 * ktc + 4 * f, f * 4 * 272>(kwa);
        else if constexpr (f < 7) lds_rd_a<A_K + 16 * ktc + 4 * (f - 4), (f - 4) * 16>(kra);
      }
      SCHED;
    });
  });
  asm volatile("" ::"v"(pfd));
  STAMP(2);

  u32x4 Qf[2][4];
  f32x4 sacc[2][6];
  u32x4 Pf[6][3];
  f32x4 oacc[2][8];
  f32x4 lacc[2];
  float mA = 0.f, mB = 0.f, mc = 0.f, li = 0.f;
  float ev[24];
  const float c = 0.08838834764831845f * 1.4426950408889634f;

  auto qp = [&](auto TI, auto JI) {
    constexpr int t = TI, j = JI, pp = j >> 2, s = j & 3;
    Qf[t & 1][s][pp] = gpk(IC<pp>{}, TI, IC<s>{});
  };
  auto sm = [&](auto TI, auto JI) {
    constexpr int t = TI, j = JI;
    const f32x4(&S)[6] = sacc[t & 1];
    auto v = [&](int i) -> float { return S[i >> 2][i & 3]; };
    if constexpr (j == 0) mA = max3(v(0), v(1), v(2));
    else if constexpr (j == 1) mB = max3(v(3), v(4), v(5));
    else if constexpr (j < 11) {
      if constexpr (j & 1) mB = max3(mB, v(2 * j + 2), v(2 * j + 3));
      else mA = max3(mA, v(2 * j + 2), v(2 * j + 3));
    } else if constexpr (j == 11) mA = max2(mA, mB);
    else if constexpr (j == 12) mA = xmax16(mA);
    else if constexpr (j == 13) mA = xmax32(mA);
    else if constexpr (j == 14) mc = -mA * c;
    else if constexpr (j < 15 + 24) {
      constexpr int i = j - 15;
      ev[i] = __builtin_amdgcn_exp2f(__builtin_fmaf(v(i), c, mc));
    } else {
      constexpr int i = j - 39;
      Pf[t][i >> 2][i & 3] = pk_bf16(ev[2 * i], ev[2 * i + 1]);
    }
  };
  constexpr int SM_N = 51;
  const int vo = lane * 16;
  const int oa1 = lb + V1_BASE + w * 16384 + vo, oa2 = lb + w * 8192 + vo;
  u32x4 ot;
  auto oe = [&](auto TI, auto JI) {
    constexpr int t = TI, j = JI;
    if constexpr (j == 0) {
      asm volatile("" : "+v"(lacc[t & 1]));
      li = __builtin_amdgcn_rcpf(lacc[t & 1][0]);
    }
    else {
      constexpr int i = j - 1, sp = i / 5, e = i % 5;
      if constexpr (e < 4) {
        const f32x4& o = oacc[t & 1][2 * sp + (e >> 1)];
        ot[e] = pk_bf16(o[2 * (e & 1)] * li, o[2 * (e & 1) + 1] * li);
      } else {
        constexpr int fi = sp * 6 + t;
        if constexpr (fi < 16) lds_wr<fi * 1024>(oa1, ot);
        else lds_wr<(fi - 16) * 1024>(oa2, ot);
      }
    }
  };
  constexpr int OE_N = 21;
  const int vtr = (4 * g + ((lane >> 2) & 3)) * 256 +
                  (((lane & 3) ^ (((g & 1) << 3) | (((lane >> 3) & 1) << 2) | ((lane >> 2) & 1))) << 4) + 8 * (lane & 1);
  const int vt1 = lb + V1_BASE + w * 16384 + vtr, vt2 = lb + w * 8192 + vtr;
  auto vrd = [&](auto JI) {
    constexpr int j = JI, f = j / 6, t3 = (j % 6) >> 1, hh = j & 1, sp = f >> 1, h = f & 1;
    constexpr int x = (sp << 6) ^ (h << 3), d = A_V + 4 * (t3 * 8 + f) + 2 * hh;
    if constexpr (t3 < 2) lds_rdtr_a<d, (32 * t3 + 16 * hh) * 256>(vt1 ^ x);
    else lds_rdtr_a<d, 16 * hh * 256>(vt2 ^ x);
  };
  const int vwoA = (8 * g + 4 * (g & 1)) * 1024 + (w * 128 + 8 * cchunk) * 2;
  const int vwoB = (8 * g - 4 * (g & 1) + 4) * 1024 + (w * 128 + 8 * cchunk) * 2;
  auto wold = [&](auto SI, auto II) {
    constexpr int S = SI, i = II;
    const int so = (((w + (S >> 2)) & 3) * 128 + 32 * (S & 3)) * 1024;
    ld_w<(i & 3) * 1024>(W[S & 3][i], ro, i < 4 ? vwoA : vwoB, so);
  };

  sfor<16>([&](auto J) { qp(IC<0>{}, J); });
  SCHED;
  nop<1>();
  sfor<6>([&](auto TI) {
    constexpr int t = TI;
    constexpr int NQ = t < 5 ? 16 : 0, NS = t > 0 ? SM_N : 0, NV = t == 5 ? 48 : 0;
    constexpr int NF = NQ + NS + NV;
    if constexpr (t == 5) wait_vm<PF_WO>();
    sfor<24>([&](auto MI) {
      constexpr int m = MI, s = m / 6, kt = m % 6;
      mf_v_av<A_K + 4 * (kt * 4 + s), s == 0>(sacc[t & 1][kt], Qf[t & 1][s]);
      SCHED;
      if constexpr (m % 8 == 2 && t < 4 && 3 * t + m / 8 < B_NV) vdma(IC<B_V[(3 * t + m / 8) < B_NV ? 3 * t + m / 8 : 0]>{});
      if constexpr (t == 4 && m % 3 == 0 && m / 3 < PF_WO) ld_pf<A_B + 4 * (m / 3)>(ro, vk, (32 * b + 8 * w + m / 3) * 1024);
      constexpr int lo = m * NF / 22 < NF ? m * NF / 22 : NF, hi = (m + 1) * NF / 22 < NF ? (m + 1) * NF / 22 : NF;
      sfor<hi - lo>([&](auto KI) {
        constexpr int k = lo + KI;
        if constexpr (k < NS) sm(IC<t - 1>{}, IC<k>{});
        else if constexpr (k < NS + NQ) qp(IC<t + 1>{}, IC<k - NS>{});
        else vrd(IC<k - NS - NQ>{});
      });
      SCHED;
    });
  });
  wait_lgkm0();
  STAMP(3);
  u32x4 ones = u32x4{0x3F803F80u, 0x3F803F80u, 0x3F803F80u, 0x3F803F80u};
  asm volatile("" : "+v"(ones));
  nop<1>();
  sfor<6>([&](auto TI) {
    constexpr int t = TI;
    constexpr int NS = t == 0 ? SM_N : 0, NO = t > 0 ? OE_N : 0, NW = t >= 3 ? 8 : 0;
    constexpr int NF = NS + NO + NW;
    sfor<27>([&](auto MI) {
      constexpr int m = MI, t3 = m / 9, f = m % 9;
      if constexpr (f > 0) mf_v_av<A_V + 4 * (t3 * 8 + f - 1), t3 == 0>(oacc[t & 1][f - 1], Pf[t][t3]);
      else mf_v_vv<t3 == 0>(lacc[t & 1], ones, Pf[t][t3]);
      SCHED;
      constexpr int lo = m * NF / 27, hi = (m + 1) * NF / 27;
      sfor<hi - lo>([&](auto KI) {
        constexpr int k = lo + KI;
        if constexpr (k < NS) sm(IC<5>{}, IC<k>{});
        else if constexpr (k < NS + NO) oe(IC<t - 1>{}, IC<k - NS>{});
        else wold(IC<t - 3>{}, IC<k - NS - NO>{});
      });
      SCHED;
    });
  });
  sfor<OE_N>([&](auto J) { oe(IC<5>{}, J); });
  SCHED;
  STAMP(4);

  int ob[4][2];
#pragma unroll
  for (int h = 0; h < 4; ++h) {
    const int wp = (w + h) & 3;
    ob[h][0] = lb + V1_BASE + wp * 16384 + vo;
    ob[h][1] = lb + wp * 8192 + vo;
  }
  auto ord = [&](auto SI, auto TI) {
    constexpr int S = SI, t = TI, fi = (S & 3) * 6 + t;
    if constexpr (fi < 16) lds_rd_a<A_B + 24 * (S & 1) + 4 * t, fi * 1024>(ob[S >> 2][0]);
    else lds_rd_a<A_B + 24 * (S & 1) + 4 * t, (fi - 16) * 1024>(ob[S >> 2][1]);
  };
  wait_vm<16>();
  sfor<6>([&](auto T) { ord(IC<0>{}, T); });
  SCHED;
#pragma unroll
  for (int pp = 0; pp < 4; ++pp) A[0][pp] = aperm(W[0], 0, pp);
  SCHED;
  wait_lgkm0();
  nop<0>();
  SCHED;
  sfor<128>([&](auto NI) {
    constexpr int n = NI, s = n >> 3, f = n & 7;
    if constexpr (n == 24) {
      barrier();
      SCHED;
    }
    sfor<6>([&](auto TI) {
      constexpr int t = TI;
      gmf(IC<f>{}, TI, IC<(s & 1)>{}, std::bool_constant<s == 0>{}, A[n & 1]);
      SCHED;
      if constexpr ((t == 0 || t == 2) && f < 4 && s + 3 < 16) wold(IC<s + 3>{}, IC<(2 * f + (t >> 1)) & 7>{});
      if constexpr (t == 0 && f == 7 && s < 15) wait_vm<(s <= 12 ? 16 : s == 13 ? 8 : 0)>();
      if constexpr (t == 5) {
        if constexpr (s + 1 < 16 && f < 6) ord(IC<s + 1>{}, IC<f>{});
        else if constexpr (s + 1 < 16 && f == 7) wait_lgkm0();
        else nop<0>();
      }
      if constexpr (n + 1 < 128 && t >= 1 && t <= 4) A[(n + 1) & 1][t - 1] = aperm(W[((n + 1) >> 3) & 3], (n + 1) & 7, t - 1);
      SCHED;
    });
  });
  STAMP(5);
  {
    const rsrc_t rp = __builtin_amdgcn_make_buffer_rsrc((void*)(part + (size_t)(((br * NBATCH + b) * 8 + hg) * 4 + w) * 24 * 64), 0,
                                                        24 * 1024, RSRC_W3);
    nop<7>();
    sfor<24>([&](auto I) {
      constexpr int t = I >> 2, r = I & 3;
      u32x4 o;
      sfor<4>([&](auto P) { o[(int)P] = gpk(P, IC<t>{}, IC<r>{}); });
      __builtin_amdgcn_raw_buffer_store_b128(o, rp, vo + (I & 3) * 1024, (I >> 2) * 4096, AUX_PST);
      SCHED;
      nop<1>();
      SCHED;
    });
  }
  STAMP(6);
}

__global__ __launch_bounds__(256) void k2_reduce(const u32x4* __restrict__ part, unsigned short* __restrict__ out) {
  const int i = blockIdx.x * 256 + threadIdx.x;
  const int bb = i / 6144, e = i % 6144;
  const rsrc_t rp = __builtin_amdgcn_make_buffer_rsrc((void*)(part + (size_t)bb * 8 * 6144), 0, 8 * 6144 * 16, RSRC_W3);
  u32x4 v[8];
#pragma unroll
  for (int h = 0; h < 8; ++h) v[h] = __builtin_amdgcn_raw_buffer_load_b128(rp, e * 16, h * 6144 * 16, AUX_PLD);
  float s[8];
#pragma unroll
  for (int j = 0; j < 8; ++j) s[j] = 0.f;
#pragma unroll
  for (int h = 0; h < 8; ++h)
#pragma unroll
    for (int j = 0; j < 4; ++j) {
      s[2 * j] += __builtin_bit_cast(float, v[h][j] << 16);
      s[2 * j + 1] += __builtin_bit_cast(float, v[h][j] & 0xffff0000u);
    }
  u32x4 o;
#pragma unroll
  for (int j = 0; j < 4; ++j) o[j] = pk_bf16(s[2 * j], s[2 * j + 1]);
  const int lane = e & 63, pc = e >> 6, r = pc & 3, t = (pc >> 2) % 6, w = pc / 24;
  const int q = lane & 15, g = lane >> 4;
  const int br = bb >> 4, b = bb & 15;
  *(u32x4*)(out + ((size_t)b * SQ + 16 * t + q) * 1024 + br * 512 + 128 * w + 32 * g + 8 * r) = o;
}

extern "C" void kernel_launch(void* const* d_in, const int* in_sizes, int n_in, void* d_out, int out_size, void* d_ws,
                              size_t ws_size, hipStream_t stream) {
  Ptrs p;
  for (int br = 0; br < 2; ++br) {
    p.x[br] = (const unsigned short*)d_in[5 * br + 0];
    p.k[br] = (const unsigned short*)d_in[5 * br + 1];
    p.v[br] = (const unsigned short*)d_in[5 * br + 2];
    p.wq[br] = (const unsigned short*)d_in[5 * br + 3];
    p.wo[br] = (const unsigned short*)d_in[5 * br + 4];
  }
  u32x4* part = (u32x4*)d_ws;
  (void)hipFuncSetAttribute((const void*)k1_fused, hipFuncAttributeMaxDynamicSharedMemorySize, LDS_BYTES);
  k1_fused<<<256, 256, LDS_BYTES, stream>>>(p, part
  );
  k2_reduce<<<2 * 16 * 96 * 64 / 256, 256, 0, stream>>>(part, (unsigned short*)d_out);
}
```

```cpp
#include <hip/hip_runtime.h>
#include <hip/hip_bf16.h>
#include <utility>

typedef unsigned u32x2 __attribute__((ext_vector_type(2)));
typedef unsigned u32x4 __attribute__((ext_vector_type(4)));
typedef float f32x4 __attribute__((ext_vector_type(4)));
#define LDSP __attribute__((address_space(3)))
#define DI __device__ __forceinline__
typedef __amdgpu_buffer_rsrc_t rsrc_t;

constexpr int SQ = 96, DM = 512, NH = 32, DH = 128, NBATCH = 16;
constexpr int RSRC_W3 = 0x00020000;
#define POL_KV " nt"
#define POL_W ""
#define POL_PF ""
#define POL_X ""
#define AUX_PST 16
#define AUX_PLD 0

template <class F, int... I>
DI void sfor_impl(F&& f, std::integer_sequence<int, I...>) {
  (f(std::integral_constant<int, I>{}), ...);
}
template <int N, class F>
DI void sfor(F&& f) {
  sfor_impl(f, std::make_integer_sequence<int, N>{});
}
template <int N>
using IC = std::integral_constant<int, N>;

typedef float f32x2 __attribute__((ext_vector_type(2)));
typedef __bf16 bf16x2 __attribute__((ext_vector_type(2)));
DI unsigned pk_bf16(float lo, float hi) {
  return __builtin_bit_cast(unsigned, __builtin_convertvector(f32x2{lo, hi}, bf16x2));
}
DI unsigned perm_lo(unsigned e, unsigned o) { return __builtin_amdgcn_perm(o, e, 0x05040100u); }
DI unsigned perm_hi(unsigned e, unsigned o) { return __builtin_amdgcn_perm(o, e, 0x07060302u); }
#define SCHED __builtin_amdgcn_sched_barrier(0)

#define MF "v_mfma_f32_16x16x32_bf16 "
template <int D, int Bq, bool Z>
DI void mf_a_va(const u32x4& A) {
  if constexpr (Z) asm volatile(MF "a[%c0:%c1], %2, a[%c3:%c4], 0" ::"i"(D), "i"(D + 3), "v"(A), "i"(Bq), "i"(Bq + 3));
  else asm volatile(MF "a[%c0:%c1], %2, a[%c3:%c4], a[%c0:%c1]" ::"i"(D), "i"(D + 3), "v"(A), "i"(Bq), "i"(Bq + 3));
}
template <int Bq, bool Z>
DI void mf_v_va(f32x4& C, const u32x4& A) {
  if constexpr (Z) asm volatile(MF "%0, %1, a[%c2:%c3], 0" : "=&v"(C) : "v"(A), "i"(Bq), "i"(Bq + 3));
  else asm volatile(MF "%0, %1, a[%c2:%c3], %0" : "+v"(C) : "v"(A), "i"(Bq), "i"(Bq + 3));
}
template <int Aq, bool Z>
DI void mf_v_av(f32x4& C, const u32x4& Bv) {
  if constexpr (Z) asm volatile(MF "%0, a[%c2:%c3], %1, 0" : "=&v"(C) : "v"(Bv), "i"(Aq), "i"(Aq + 3));
  else asm volatile(MF "%0, a[%c2:%c3], %1, %0" : "+v"(C) : "v"(Bv), "i"(Aq), "i"(Aq + 3));
}
template <bool Z>
DI void mf_v_vv(f32x4& C, const u32x4& A, const u32x4& Bv) {
  if constexpr (Z) asm volatile(MF "%0, %1, %2, 0" : "=&v"(C) : "v"(A), "v"(Bv));
  else asm volatile(MF "%0, %1, %2, %0" : "+v"(C) : "v"(A), "v"(Bv));
}
template <int X, int Y>
DI unsigned pk_acc() {
  unsigned r, t0, t1;
  asm volatile("v_accvgpr_read_b32 %1, a[%c3]\n\tv_accvgpr_read_b32 %2, a[%c4]\n\tv_cvt_pk_bf16_f32 %0, %1, %2"
               : "=v"(r), "=&v"(t0), "=&v"(t1)
               : "i"(X), "i"(Y));
  return r;
}
template <int IMM>
DI void ld_w(u32x4& d, rsrc_t r, int voff, int soff) {
  asm volatile("buffer_load_dwordx4 %0, %1, %2, %3 offen offset:%c4" POL_W : "=v"(d) : "v"(voff), "s"(r), "s"(soff), "i"(IMM));
}
template <int D, int IMM>
DI void ld_k(rsrc_t r, int voff, int soff) {
  asm volatile("buffer_load_dwordx4 a[%c0:%c1], %2, %3, %4 offen offset:%c5" POL_KV ::"i"(D), "i"(D + 3), "v"(voff), "s"(r),
               "s"(soff), "i"(IMM));
}
template <int D>
DI void ld_pf(rsrc_t r, int voff, int soff) {
  asm volatile("buffer_load_dwordx4 a[%c0:%c1], %2, %3, %4 offen" POL_PF ::"i"(D), "i"(D + 3), "v"(voff), "s"(r), "s"(soff));
}
DI void ld_pfv(u32x4& d, rsrc_t r, int voff, int soff) {
  asm volatile("buffer_load_dwordx4 %0, %1, %2, %3 offen" POL_PF : "+v"(d) : "v"(voff), "s"(r), "s"(soff));
}
DI void dma_x(int m0v, rsrc_t r, int voff, int soff) {
  asm volatile("s_mov_b32 m0, %0\n\ts_nop 0\n\tbuffer_load_dwordx4 %1, %2, %3 offen" POL_X " lds" ::"s"(m0v), "v"(voff), "s"(r),
               "s"(soff)
               : "memory");
}
DI void dma_v(int m0v, rsrc_t r, int voff, int soff) {
  asm volatile("s_mov_b32 m0, %0\n\ts_nop 0\n\tbuffer_load_dwordx4 %1, %2, %3 offen" POL_KV " lds" ::"s"(m0v), "v"(voff), "s"(r),
               "s"(soff)
               : "memory");
}
template <int D, int IMM>
DI void lds_rd_a(int addr) {
  asm volatile("ds_read_b128 a[%c0:%c1], %2 offset:%c3" ::"i"(D), "i"(D + 3), "v"(addr), "i"(IMM) : "memory");
}
template <int D, int IMM>
DI void lds_rdtr_a(int addr) {
  asm volatile("ds_read_b64_tr_b16 a[%c0:%c1], %2 offset:%c3" ::"i"(D), "i"(D + 1), "v"(addr), "i"(IMM) : "memory");
}
template <int D, int IMM>
DI void lds_wr_a(int addr) {
  asm volatile("ds_write_b128 %2, a[%c0:%c1] offset:%c3" ::"i"(D), "i"(D + 3), "v"(addr), "i"(IMM) : "memory");
}
template <int IMM>
DI void lds_wr(int addr, const u32x4& d) {
  asm volatile("ds_write_b128 %0, %1 offset:%c2" ::"v"(addr), "v"(d), "i"(IMM) : "memory");
}
template <int N>
DI void wait_vm() {
  static_assert(N >= 0 && N < 64, "vmcnt");
  asm volatile("s_waitcnt vmcnt(%c0)" ::"i"(N) : "memory");
}
DI void wait_lgkm0() { asm volatile("s_waitcnt lgkmcnt(0)" ::: "memory"); }
DI void barrier() { asm volatile("s_barrier" ::: "memory"); }
template <int N>
DI void nop() {
  asm volatile("s_nop %c0" ::"i"(N));
}
DI float max3(float a, float b, float c) {
  float r;
  asm volatile("v_max3_f32 %0, %1, %2, %3" : "=v"(r) : "v"(a), "v"(b), "v"(c));
  return r;
}
DI float max2(float a, float b) {
  float r;
  asm volatile("v_max_f32 %0, %1, %2" : "=v"(r) : "v"(a), "v"(b));
  return r;
}
DI float xmax16(float m) {
  float t;
  asm volatile("v_mov_b32 %1, %0\n\ts_nop 1\n\tv_permlane16_swap_b32 %0, %1\n\tv_max_f32 %0, %0, %1" : "+v"(m), "=&v"(t));
  return m;
}
DI float xmax32(float m) {
  float t;
  asm volatile("v_mov_b32 %1, %0\n\ts_nop 1\n\tv_permlane32_swap_b32 %0, %1\n\tv_max_f32 %0, %0, %1" : "+v"(m), "=&v"(t));
  return m;
}

#define STAMP(i)

struct Ptrs {
  const unsigned short* x[2];
  const unsigned short* k[2];
  const unsigned short* v[2];
  const unsigned short* wq[2];
  const unsigned short* wo[2];
};

constexpr int LDS_BYTES = 163840;
constexpr int XQ_BYTES = 24576, V1_BASE = 98304;

constexpr int A_B = 112, A_K = 160, A_V = 0;
constexpr bool tile_v(int f, int t) { return t < 2 || (t == 2 && f < 4); }
constexpr int tile_vi(int f, int t) { return t * 8 + f; }
constexpr int tile_a(int f, int t) { return 4 * (t >= 3 ? (t - 3) * 8 + f : 24 + (f - 4)); }

#define PF_WO 8
enum { OX = 1000, OK_ = 2000, OP = 3000, OV = 4000, OW = 5000 };
struct ASched {
  int op[16][24];
  bool bar[16];
};
constexpr ASched a_make() {
  ASched a{};
  const int burst[6][8] = {{OX + 6, OX + 7, OX + 8, OX + 9, OX + 10, OX + 11, OP + 2, OP + 3},
                           {OX + 12, OX + 13, OX + 14, OX + 15, OX + 16, OX + 17, OP + 4, OP + 5},
                           {OK_ + 0, OK_ + 1, OK_ + 2, OK_ + 3, OK_ + 4, OK_ + 5, OK_ + 6, OK_ + 7},
                           {OX + 18, OX + 19, OX + 20, OX + 21, OX + 22, OX + 23, OP + 6, OP + 7},
                           {OK_ + 8, OK_ + 9, OK_ + 10, OK_ + 11, OK_ + 12, OK_ + 13, OK_ + 14, OK_ + 15},
                           {OK_ + 16, OK_ + 17, OK_ + 18, OK_ + 19, OK_ + 20, OK_ + 21, OK_ + 22, OK_ + 23}};
  for (int s = 0; s <= 12; s += 2)
    for (int i = 0; i < 16; ++i) a.op[s][(i >> 1) * 3 + (i & 1)] = OW + 8 * (s + 2) + i;
  for (int s = 1; s <= 11; s += 2) {
    a.bar[s] = true;
    for (int i = 0; i < 8; ++i) a.op[s][i] = burst[s >> 1][i];
  }
  for (int i = 0; i < 8; ++i) a.op[13][3 * i] = OV + i;
  for (int i = 0; i < 3; ++i) a.op[14][9 * i] = OV + 8 + i;
  for (int i = 0; i < 3; ++i) a.op[15][9 * i] = OV + 16 + i;
  return a;
}
constexpr ASched A_S = a_make();
constexpr int A_KST[6] = {8, 9, 12, 13, 14, 15};
constexpr int B_V[] = {19, 20, 21, 22, 23, 11, 12, 13, 14, 15};
constexpr int B_NV = sizeof(B_V) / sizeof(int);
constexpr int A_NP = 3 + 6 + 16;
constexpr int a_seq(int op) {
  if (op >= OW && op < OW + 16) return 9 + op - OW;
  int c = A_NP;
  for (int s = 0; s < 16; ++s)
    for (int k = 0; k < 24; ++k)
      if (A_S.op[s][k]) {
        if (A_S.op[s][k] == op) return c;
        ++c;
      }
  return -1;
}
constexpr int a_issued(int s, int k) {
  int c = A_NP;
  for (int i = 0; i < 16; ++i)
    for (int j = 0; j < 24; ++j)
      if (A_S.op[i][j] && (i < s || (i == s && j < k))) ++c;
  return c;
}
constexpr int a_kt(int s) {
  for (int k = 0; k < 6; ++k)
    if (A_KST[k] == s) return k;
  return -1;
}
constexpr int imax(int a, int b) { return a > b ? a : b; }
constexpr int a_need(int s) {
  int n = 0;
  for (int i = 0; i < 8; ++i) n = imax(n, a_seq(OW + 8 * (s + 1) + i));
  if (s == 2 || s == 6 || s == 10)
    for (int i = 0; i < 6; ++i) n = imax(n, a_seq(OX + 6 * ((s + 2) / 4) + i));
  if (a_kt(s + 1) >= 0)
    for (int i = 0; i < 4; ++i) n = imax(n, a_seq(OK_ + 4 * a_kt(s + 1) + i));
  if (s == 14)
    for (int i = 2; i < 8; ++i) n = imax(n, a_seq(OP + i));
  return n;
}
constexpr int a_wait(int s) { return a_issued(s, 21) - a_need(s) - 1; }
constexpr bool a_check() {
  for (int i = 0; i < 24; ++i)
    if (a_seq(OK_ + i) < 0) return false;
  for (int i = 6; i < 24; ++i)
    if (a_seq(OX + i) < 0) return false;
  for (int i = 0; i < 128; ++i)
    if (a_seq(OW + i) < 0 || (i >= 32 && a_seq(OW + i) < a_issued(i / 8 - 3, 0))) return false;
  int nv = B_NV;
  for (int i = 0; i < 24; ++i)
    if (a_seq(OV + i) >= 0) {
      ++nv;
      if (i >= 11 && i < 16) return false;
      if (i >= 16 && a_seq(OV + i) < a_issued(7, 0)) return false;
    }
  for (int s = 0; s < 15; ++s)
    if (a_wait(s) < 0 || a_wait(s) > 63) return false;
  return nv == 24 && A_S.bar[3] && A_S.bar[7] && A_S.bar[11];
}
static_assert(a_check(), "phase A schedule");

DI unsigned aperm(const u32x4 (&W)[8], int f, int p) {
  return (f & 1) ? perm_hi(W[2 * p][f >> 1], W[2 * p + 1][f >> 1]) : perm_lo(W[2 * p][f >> 1], W[2 * p + 1][f >> 1]);
}

__global__ __launch_bounds__(256, 1) void k1_fused(Ptrs p, u32x4* __restrict__ part
) {
  extern __shared__ __attribute__((aligned(16))) char lds[];
  asm volatile("" ::: "a0", "a255");
  const int bid = blockIdx.x;
  const int xcd = bid & 7, idx = bid >> 3;
  const int br = xcd >> 2, hg = (xcd & 3) * 2 + (idx & 1), b = idx >> 1;
  const int tid = threadIdx.x, lane = tid & 63;
  const int w = __builtin_amdgcn_readfirstlane(tid >> 6);
  const int q = lane & 15, g = lane >> 4;
  const int cchunk = q;
  const int lb = (int)(unsigned)(size_t)(LDSP char*)lds;
  STAMP(0);

  const int head = hg * 4 + w;
  const size_t kvoff = ((size_t)b * NH + head) * SQ * DH;
  const rsrc_t rx = __builtin_amdgcn_make_buffer_rsrc((void*)(p.x[br] + (size_t)b * SQ * DM), 0, SQ * DM * 2, RSRC_W3);
  const rsrc_t rk = __builtin_amdgcn_make_buffer_rsrc((void*)(p.k[br] + kvoff), 0, SQ * DH * 2, RSRC_W3);
  const rsrc_t rv = __builtin_amdgcn_make_buffer_rsrc((void*)(p.v[br] + kvoff), 0, SQ * DH * 2, RSRC_W3);
  const rsrc_t rq = __builtin_amdgcn_make_buffer_rsrc((void*)p.wq[br], 0, DM * NH * DH * 2, RSRC_W3);
  const rsrc_t ro = __builtin_amdgcn_make_buffer_rsrc((void*)(p.wo[br] + (size_t)hg * 512 * DM), 0, 512 * DM * 2, RSRC_W3);

  f32x4 accv[20];
  u32x4 W[4][8];
  u32x4 A[2];

  auto gmf = [&](auto FI, auto TI, auto BI, auto ZI, const u32x4& Af) {
    constexpr int f = FI, t = TI, bq = A_B + 24 * BI + 4 * t;
    constexpr bool z = ZI;
    if constexpr (tile_v(f, t)) mf_v_va<bq, z>(accv[tile_vi(f, t)], Af);
    else mf_a_va<tile_a(f, t), bq, z>(Af);
  };
  auto gpk = [&](auto PI, auto TI, auto RI) -> unsigned {
    constexpr int pp = PI, t = TI, r = RI;
    if constexpr (tile_v(2 * pp, t)) return pk_bf16(accv[tile_vi(2 * pp, t)][r], accv[tile_vi(2 * pp + 1, t)][r]);
    else return pk_acc<tile_a(2 * pp, t) + r, tile_a(2 * pp + 1, t) + r>();
  };

  const int vx = g * 1024 + ((q ^ g ^ (4 * w)) << 4);
  const int vwq = (8 * g) * 8192 + (hg * 512 + w * 128 + 8 * cchunk) * 2;
  nop<4>();
  const int vk = lane * 16;
  auto xdma = [&](auto II) {
    constexpr int qt = II / 6, i = II % 6;
    dma_x(lb + qt * XQ_BYTES + (4 * i + w) * 1024, rx, vx, (4 * i + w) * 4096 + qt * 256);
  };
  const int vpf = ((lane >> 5) * 32 + 2 * b + (w >> 1)) * 8192 + hg * 1024 + (w & 1) * 512 + (lane & 31) * 16;
  u32x4 pfd = u32x4{0, 0, 0, 0};
  nop<4>();
  STAMP(8);
  {
    unsigned long long pc;
    int len;
    asm volatile("s_getpc_b64 %0\n.Lk1_pc:\n\ts_mov_b32 %1, .Lk2_end-.Lk1_pc" : "=s"(pc), "=s"(len));
    const rsrc_t rc = __builtin_amdgcn_make_buffer_rsrc((void*)pc, 0, len, RSRC_W3);
    nop<4>();
    ld_pfv(pfd, rc, vk, (idx * 4 + w) * 1024);
  }
  ld_pfv(pfd, rq, vpf, 0);
  ld_pfv(pfd, rq, vpf, 64 * 8192);
  sfor<6>([&](auto I) { xdma(I); });
  STAMP(9);
  wait_vm<7>();
  STAMP(10);
  sfor<16>([&](auto I) { ld_w<0>(W[I >> 3][I & 7], rq, vwq, (32 * (I >> 3) + (I & 7)) * 8192); });
  int xa[2][4];
#pragma unroll
  for (int h = 0; h < 2; ++h)
#pragma unroll
    for (int j = 0; j < 4; ++j) xa[h][j] = lb + h * 2 * XQ_BYTES + q * 256 + (((4 * j + g) ^ q) << 4);
  const int kst = lb + V1_BASE + w * 16384 + 16384 - 16 * 272;
  const int kwa = kst + g * 272 + q * 16, kra = kst + q * 272 + g * 64;
  const int vv0 = g * 256 + ((q ^ (((g >> 1) << 2) | (g & 1))) << 4);
  const int vv1 = vv0 ^ 128;
  auto vdma = [&](auto JI) {
    constexpr int j = JI;
    dma_v(j < 16 ? lb + V1_BASE + w * 16384 + j * 1024 : lb + w * 8192 + (j - 16) * 1024, rv, (j & 1) ? vv1 : vv0, j * 1024);
  };
  STAMP(11);
  SCHED;
  wait_vm<A_NP - 9>();
  barrier();
  STAMP(1);

  auto xrd = [&](auto SI, auto TI) {
    constexpr int s = SI, t = TI;
    lds_rd_a<A_B + 24 * (s & 1) + 4 * t, ((s >> 2) & 1) * XQ_BYTES + t * 4096>(xa[s >> 3][s & 3]);
  };
  sfor<6>([&](auto T) { xrd(IC<0>{}, T); });
  wait_vm<8>();
  SCHED;
#pragma unroll
  for (int pp = 0; pp < 4; ++pp) A[0][pp] = aperm(W[0], 0, pp);
  SCHED;
  wait_lgkm0();
  nop<0>();
  SCHED;
  sfor<128>([&](auto NI) {
    constexpr int n = NI, s = n >> 3, f = n & 7;
    constexpr int kt = a_kt(s), ktc = kt < 0 ? 0 : kt;
    if constexpr (f == 0 && A_S.bar[s]) {
      barrier();
      SCHED;
    }
    sfor<6>([&](auto TI) {
      constexpr int t = TI;
      gmf(IC<f>{}, TI, IC<(s & 1)>{}, std::bool_constant<s == 0>{}, A[n & 1]);
      SCHED;
      if constexpr (t == 0 && f == 7 && s < 15) wait_vm<a_wait(s)>();
      constexpr int op = (t & 1) ? 0 : A_S.op[s][3 * f + t / 2], j = op % 1000;
      if constexpr (op >= OW) ld_w<0>(W[(j >> 3) & 3][j & 7], rq, vwq, (32 * (j >> 3) + (j & 7)) * 8192);
      else if constexpr (op >= OV) vdma(IC<j>{});
      else if constexpr (op >= OP) ld_pfv(pfd, rq, vpf, j * 64 * 8192);
      else if constexpr (op >= OK_) ld_k<A_K + 4 * j, (j & 3) * 1024>(rk, vk, (j >> 2) * 4096);
      else if constexpr (op >= OX) xdma(IC<j>{});
      if constexpr (t == 5) {
        if constexpr (s + 1 < 16 && f < 6) xrd(IC<s + 1>{}, IC<f>{});
        else if constexpr (f == 7) wait_lgkm0();
        else if constexpr (kt >= 0 && f == 6) lds_rd_a<A_K + 16 * ktc + 12, 48>(kra);
        else nop<0>();
      }
      if constexpr (n + 1 < 128 && t >= 1 && t <= 4) A[(n + 1) & 1][t - 1] = aperm(W[((n + 1) >> 3) & 3], (n + 1) & 7, t - 1);
      if constexpr (t == 3 && kt >= 0) {
        if constexpr (f < 4) lds_wr_a<A_K + 16 * ktc + 4 * f, f * 4 * 272>(kwa);
        else if constexpr (f < 7) lds_rd_a<A_K + 16 * ktc + 4 * (f - 4), (f - 4) * 16>(kra);
      }
      SCHED;
    });
  });
  asm volatile("" ::"v"(pfd));
  STAMP(2);

  u32x4 Qf[2][4];
  f32x4 sacc[2][6];
  u32x4 Pf[6][3];
  f32x4 oacc[2][8];
  f32x4 lacc[2];
  float mA = 0.f, mB = 0.f, mc = 0.f, li = 0.f;
  float ev[24];
  const float c = 0.08838834764831845f * 1.4426950408889634f;

  auto qp = [&](auto TI, auto JI) {
    constexpr int t = TI, j = JI, pp = j >> 2, s = j & 3;
    Qf[t & 1][s][pp] = gpk(IC<pp>{}, TI, IC<s>{});
  };
  auto sm = [&](auto TI, auto JI) {
    constexpr int t = TI, j = JI;
    const f32x4(&S)[6] = sacc[t & 1];
    auto v = [&](int i) -> float { return S[i >> 2][i & 3]; };
    if constexpr (j == 0) mA = max3(v(0), v(1), v(2));
    else if constexpr (j == 1) mB = max3(v(3), v(4), v(5));
    else if constexpr (j < 11) {
      if constexpr (j & 1) mB = max3(mB, v(2 * j + 2), v(2 * j + 3));
      else mA = max3(mA, v(2 * j + 2), v(2 * j + 3));
    } else if constexpr (j == 11) mA = max2(mA, mB);
    else if constexpr (j == 12) mA = xmax16(mA);
    else if constexpr (j == 13) mA = xmax32(mA);
    else if constexpr (j == 14) mc = -mA * c;
    else if constexpr (j < 15 + 24) {
      constexpr int i = j - 15;
      ev[i] = __builtin_amdgcn_exp2f(__builtin_fmaf(v(i), c, mc));
    } else {
      constexpr int i = j - 39;
      Pf[t][i >> 2][i & 3] = pk_bf16(ev[2 * i], ev[2 * i + 1]);
    }
  };
  constexpr int SM_N = 51;
  const int vo = lane * 16;
  const int oa1 = lb + V1_BASE + w * 16384 + vo, oa2 = lb + w * 8192 + vo;
  u32x4 ot;
  auto oe = [&](auto TI, auto JI) {
    constexpr int t = TI, j = JI;
    if constexpr (j == 0) {
      asm volatile("" : "+v"(lacc[t & 1]));
      li = __builtin_amdgcn_rcpf(lacc[t & 1][0]);
    }
    else {
      constexpr int i = j - 1, sp = i / 5, e = i % 5;
      if constexpr (e < 4) {
        const f32x4& o = oacc[t & 1][2 * sp + (e >> 1)];
        ot[e] = pk_bf16(o[2 * (e & 1)] * li, o[2 * (e & 1) + 1] * li);
      } else {
        constexpr int fi = sp * 6 + t;
        if constexpr (fi < 16) lds_wr<fi * 1024>(oa1, ot);
        else lds_wr<(fi - 16) * 1024>(oa2, ot);
      }
    }
  };
  constexpr int OE_N = 21;
  const int vtr = (4 * g + ((lane >> 2) & 3)) * 256 +
                  (((lane & 3) ^ (((g & 1) << 3) | (((lane >> 3) & 1) << 2) | ((lane >> 2) & 1))) << 4) + 8 * (lane & 1);
  const int vt1 = lb + V1_BASE + w * 16384 + vtr, vt2 = lb + w * 8192 + vtr;
  auto vrd = [&](auto JI) {
    constexpr int j = JI, f = j / 6, t3 = (j % 6) >> 1, hh = j & 1, sp = f >> 1, h = f & 1;
    constexpr int x = (sp << 6) ^ (h << 3), d = A_V + 4 * (t3 * 8 + f) + 2 * hh;
    if constexpr (t3 < 2) lds_rdtr_a<d, (32 * t3 + 16 * hh) * 256>(vt1 ^ x);
    else lds_rdtr_a<d, 16 * hh * 256>(vt2 ^ x);
  };
  const int vwoA = (8 * g + 4 * (g & 1)) * 1024 + (w * 128 + 8 * cchunk) * 2;
  const int vwoB = (8 * g - 4 * (g & 1) + 4) * 1024 + (w * 128 + 8 * cchunk) * 2;
  auto wold = [&](auto SI, auto II) {
    constexpr int S = SI, i = II;
    const int so = (((w + (S >> 2)) & 3) * 128 + 32 * (S & 3)) * 1024;
    ld_w<(i & 3) * 1024>(W[S & 3][i], ro, i < 4 ? vwoA : vwoB, so);
  };

  sfor<16>([&](auto J) { qp(IC<0>{}, J); });
  SCHED;
  nop<1>();
  sfor<6>([&](auto TI) {
    constexpr int t = TI;
    constexpr int NQ = t < 5 ? 16 : 0, NS = t > 0 ? SM_N : 0, NV = t == 5 ? 48 : 0;
    constexpr int NF = NQ + NS + NV;
    if constexpr (t == 5) wait_vm<PF_WO>();
    sfor<24>([&](auto MI) {
      constexpr int m = MI, s = m / 6, kt = m % 6;
      mf_v_av<A_K + 4 * (kt * 4 + s), s == 0>(sacc[t & 1][kt], Qf[t & 1][s]);
      SCHED;
      if constexpr (m % 8 == 2 && t < 4 && 3 * t + m / 8 < B_NV) vdma(IC<B_V[(3 * t + m / 8) < B_NV ? 3 * t + m / 8 : 0]>{});
      if constexpr (t == 4 && m % 3 == 0 && m / 3 < PF_WO) ld_pf<A_B + 4 * (m / 3)>(ro, vk, (32 * b + 8 * w + m / 3) * 1024);
      constexpr int lo = m * NF / 22 < NF ? m * NF / 22 : NF, hi = (m + 1) * NF / 22 < NF ? (m + 1) * NF / 22 : NF;
      sfor<hi - lo>([&](auto KI) {
        constexpr int k = lo + KI;
        if constexpr (k < NS) sm(IC<t - 1>{}, IC<k>{});
        else if constexpr (k < NS + NQ) qp(IC<t + 1>{}, IC<k - NS>{});
        else vrd(IC<k - NS - NQ>{});
      });
      SCHED;
    });
  });
  wait_lgkm0();
  STAMP(3);
  u32x4 ones = u32x4{0x3F803F80u, 0x3F803F80u, 0x3F803F80u, 0x3F803F80u};
  asm volatile("" : "+v"(ones));
  nop<1>();
  sfor<6>([&](auto TI) {
    constexpr int t = TI;
    constexpr int NS = t == 0 ? SM_N : 0, NO = t > 0 ? OE_N : 0, NW = t >= 3 ? 8 : 0;
    constexpr int NF = NS + NO + NW;
    sfor<27>([&](auto MI) {
      constexpr int m = MI, t3 = m / 9, f = m % 9;
      if constexpr (f > 0) mf_v_av<A_V + 4 * (t3 * 8 + f - 1), t3 == 0>(oacc[t & 1][f - 1], Pf[t][t3]);
      else mf_v_vv<t3 == 0>(lacc[t & 1], ones, Pf[t][t3]);
      SCHED;
      constexpr int lo = m * NF / 27, hi = (m + 1) * NF / 27;
      sfor<hi - lo>([&](auto KI) {
        constexpr int k = lo + KI;
        if constexpr (k < NS) sm(IC<5>{}, IC<k>{});
        else if constexpr (k < NS + NO) oe(IC<t - 1>{}, IC<k - NS>{});
        else wold(IC<t - 3>{}, IC<k - NS - NO>{});
      });
      SCHED;
    });
  });
  sfor<OE_N>([&](auto J) { oe(IC<5>{}, J); });
  SCHED;
  STAMP(4);

  int ob[4][2];
#pragma unroll
  for (int h = 0; h < 4; ++h) {
    const int wp = (w + h) & 3;
    ob[h][0] = lb + V1_BASE + wp * 16384 + vo;
    ob[h][1] = lb + wp * 8192 + vo;
  }
  auto ord = [&](auto SI, auto TI) {
    constexpr int S = SI, t = TI, fi = (S & 3) * 6 + t;
    if constexpr (fi < 16) lds_rd_a<A_B + 24 * (S & 1) + 4 * t, fi * 1024>(ob[S >> 2][0]);
    else lds_rd_a<A_B + 24 * (S & 1) + 4 * t, (fi - 16) * 1024>(ob[S >> 2][1]);
  };
  wait_vm<16>();
  sfor<6>([&](auto T) { ord(IC<0>{}, T); });
  SCHED;
#pragma unroll
  for (int pp = 0; pp < 4; ++pp) A[0][pp] = aperm(W[0], 0, pp);
  SCHED;
  wait_lgkm0();
  nop<0>();
  SCHED;
  sfor<128>([&](auto NI) {
    constexpr int n = NI, s = n >> 3, f = n & 7;
    if constexpr (n == 24) {
      barrier();
      SCHED;
    }
    sfor<6>([&](auto TI) {
      constexpr int t = TI;
      gmf(IC<f>{}, TI, IC<(s & 1)>{}, std::bool_constant<s == 0>{}, A[n & 1]);
      SCHED;
      if constexpr ((t == 0 || t == 2) && f < 4 && s + 3 < 16) wold(IC<s + 3>{}, IC<(2 * f + (t >> 1)) & 7>{});
      if constexpr (t == 0 && f == 7 && s < 15) wait_vm<(s <= 12 ? 16 : s == 13 ? 8 : 0)>();
      if constexpr (t == 5) {
        if constexpr (s + 1 < 16 && f < 6) ord(IC<s + 1>{}, IC<f>{});
        else if constexpr (s + 1 < 16 && f == 7) wait_lgkm0();
        else nop<0>();
      }
      if constexpr (n + 1 < 128 && t >= 1 && t <= 4) A[(n + 1) & 1][t - 1] = aperm(W[((n + 1) >> 3) & 3], (n + 1) & 7, t - 1);
      SCHED;
    });
  });
  STAMP(5);
  {
    const rsrc_t rp = __builtin_amdgcn_make_buffer_rsrc((void*)(part + (size_t)(((br * NBATCH + b) * 8 + hg) * 4 + w) * 24 * 64), 0,
                                                        24 * 1024, RSRC_W3);
    nop<7>();
    sfor<24>([&](auto I) {
      constexpr int t = I >> 2, r = I & 3;
      u32x4 o;
      sfor<4>([&](auto P) { o[(int)P] = gpk(P, IC<t>{}, IC<r>{}); });
      __builtin_amdgcn_raw_buffer_store_b128(o, rp, vo + (I & 3) * 1024, (I >> 2) * 4096, AUX_PST);
      SCHED;
      nop<1>();
      SCHED;
    });
  }
  STAMP(6);
}

__global__ __launch_bounds__(256) void k2_reduce(const u32x4* __restrict__ part, unsigned short* __restrict__ out) {
  const int i = blockIdx.x * 256 + threadIdx.x;
  const int bb = i / 6144, e = i % 6144;
  const rsrc_t rp = __builtin_amdgcn_make_buffer_rsrc((void*)(part + (size_t)bb * 8 * 6144), 0, 8 * 6144 * 16, RSRC_W3);
  u32x4 v[8];
#pragma unroll
  for (int h = 0; h < 8; ++h) v[h] = __builtin_amdgcn_raw_buffer_load_b128(rp, e * 16, h * 6144 * 16, AUX_PLD);
  float s[8];
#pragma unroll
  for (int j = 0; j < 8; ++j) s[j] = 0.f;
#pragma unroll
  for (int h = 0; h < 8; ++h)
#pragma unroll
    for (int j = 0; j < 4; ++j) {
      s[2 * j] += __builtin_bit_cast(float, v[h][j] << 16);
      s[2 * j + 1] += __builtin_bit_cast(float, v[h][j] & 0xffff0000u);
    }
  u32x4 o;
#pragma unroll
  for (int j = 0; j < 4; ++j) o[j] = pk_bf16(s[2 * j], s[2 * j + 1]);
  const int lane = e & 63, pc = e >> 6, r = pc & 3, t = (pc >> 2) % 6, w = pc / 24;
  const int q = lane & 15, g = lane >> 4;
  const int br = bb >> 4, b = bb & 15;
  *(u32x4*)(out + ((size_t)b * SQ + 16 * t + q) * 1024 + br * 512 + 128 * w + 32 * g + 8 * r) = o;
  asm volatile(".Lk2_end:");
}

extern "C" void kernel_launch(void* const* d_in, const int* in_sizes, int n_in, void* d_out, int out_size, void* d_ws,
                              size_t ws_size, hipStream_t stream) {
  Ptrs p;
  for (int br = 0; br < 2; ++br) {
    p.x[br] = (const unsigned short*)d_in[5 * br + 0];
    p.k[br] = (const unsigned short*)d_in[5 * br + 1];
    p.v[br] = (const unsigned short*)d_in[5 * br + 2];
    p.wq[br] = (const unsigned short*)d_in[5 * br + 3];
    p.wo[br] = (const unsigned short*)d_in[5 * br + 4];
  }
  u32x4* part = (u32x4*)d_ws;
  (void)hipFuncSetAttribute((const void*)k1_fused, hipFuncAttributeMaxDynamicSharedMemorySize, LDS_BYTES);
  k1_fused<<<256, 256, LDS_BYTES, stream>>>(p, part
  );
  k2_reduce<<<2 * 16 * 96 * 64 / 256, 256, 0, stream>>>(part, (unsigned short*)d_out);
}
```

```cpp
#include <hip/hip_runtime.h>
#include <hip/hip_bf16.h>
#include <utility>

typedef unsigned u32x2 __attribute__((ext_vector_type(2)));
typedef unsigned u32x4 __attribute__((ext_vector_type(4)));
typedef float f32x4 __attribute__((ext_vector_type(4)));
#define LDSP __attribute__((address_space(3)))
#define DI __device__ __forceinline__
typedef __amdgpu_buffer_rsrc_t rsrc_t;

constexpr int SQ = 96, DM = 512, NH = 32, DH = 128, NBATCH = 16;
constexpr int RSRC_W3 = 0x00020000;
#define POL_KV " nt"
#define POL_W ""
#define POL_PF ""
#define POL_X ""
#define AUX_PST 16
#define AUX_PLD 0

template <class F, int... I>
DI void sfor_impl(F&& f, std::integer_sequence<int, I...>) {
  (f(std::integral_constant<int, I>{}), ...);
}
template <int N, class F>
DI void sfor(F&& f) {
  sfor_impl(f, std::make_integer_sequence<int, N>{});
}
template <int N>
using IC = std::integral_constant<int, N>;

typedef float f32x2 __attribute__((ext_vector_type(2)));
typedef __bf16 bf16x2 __attribute__((ext_vector_type(2)));
DI unsigned pk_bf16(float lo, float hi) {
  return __builtin_bit_cast(unsigned, __builtin_convertvector(f32x2{lo, hi}, bf16x2));
}
DI unsigned perm_lo(unsigned e, unsigned o) { return __builtin_amdgcn_perm(o, e, 0x05040100u); }
DI unsigned perm_hi(unsigned e, unsigned o) { return __builtin_amdgcn_perm(o, e, 0x07060302u); }
#define SCHED __builtin_amdgcn_sched_barrier(0)

#define MF "v_mfma_f32_16x16x32_bf16 "
template <int D, int Bq, bool Z>
DI void mf_a_va(const u32x4& A) {
  if constexpr (Z) asm volatile(MF "a[%c0:%c1], %2, a[%c3:%c4], 0" ::"i"(D), "i"(D + 3), "v"(A), "i"(Bq), "i"(Bq + 3));
  else asm volatile(MF "a[%c0:%c1], %2, a[%c3:%c4], a[%c0:%c1]" ::"i"(D), "i"(D + 3), "v"(A), "i"(Bq), "i"(Bq + 3));
}
template <int Bq, bool Z>
DI void mf_v_va(f32x4& C, const u32x4& A) {
  if constexpr (Z) asm volatile(MF "%0, %1, a[%c2:%c3], 0" : "=&v"(C) : "v"(A), "i"(Bq), "i"(Bq + 3));
  else asm volatile(MF "%0, %1, a[%c2:%c3], %0" : "+v"(C) : "v"(A), "i"(Bq), "i"(Bq + 3));
}
template <int Aq, bool Z>
DI void mf_v_av(f32x4& C, const u32x4& Bv) {
  if constexpr (Z) asm volatile(MF "%0, a[%c2:%c3], %1, 0" : "=&v"(C) : "v"(Bv), "i"(Aq), "i"(Aq + 3));
  else asm volatile(MF "%0, a[%c2:%c3], %1, %0" : "+v"(C) : "v"(Bv), "i"(Aq), "i"(Aq + 3));
}
template <bool Z>
DI void mf_v_vv(f32x4& C, const u32x4& A, const u32x4& Bv) {
  if constexpr (Z) asm volatile(MF "%0, %1, %2, 0" : "=&v"(C) : "v"(A), "v"(Bv));
  else asm volatile(MF "%0, %1, %2, %0" : "+v"(C) : "v"(A), "v"(Bv));
}
template <int X, int Y>
DI unsigned pk_acc() {
  unsigned r, t0, t1;
  asm volatile("v_accvgpr_read_b32 %1, a[%c3]\n\tv_accvgpr_read_b32 %2, a[%c4]\n\tv_cvt_pk_bf16_f32 %0, %1, %2"
               : "=v"(r), "=&v"(t0), "=&v"(t1)
               : "i"(X), "i"(Y));
  return r;
}
template <int IMM>
DI void ld_w(u32x4& d, rsrc_t r, int voff, int soff) {
  asm volatile("buffer_load_dwordx4 %0, %1, %2, %3 offen offset:%c4" POL_W : "=v"(d) : "v"(voff), "s"(r), "s"(soff), "i"(IMM));
}
template <int D, int IMM>
DI void ld_k(rsrc_t r, int voff, int soff) {
  asm volatile("buffer_load_dwordx4 a[%c0:%c1], %2, %3, %4 offen offset:%c5" POL_KV ::"i"(D), "i"(D + 3), "v"(voff), "s"(r),
               "s"(soff), "i"(IMM));
}
template <int D>
DI void ld_pf(rsrc_t r, int voff, int soff) {
  asm volatile("buffer_load_dwordx4 a[%c0:%c1], %2, %3, %4 offen" POL_PF ::"i"(D), "i"(D + 3), "v"(voff), "s"(r), "s"(soff));
}
DI void ld_pfv(u32x4& d, rsrc_t r, int voff, int soff) {
  asm volatile("buffer_load_dwordx4 %0, %1, %2, %3 offen" POL_PF : "+v"(d) : "v"(voff), "s"(r), "s"(soff));
}
DI void dma_x(int m0v, rsrc_t r, int voff, int soff) {
  asm volatile("s_mov_b32 m0, %0\n\ts_nop 0\n\tbuffer_load_dwordx4 %1, %2, %3 offen" POL_X " lds" ::"s"(m0v), "v"(voff), "s"(r),
               "s"(soff)
               : "memory");
}
DI void dma_v(int m0v, rsrc_t r, int voff, int soff) {
  asm volatile("s_mov_b32 m0, %0\n\ts_nop 0\n\tbuffer_load_dwordx4 %1, %2, %3 offen" POL_KV " lds" ::"s"(m0v), "v"(voff), "s"(r),
               "s"(soff)
               : "memory");
}
template <int D, int IMM>
DI void lds_rd_a(int addr) {
  asm volatile("ds_read_b128 a[%c0:%c1], %2 offset:%c3" ::"i"(D), "i"(D + 3), "v"(addr), "i"(IMM) : "memory");
}
template <int D, int IMM>
DI void lds_rdtr_a(int addr) {
  asm volatile("ds_read_b64_tr_b16 a[%c0:%c1], %2 offset:%c3" ::"i"(D), "i"(D + 1), "v"(addr), "i"(IMM) : "memory");
}
template <int D, int IMM>
DI void lds_wr_a(int addr) {
  asm volatile("ds_write_b128 %2, a[%c0:%c1] offset:%c3" ::"i"(D), "i"(D + 3), "v"(addr), "i"(IMM) : "memory");
}
template <int IMM>
DI void lds_wr(int addr, const u32x4& d) {
  asm volatile("ds_write_b128 %0, %1 offset:%c2" ::"v"(addr), "v"(d), "i"(IMM) : "memory");
}
template <int N>
DI void wait_vm() {
  static_assert(N >= 0 && N < 64, "vmcnt");
  asm volatile("s_waitcnt vmcnt(%c0)" ::"i"(N) : "memory");
}
DI void wait_lgkm0() { asm volatile("s_waitcnt lgkmcnt(0)" ::: "memory"); }
DI void barrier() { asm volatile("s_barrier" ::: "memory"); }
template <int N>
DI void nop() {
  asm volatile("s_nop %c0" ::"i"(N));
}
DI void max3x2i(float& a, float& b, float x0, float x1, float x2, float y0, float y1, float y2) {
  asm volatile("v_max3_f32 %0, %2, %3, %4\n\tv_max3_f32 %1, %5, %6, %7"
               : "=&v"(a), "=&v"(b)
               : "v"(x0), "v"(x1), "v"(x2), "v"(y0), "v"(y1), "v"(y2));
}
DI void max3x2(float& a, float& b, float x0, float x1, float y0, float y1) {
  asm volatile("v_max3_f32 %0, %0, %2, %3\n\tv_max3_f32 %1, %1, %4, %5" : "+v"(a), "+v"(b) : "v"(x0), "v"(x1), "v"(y0), "v"(y1));
}
DI void max_fin16(float& a, float& b, float x0, float x1) {
  asm volatile(
      "v_max3_f32 %0, %0, %2, %3\n\tv_max_f32 %0, %0, %1\n\tv_mov_b32 %1, %0\n\ts_nop 1\n\t"
      "v_permlane16_swap_b32 %0, %1\n\tv_max_f32 %0, %0, %1"
      : "+v"(a), "+v"(b)
      : "v"(x0), "v"(x1));
}
DI void max_fin32(float& a, float& b, float s) {
  asm volatile("v_mov_b32 %1, %0\n\ts_nop 1\n\tv_permlane32_swap_b32 %0, %1\n\tv_max_f32 %0, %0, %1\n\tv_mul_f32 %0, %2, %0"
               : "+v"(a), "=&v"(b)
               : "s"(s));
}
DI unsigned pk_scaled(float a, float b, float s) {
  unsigned r;
  float t0, t1;
  asm volatile("v_mul_f32 %1, %3, %5\n\tv_mul_f32 %2, %4, %5\n\tv_cvt_pk_bf16_f32 %0, %1, %2"
               : "=v"(r), "=&v"(t0), "=&v"(t1)
               : "v"(a), "v"(b), "v"(s));
  return r;
}

#define STAMP(i)

struct Ptrs {
  const unsigned short* x[2];
  const unsigned short* k[2];
  const unsigned short* v[2];
  const unsigned short* wq[2];
  const unsigned short* wo[2];
};

constexpr int LDS_BYTES = 163840;
constexpr int XQ_BYTES = 24576, V1_BASE = 98304;

constexpr int A_B = 112, A_K = 160, A_V = 0;
constexpr bool tile_v(int f, int t) { return t < 2 || (t == 2 && f < 4); }
constexpr int tile_vi(int f, int t) { return t * 8 + f; }
constexpr int tile_a(int f, int t) { return 4 * (t >= 3 ? (t - 3) * 8 + f : 24 + (f - 4)); }

#define PF_WO 8
enum { OX = 1000, OK_ = 2000, OP = 3000, OV = 4000, OW = 5000 };
struct ASched {
  int op[16][24];
  bool bar[16];
};
constexpr ASched a_make() {
  ASched a{};
  const int burst[6][8] = {{OX + 6, OX + 7, OX + 8, OX + 9, OX + 10, OX + 11, OP + 2, OP + 3},
                           {OX + 12, OX + 13, OX + 14, OX + 15, OX + 16, OX + 17, OP + 4, OP + 5},
                           {OK_ + 0, OK_ + 1, OK_ + 2, OK_ + 3, OK_ + 4, OK_ + 5, OK_ + 6, OK_ + 7},
                           {OX + 18, OX + 19, OX + 20, OX + 21, OX + 22, OX + 23, OP + 6, OP + 7},
                           {OK_ + 8, OK_ + 9, OK_ + 10, OK_ + 11, OK_ + 12, OK_ + 13, OK_ + 14, OK_ + 15},
                           {OK_ + 16, OK_ + 17, OK_ + 18, OK_ + 19, OK_ + 20, OK_ + 21, OK_ + 22, OK_ + 23}};
  for (int s = 0; s <= 12; s += 2)
    for (int i = 0; i < 16; ++i) a.op[s][(i >> 1) * 3 + (i & 1)] = OW + 8 * (s + 2) + i;
  for (int s = 1; s <= 11; s += 2) {
    a.bar[s] = true;
    for (int i = 0; i < 8; ++i) a.op[s][i] = burst[s >> 1][i];
  }
  for (int i = 0; i < 8; ++i) a.op[13][3 * i] = OV + i;
  for (int i = 0; i < 3; ++i) a.op[14][9 * i] = OV + 8 + i;
  for (int i = 0; i < 3; ++i) a.op[15][9 * i] = OV + 16 + i;
  return a;
}
constexpr ASched A_S = a_make();
constexpr int A_KST[6] = {8, 9, 12, 13, 14, 15};
constexpr int B_V[] = {19, 20, 21, 22, 23, 11, 12, 13, 14, 15};
constexpr int B_NV = sizeof(B_V) / sizeof(int);
constexpr int A_NP = 3 + 6 + 16;
constexpr int a_seq(int op) {
  if (op >= OW && op < OW + 16) return 9 + op - OW;
  int c = A_NP;
  for (int s = 0; s < 16; ++s)
    for (int k = 0; k < 24; ++k)
      if (A_S.op[s][k]) {
        if (A_S.op[s][k] == op) return c;
        ++c;
      }
  return -1;
}
constexpr int a_issued(int s, int k) {
  int c = A_NP;
  for (int i = 0; i < 16; ++i)
    for (int j = 0; j < 24; ++j)
      if (A_S.op[i][j] && (i < s || (i == s && j < k))) ++c;
  return c;
}
constexpr int a_kt(int s) {
  for (int k = 0; k < 6; ++k)
    if (A_KST[k] == s) return k;
  return -1;
}
constexpr int imax(int a, int b) { return a > b ? a : b; }
constexpr int a_need(int s) {
  int n = 0;
  for (int i = 0; i < 8; ++i) n = imax(n, a_seq(OW + 8 * (s + 1) + i));
  if (s == 2 || s == 6 || s == 10)
    for (int i = 0; i < 6; ++i) n = imax(n, a_seq(OX + 6 * ((s + 2) / 4) + i));
  if (a_kt(s + 1) >= 0)
    for (int i = 0; i < 4; ++i) n = imax(n, a_seq(OK_ + 4 * a_kt(s + 1) + i));
  if (s == 14)
    for (int i = 2; i < 8; ++i) n = imax(n, a_seq(OP + i));
  return n;
}
constexpr int a_wait(int s) { return a_issued(s, 21) - a_need(s) - 1; }
constexpr bool a_check() {
  for (int i = 0; i < 24; ++i)
    if (a_seq(OK_ + i) < 0) return false;
  for (int i = 6; i < 24; ++i)
    if (a_seq(OX + i) < 0) return false;
  for (int i = 0; i < 128; ++i)
    if (a_seq(OW + i) < 0 || (i >= 32 && a_seq(OW + i) < a_issued(i / 8 - 3, 0))) return false;
  int nv = B_NV;
  for (int i = 0; i < 24; ++i)
    if (a_seq(OV + i) >= 0) {
      ++nv;
      if (i >= 11 && i < 16) return false;
      if (i >= 16 && a_seq(OV + i) < a_issued(7, 0)) return false;
    }
  for (int s = 0; s < 15; ++s)
    if (a_wait(s) < 0 || a_wait(s) > 63) return false;
  return nv == 24 && A_S.bar[3] && A_S.bar[7] && A_S.bar[11];
}
static_assert(a_check(), "phase A schedule");

DI unsigned aperm(const u32x4 (&W)[8], int f, int p) {
  return (f & 1) ? perm_hi(W[2 * p][f >> 1], W[2 * p + 1][f >> 1]) : perm_lo(W[2 * p][f >> 1], W[2 * p + 1][f >> 1]);
}

__global__ __launch_bounds__(256, 1) void k1_fused(Ptrs p, u32x4* __restrict__ part
) {
  extern __shared__ __attribute__((aligned(16))) char lds[];
  asm volatile("" ::: "a0", "a255");
  const int bid = blockIdx.x;
  const int xcd = bid & 7, idx = bid >> 3;
  const int br = xcd >> 2, hg = (xcd & 3) * 2 + (idx & 1), b = idx >> 1;
  const int tid = threadIdx.x, lane = tid & 63;
  const int w = __builtin_amdgcn_readfirstlane(tid >> 6);
  const int q = lane & 15, g = lane >> 4;
  const int cchunk = q;
  const int lb = (int)(unsigned)(size_t)(LDSP char*)lds;
  STAMP(0);

  const int head = hg * 4 + w;
  const size_t kvoff = ((size_t)b * NH + head) * SQ * DH;
  const rsrc_t rx = __builtin_amdgcn_make_buffer_rsrc((void*)(p.x[br] + (size_t)b * SQ * DM), 0, SQ * DM * 2, RSRC_W3);
  const rsrc_t rk = __builtin_amdgcn_make_buffer_rsrc((void*)(p.k[br] + kvoff), 0, SQ * DH * 2, RSRC_W3);
  const rsrc_t rv = __builtin_amdgcn_make_buffer_rsrc((void*)(p.v[br] + kvoff), 0, SQ * DH * 2, RSRC_W3);
  const rsrc_t rq = __builtin_amdgcn_make_buffer_rsrc((void*)p.wq[br], 0, DM * NH * DH * 2, RSRC_W3);
  const rsrc_t ro = __builtin_amdgcn_make_buffer_rsrc((void*)(p.wo[br] + (size_t)hg * 512 * DM), 0, 512 * DM * 2, RSRC_W3);

  f32x4 accv[20];
  u32x4 W[4][8];
  u32x4 A[2];

  auto gmf = [&](auto FI, auto TI, auto BI, auto ZI, const u32x4& Af) {
    constexpr int f = FI, t = TI, bq = A_B + 24 * BI + 4 * t;
    constexpr bool z = ZI;
    if constexpr (tile_v(f, t)) mf_v_va<bq, z>(accv[tile_vi(f, t)], Af);
    else mf_a_va<tile_a(f, t), bq, z>(Af);
  };
  auto gpk = [&](auto PI, auto TI, auto RI) -> unsigned {
    constexpr int pp = PI, t = TI, r = RI;
    if constexpr (tile_v(2 * pp, t)) return pk_bf16(accv[tile_vi(2 * pp, t)][r], accv[tile_vi(2 * pp + 1, t)][r]);
    else return pk_acc<tile_a(2 * pp, t) + r, tile_a(2 * pp + 1, t) + r>();
  };

  const int vx = g * 1024 + ((q ^ g ^ (4 * w)) << 4);
  const int vwq = (8 * g) * 8192 + (hg * 512 + w * 128 + 8 * cchunk) * 2;
  nop<4>();
  const int vk = lane * 16;
  auto xdma = [&](auto II) {
    constexpr int qt = II / 6, i = II % 6;
    dma_x(lb + qt * XQ_BYTES + (4 * i + w) * 1024, rx, vx, (4 * i + w) * 4096 + qt * 256);
  };
  const int vpf = ((lane >> 5) * 32 + 2 * b + (w >> 1)) * 8192 + hg * 1024 + (w & 1) * 512 + (lane & 31) * 16;
  u32x4 pfd = u32x4{0, 0, 0, 0};
  nop<4>();
  STAMP(8);
  {
    unsigned long long pc;
    int len;
    asm volatile("s_getpc_b64 %0\n.Lk1_pc:\n\ts_mov_b32 %1, .Lk2_end-.Lk1_pc" : "=s"(pc), "=s"(len));
    const rsrc_t rc = __builtin_amdgcn_make_buffer_rsrc((void*)pc, 0, len, RSRC_W3);
    nop<4>();
    ld_pfv(pfd, rc, vk, (idx * 4 + w) * 1024);
  }
  ld_pfv(pfd, rq, vpf, 0);
  ld_pfv(pfd, rq, vpf, 64 * 8192);
  sfor<6>([&](auto I) { xdma(I); });
  STAMP(9);
  wait_vm<7>();
  STAMP(10);
  sfor<16>([&](auto I) { ld_w<0>(W[I >> 3][I & 7], rq, vwq, (32 * (I >> 3) + (I & 7)) * 8192); });
  int xa[2][4];
#pragma unroll
  for (int h = 0; h < 2; ++h)
#pragma unroll
    for (int j = 0; j < 4; ++j) xa[h][j] = lb + h * 2 * XQ_BYTES + q * 256 + (((4 * j + g) ^ q) << 4);
  const int kst = lb + V1_BASE + w * 16384 + 16384 - 16 * 272;
  const int kwa = kst + g * 272 + q * 16, kra = kst + q * 272 + g * 64;
  const int vv0 = g * 256 + ((q ^ (((g >> 1) << 2) | (g & 1))) << 4);
  const int vv1 = vv0 ^ 128;
  auto vdma = [&](auto JI) {
    constexpr int j = JI;
    dma_v(j < 16 ? lb + V1_BASE + w * 16384 + j * 1024 : lb + w * 8192 + (j - 16) * 1024, rv, (j & 1) ? vv1 : vv0, j * 1024);
  };
  STAMP(11);
  SCHED;
  wait_vm<A_NP - 9>();
  barrier();
  STAMP(1);

  auto xrd = [&](auto SI, auto TI) {
    constexpr int s = SI, t = TI;
    lds_rd_a<A_B + 24 * (s & 1) + 4 * t, ((s >> 2) & 1) * XQ_BYTES + t * 4096>(xa[s >> 3][s & 3]);
  };
  sfor<6>([&](auto T) { xrd(IC<0>{}, T); });
  wait_vm<8>();
  SCHED;
#pragma unroll
  for (int pp = 0; pp < 4; ++pp) A[0][pp] = aperm(W[0], 0, pp);
  SCHED;
  wait_lgkm0();
  nop<0>();
  SCHED;
  sfor<128>([&](auto NI) {
    constexpr int n = NI, s = n >> 3, f = n & 7;
    constexpr int kt = a_kt(s), ktc = kt < 0 ? 0 : kt;
    if constexpr (f == 0 && A_S.bar[s]) {
      barrier();
      SCHED;
    }
    sfor<6>([&](auto TI) {
      constexpr int t = TI;
      gmf(IC<f>{}, TI, IC<(s & 1)>{}, std::bool_constant<s == 0>{}, A[n & 1]);
      SCHED;
      if constexpr (t == 0 && f == 7 && s < 15) wait_vm<a_wait(s)>();
      constexpr int op = (t & 1) ? 0 : A_S.op[s][3 * f + t / 2], j = op % 1000;
      if constexpr (op >= OW) ld_w<0>(W[(j >> 3) & 3][j & 7], rq, vwq, (32 * (j >> 3) + (j & 7)) * 8192);
      else if constexpr (op >= OV) vdma(IC<j>{});
      else if constexpr (op >= OP) ld_pfv(pfd, rq, vpf, j * 64 * 8192);
      else if constexpr (op >= OK_) ld_k<A_K + 4 * j, (j & 3) * 1024>(rk, vk, (j >> 2) * 4096);
      else if constexpr (op >= OX) xdma(IC<j>{});
      if constexpr (t == 5) {
        if constexpr (s + 1 < 16 && f < 6) xrd(IC<s + 1>{}, IC<f>{});
        else if constexpr (f == 7) wait_lgkm0();
        else if constexpr (kt >= 0 && f == 6) lds_rd_a<A_K + 16 * ktc + 12, 48>(kra);
        else nop<0>();
      }
      if constexpr (n + 1 < 128 && t >= 1 && t <= 4) A[(n + 1) & 1][t - 1] = aperm(W[((n + 1) >> 3) & 3], (n + 1) & 7, t - 1);
      if constexpr (t == 3 && kt >= 0) {
        if constexpr (f < 4) lds_wr_a<A_K + 16 * ktc + 4 * f, f * 4 * 272>(kwa);
        else if constexpr (f < 7) lds_rd_a<A_K + 16 * ktc + 4 * (f - 4), (f - 4) * 16>(kra);
      }
      SCHED;
    });
  });
  asm volatile("" ::"v"(pfd));
  STAMP(2);

  u32x4 Qf[2][4];
  f32x4 sacc[2][6];
  u32x4 Pf[6][3];
  f32x4 oacc[2][8];
  f32x4 lacc[2];
  float mA = 0.f, mB = 0.f, li = 0.f;
  float ev[24];
  const float c = 0.08838834764831845f * 1.4426950408889634f;

  auto qp = [&](auto TI, auto JI) {
    constexpr int t = TI, j = JI, pp = j >> 2, s = j & 3;
    Qf[t & 1][s][pp] = gpk(IC<pp>{}, TI, IC<s>{});
  };
  auto sm = [&](auto TI, auto JI) {
    constexpr int t = TI, j = JI;
    const f32x4(&S)[6] = sacc[t & 1];
    auto v = [&](int i) -> float { return S[i >> 2][i & 3]; };
    if constexpr (j == 0) max3x2i(mA, mB, v(0), v(1), v(2), v(3), v(4), v(5));
    else if constexpr (j < 5) max3x2(mA, mB, v(4 * j + 2), v(4 * j + 3), v(4 * j + 4), v(4 * j + 5));
    else if constexpr (j == 5) max_fin16(mA, mB, v(22), v(23));
    else if constexpr (j == 6) max_fin32(mA, mB, -c);
    else if constexpr (j < 7 + 24) {
      constexpr int i = j - 7;
      ev[i] = __builtin_amdgcn_exp2f(__builtin_fmaf(v(i), c, mA));
    } else {
      constexpr int i = j - 31;
      Pf[t][i >> 2][i & 3] = pk_bf16(ev[2 * i], ev[2 * i + 1]);
    }
  };
  constexpr int SM_N = 43;
  const int vo = lane * 16;
  const int oa1 = lb + V1_BASE + w * 16384 + vo, oa2 = lb + w * 8192 + vo;
  u32x4 ot;
  auto oe = [&](auto TI, auto JI) {
    constexpr int t = TI, j = JI;
    if constexpr (j == 0) {
      asm volatile("" : "+v"(lacc[t & 1]));
      asm volatile("v_rcp_f32 %0, %1\n\ts_nop 0" : "=v"(li) : "v"(lacc[t & 1][0]));
    }
    else {
      constexpr int i = j - 1, sp = i / 5, e = i % 5;
      if constexpr (e < 4) {
        const f32x4& o = oacc[t & 1][2 * sp + (e >> 1)];
        ot[e] = pk_scaled(o[2 * (e & 1)], o[2 * (e & 1) + 1], li);
      } else {
        constexpr int fi = sp * 6 + t;
        if constexpr (fi < 16) lds_wr<fi * 1024>(oa1, ot);
        else lds_wr<(fi - 16) * 1024>(oa2, ot);
      }
    }
  };
  constexpr int OE_N = 21;
  const int vtr = (4 * g + ((lane >> 2) & 3)) * 256 +
                  (((lane & 3) ^ (((g & 1) << 3) | (((lane >> 3) & 1) << 2) | ((lane >> 2) & 1))) << 4) + 8 * (lane & 1);
  const int vt1 = lb + V1_BASE + w * 16384 + vtr, vt2 = lb + w * 8192 + vtr;
  auto vrd = [&](auto JI) {
    constexpr int j = JI, f = j / 6, t3 = (j % 6) >> 1, hh = j & 1, sp = f >> 1, h = f & 1;
    constexpr int x = (sp << 6) ^ (h << 3), d = A_V + 4 * (t3 * 8 + f) + 2 * hh;
    if constexpr (t3 < 2) lds_rdtr_a<d, (32 * t3 + 16 * hh) * 256>(vt1 ^ x);
    else lds_rdtr_a<d, 16 * hh * 256>(vt2 ^ x);
  };
  const int vwoA = (8 * g + 4 * (g & 1)) * 1024 + (w * 128 + 8 * cchunk) * 2;
  const int vwoB = (8 * g - 4 * (g & 1) + 4) * 1024 + (w * 128 + 8 * cchunk) * 2;
  auto wold = [&](auto SI, auto II) {
    constexpr int S = SI, i = II;
    const int so = (((w + (S >> 2)) & 3) * 128 + 32 * (S & 3)) * 1024;
    ld_w<(i & 3) * 1024>(W[S & 3][i], ro, i < 4 ? vwoA : vwoB, so);
  };

  sfor<16>([&](auto J) { qp(IC<0>{}, J); });
  SCHED;
  nop<1>();
  sfor<6>([&](auto TI) {
    constexpr int t = TI;
    constexpr int NQ = t < 5 ? 16 : 0, NS = t > 0 ? SM_N : 0, NV = t == 5 ? 48 : 0;
    constexpr int NF = NQ + NS + NV;
    if constexpr (t == 5) wait_vm<PF_WO>();
    sfor<24>([&](auto MI) {
      constexpr int m = MI, s = m / 6, kt = m % 6;
      mf_v_av<A_K + 4 * (kt * 4 + s), s == 0>(sacc[t & 1][kt], Qf[t & 1][s]);
      SCHED;
      if constexpr (m % 8 == 2 && t < 4 && 3 * t + m / 8 < B_NV) vdma(IC<B_V[(3 * t + m / 8) < B_NV ? 3 * t + m / 8 : 0]>{});
      if constexpr (t == 4 && m % 3 == 0 && m / 3 < PF_WO) ld_pf<A_B + 4 * (m / 3)>(ro, vk, (32 * b + 8 * w + m / 3) * 1024);
      constexpr int lo = m * NF / 22 < NF ? m * NF / 22 : NF, hi = (m + 1) * NF / 22 < NF ? (m + 1) * NF / 22 : NF;
      sfor<hi - lo>([&](auto KI) {
        constexpr int k = lo + KI;
        if constexpr (k < NS) sm(IC<t - 1>{}, IC<k>{});
        else if constexpr (k < NS + NQ) qp(IC<t + 1>{}, IC<k - NS>{});
        else vrd(IC<k - NS - NQ>{});
      });
      SCHED;
    });
  });
  wait_lgkm0();
  STAMP(3);
  u32x4 ones = u32x4{0x3F803F80u, 0x3F803F80u, 0x3F803F80u, 0x3F803F80u};
  asm volatile("" : "+v"(ones));
  nop<1>();
  sfor<6>([&](auto TI) {
    constexpr int t = TI;
    constexpr int NS = t == 0 ? SM_N : 0, NO = t > 0 ? OE_N : 0, NW = t >= 3 ? 8 : 0;
    constexpr int NF = NS + NO + NW;
    sfor<27>([&](auto MI) {
      constexpr int m = MI, t3 = m / 9, f = m % 9;
      if constexpr (f > 0) mf_v_av<A_V + 4 * (t3 * 8 + f - 1), t3 == 0>(oacc[t & 1][f - 1], Pf[t][t3]);
      else mf_v_vv<t3 == 0>(lacc[t & 1], ones, Pf[t][t3]);
      SCHED;
      constexpr int lo = m * NF / 27, hi = (m + 1) * NF / 27;
      sfor<hi - lo>([&](auto KI) {
        constexpr int k = lo + KI;
        if constexpr (k < NS) sm(IC<5>{}, IC<k>{});
        else if constexpr (k < NS + NO) oe(IC<t - 1>{}, IC<k - NS>{});
        else wold(IC<t - 3>{}, IC<k - NS - NO>{});
      });
      SCHED;
    });
  });
  sfor<OE_N>([&](auto J) { oe(IC<5>{}, J); });
  SCHED;
  STAMP(4);

  int ob[4][2];
#pragma unroll
  for (int h = 0; h < 4; ++h) {
    const int wp = (w + h) & 3;
    ob[h][0] = lb + V1_BASE + wp * 16384 + vo;
    ob[h][1] = lb + wp * 8192 + vo;
  }
  auto ord = [&](auto SI, auto TI) {
    constexpr int S = SI, t = TI, fi = (S & 3) * 6 + t;
    if constexpr (fi < 16) lds_rd_a<A_B + 24 * (S & 1) + 4 * t, fi * 1024>(ob[S >> 2][0]);
    else lds_rd_a<A_B + 24 * (S & 1) + 4 * t, (fi - 16) * 1024>(ob[S >> 2][1]);
  };
  wait_vm<16>();
  sfor<6>([&](auto T) { ord(IC<0>{}, T); });
  SCHED;
#pragma unroll
  for (int pp = 0; pp < 4; ++pp) A[0][pp] = aperm(W[0], 0, pp);
  SCHED;
  wait_lgkm0();
  nop<0>();
  SCHED;
  sfor<128>([&](auto NI) {
    constexpr int n = NI, s = n >> 3, f = n & 7;
    if constexpr (n == 24) {
      barrier();
      SCHED;
    }
    sfor<6>([&](auto TI) {
      constexpr int t = TI;
      gmf(IC<f>{}, TI, IC<(s & 1)>{}, std::bool_constant<s == 0>{}, A[n & 1]);
      SCHED;
      if constexpr ((t == 0 || t == 2) && f < 4 && s + 3 < 16) wold(IC<s + 3>{}, IC<(2 * f + (t >> 1)) & 7>{});
      if constexpr (t == 0 && f == 7 && s < 15) wait_vm<(s <= 12 ? 16 : s == 13 ? 8 : 0)>();
      if constexpr (t == 5) {
        if constexpr (s + 1 < 16 && f < 6) ord(IC<s + 1>{}, IC<f>{});
        else if constexpr (s + 1 < 16 && f == 7) wait_lgkm0();
        else nop<0>();
      }
      if constexpr (n + 1 < 128 && t >= 1 && t <= 4) A[(n + 1) & 1][t - 1] = aperm(W[((n + 1) >> 3) & 3], (n + 1) & 7, t - 1);
      SCHED;
    });
  });
  STAMP(5);
  {
    const rsrc_t rp = __builtin_amdgcn_make_buffer_rsrc((void*)(part + (size_t)(((br * NBATCH + b) * 8 + hg) * 4 + w) * 24 * 64), 0,
                                                        24 * 1024, RSRC_W3);
    nop<7>();
    sfor<24>([&](auto I) {
      constexpr int t = I >> 2, r = I & 3;
      u32x4 o;
      sfor<4>([&](auto P) { o[(int)P] = gpk(P, IC<t>{}, IC<r>{}); });
      __builtin_amdgcn_raw_buffer_store_b128(o, rp, vo + (I & 3) * 1024, (I >> 2) * 4096, AUX_PST);
      SCHED;
      nop<1>();
      SCHED;
    });
  }
  STAMP(6);
}

__global__ __launch_bounds__(256) void k2_reduce(const u32x4* __restrict__ part, unsigned short* __restrict__ out) {
  const int i = blockIdx.x * 256 + threadIdx.x;
  const int bb = i / 6144, e = i % 6144;
  const rsrc_t rp = __builtin_amdgcn_make_buffer_rsrc((void*)(part + (size_t)bb * 8 * 6144), 0, 8 * 6144 * 16, RSRC_W3);
  u32x4 v[8];
#pragma unroll
  for (int h = 0; h < 8; ++h) v[h] = __builtin_amdgcn_raw_buffer_load_b128(rp, e * 16, h * 6144 * 16, AUX_PLD);
  float s[8];
#pragma unroll
  for (int j = 0; j < 8; ++j) s[j] = 0.f;
#pragma unroll
  for (int h = 0; h < 8; ++h)
#pragma unroll
    for (int j = 0; j < 4; ++j) {
      s[2 * j] += __builtin_bit_cast(float, v[h][j] << 16);
      s[2 * j + 1] += __builtin_bit_cast(float, v[h][j] & 0xffff0000u);
    }
  u32x4 o;
#pragma unroll
  for (int j = 0; j < 4; ++j) o[j] = pk_bf16(s[2 * j], s[2 * j + 1]);
  const int lane = e & 63, pc = e >> 6, r = pc & 3, t = (pc >> 2) % 6, w = pc / 24;
  const int q = lane & 15, g = lane >> 4;
  const int br = bb >> 4, b = bb & 15;
  *(u32x4*)(out + ((size_t)b * SQ + 16 * t + q) * 1024 + br * 512 + 128 * w + 32 * g + 8 * r) = o;
  asm volatile(".Lk2_end:");
}

extern "C" void kernel_launch(void* const* d_in, const int* in_sizes, int n_in, void* d_out, int out_size, void* d_ws,
                              size_t ws_size, hipStream_t stream) {
  Ptrs p;
  for (int br = 0; br < 2; ++br) {
    p.x[br] = (const unsigned short*)d_in[5 * br + 0];
    p.k[br] = (const unsigned short*)d_in[5 * br + 1];
    p.v[br] = (const unsigned short*)d_in[5 * br + 2];
    p.wq[br] = (const unsigned short*)d_in[5 * br + 3];
    p.wo[br] = (const unsigned short*)d_in[5 * br + 4];
  }
  u32x4* part = (u32x4*)d_ws;
  (void)hipFuncSetAttribute((const void*)k1_fused, hipFuncAttributeMaxDynamicSharedMemorySize, LDS_BYTES);
  k1_fused<<<256, 256, LDS_BYTES, stream>>>(p, part
  );
  k2_reduce<<<2 * 16 * 96 * 64 / 256, 256, 0, stream>>>(part, (unsigned short*)d_out);
}
```

```cpp
#include <hip/hip_runtime.h>
#include <hip/hip_bf16.h>
#include <utility>

typedef unsigned u32x2 __attribute__((ext_vector_type(2)));
typedef unsigned u32x4 __attribute__((ext_vector_type(4)));
typedef float f32x4 __attribute__((ext_vector_type(4)));
#define LDSP __attribute__((address_space(3)))
#define DI __device__ __forceinline__
typedef __amdgpu_buffer_rsrc_t rsrc_t;

constexpr int SQ = 96, DM = 512, NH = 32, DH = 128, NBATCH = 16;
constexpr int RSRC_W3 = 0x00020000;
#define POL_KV " nt"
#define POL_W ""
#define POL_PF ""
#define POL_X ""
#define AUX_PST 16
#define AUX_PLD 0

template <class F, int... I>
DI void sfor_impl(F&& f, std::integer_sequence<int, I...>) {
  (f(std::integral_constant<int, I>{}), ...);
}
template <int N, class F>
DI void sfor(F&& f) {
  sfor_impl(f, std::make_integer_sequence<int, N>{});
}
template <int N>
using IC = std::integral_constant<int, N>;

typedef float f32x2 __attribute__((ext_vector_type(2)));
typedef __bf16 bf16x2 __attribute__((ext_vector_type(2)));
DI unsigned pk_bf16(float lo, float hi) {
  return __builtin_bit_cast(unsigned, __builtin_convertvector(f32x2{lo, hi}, bf16x2));
}
DI unsigned perm_lo(unsigned e, unsigned o) { return __builtin_amdgcn_perm(o, e, 0x05040100u); }
DI unsigned perm_hi(unsigned e, unsigned o) { return __builtin_amdgcn_perm(o, e, 0x07060302u); }
#define SCHED __builtin_amdgcn_sched_barrier(0)

#define MF "v_mfma_f32_16x16x32_bf16 "
template <int D, int Bq, bool Z>
DI void mf_a_va(const u32x4& A) {
  if constexpr (Z) asm volatile(MF "a[%c0:%c1], %2, a[%c3:%c4], 0" ::"i"(D), "i"(D + 3), "v"(A), "i"(Bq), "i"(Bq + 3));
  else asm volatile(MF "a[%c0:%c1], %2, a[%c3:%c4], a[%c0:%c1]" ::"i"(D), "i"(D + 3), "v"(A), "i"(Bq), "i"(Bq + 3));
}
template <int Bq, bool Z>
DI void mf_v_va(f32x4& C, const u32x4& A) {
  if constexpr (Z) asm volatile(MF "%0, %1, a[%c2:%c3], 0" : "=&v"(C) : "v"(A), "i"(Bq), "i"(Bq + 3));
  else asm volatile(MF "%0, %1, a[%c2:%c3], %0" : "+v"(C) : "v"(A), "i"(Bq), "i"(Bq + 3));
}
template <int Aq, bool Z>
DI void mf_v_av(f32x4& C, const u32x4& Bv) {
  if constexpr (Z) asm volatile(MF "%0, a[%c2:%c3], %1, 0" : "=&v"(C) : "v"(Bv), "i"(Aq), "i"(Aq + 3));
  else asm volatile(MF "%0, a[%c2:%c3], %1, %0" : "+v"(C) : "v"(Bv), "i"(Aq), "i"(Aq + 3));
}
template <bool Z>
DI void mf_v_vv(f32x4& C, const u32x4& A, const u32x4& Bv) {
  if constexpr (Z) asm volatile(MF "%0, %1, %2, 0" : "=&v"(C) : "v"(A), "v"(Bv));
  else asm volatile(MF "%0, %1, %2, %0" : "+v"(C) : "v"(A), "v"(Bv));
}
template <int X, int Y>
DI unsigned pk_acc() {
  unsigned r, t0, t1;
  asm volatile("v_accvgpr_read_b32 %1, a[%c3]\n\tv_accvgpr_read_b32 %2, a[%c4]\n\tv_cvt_pk_bf16_f32 %0, %1, %2"
               : "=v"(r), "=&v"(t0), "=&v"(t1)
               : "i"(X), "i"(Y));
  return r;
}
template <int IMM>
DI void ld_w(u32x4& d, rsrc_t r, int voff, int soff) {
  asm volatile("buffer_load_dwordx4 %0, %1, %2, %3 offen offset:%c4" POL_W : "=v"(d) : "v"(voff), "s"(r), "s"(soff), "i"(IMM));
}
template <int D, int IMM>
DI void ld_k(rsrc_t r, int voff, int soff) {
  asm volatile("buffer_load_dwordx4 a[%c0:%c1], %2, %3, %4 offen offset:%c5" POL_KV ::"i"(D), "i"(D + 3), "v"(voff), "s"(r),
               "s"(soff), "i"(IMM));
}
template <int D>
DI void ld_pf(rsrc_t r, int voff, int soff) {
  asm volatile("buffer_load_dwordx4 a[%c0:%c1], %2, %3, %4 offen" POL_PF ::"i"(D), "i"(D + 3), "v"(voff), "s"(r), "s"(soff));
}
DI void ld_pfv(u32x4& d, rsrc_t r, int voff, int soff) {
  asm volatile("buffer_load_dwordx4 %0, %1, %2, %3 offen" POL_PF : "+v"(d) : "v"(voff), "s"(r), "s"(soff));
}
DI void dma_x(int m0v, rsrc_t r, int voff, int soff) {
  asm volatile("s_mov_b32 m0, %0\n\ts_nop 0\n\tbuffer_load_dwordx4 %1, %2, %3 offen" POL_X " lds" ::"s"(m0v), "v"(voff), "s"(r),
               "s"(soff)
               : "memory");
}
DI void dma_v(int m0v, rsrc_t r, int voff, int soff) {
  asm volatile("s_mov_b32 m0, %0\n\ts_nop 0\n\tbuffer_load_dwordx4 %1, %2, %3 offen" POL_KV " lds" ::"s"(m0v), "v"(voff), "s"(r),
               "s"(soff)
               : "memory");
}
template <int D, int IMM>
DI void lds_rd_a(int addr) {
  asm volatile("ds_read_b128 a[%c0:%c1], %2 offset:%c3" ::"i"(D), "i"(D + 3), "v"(addr), "i"(IMM) : "memory");
}
template <int D, int IMM>
DI void lds_rdtr_a(int addr) {
  asm volatile("ds_read_b64_tr_b16 a[%c0:%c1], %2 offset:%c3" ::"i"(D), "i"(D + 1), "v"(addr), "i"(IMM) : "memory");
}
template <int D, int IMM>
DI void lds_wr_a(int addr) {
  asm volatile("ds_write_b128 %2, a[%c0:%c1] offset:%c3" ::"i"(D), "i"(D + 3), "v"(addr), "i"(IMM) : "memory");
}
template <int IMM>
DI void lds_wr(int addr, const u32x4& d) {
  asm volatile("ds_write_b128 %0, %1 offset:%c2" ::"v"(addr), "v"(d), "i"(IMM) : "memory");
}
template <int N>
DI void wait_vm() {
  static_assert(N >= 0 && N < 64, "vmcnt");
  asm volatile("s_waitcnt vmcnt(%c0)" ::"i"(N) : "memory");
}
DI void wait_lgkm0() { asm volatile("s_waitcnt lgkmcnt(0)" ::: "memory"); }
DI void barrier() { asm volatile("s_barrier" ::: "memory"); }
template <int N>
DI void nop() {
  asm volatile("s_nop %c0" ::"i"(N));
}
DI float max3(float a, float b, float c) { return __builtin_elementwise_maximum(__builtin_elementwise_maximum(a, b), c); }
DI void xmax16(float& a) {
  float t;
  asm volatile("v_mov_b32 %1, %0\n\ts_nop 1\n\tv_permlane16_swap_b32 %0, %1\n\tv_max_f32 %0, %0, %1" : "+v"(a), "=&v"(t));
}
DI void xmax32s(float& a, float s) {
  float t;
  asm volatile("v_mov_b32 %1, %0\n\ts_nop 1\n\tv_permlane32_swap_b32 %0, %1\n\tv_max_f32 %0, %0, %1\n\tv_mul_f32 %0, %2, %0"
               : "+v"(a), "=&v"(t)
               : "s"(s));
}
template <int X, int Y>
DI void acc_rd2(float& a, float& b) {
  asm volatile("v_accvgpr_read_b32 %0, a[%c2]\n\tv_accvgpr_read_b32 %1, a[%c3]" : "=v"(a), "=v"(b) : "i"(X), "i"(Y));
}

#define STAMP(i)

struct Ptrs {
  const unsigned short* x[2];
  const unsigned short* k[2];
  const unsigned short* v[2];
  const unsigned short* wq[2];
  const unsigned short* wo[2];
};

constexpr int LDS_BYTES = 163840;
constexpr int XQ_BYTES = 24576, V1_BASE = 98304;

constexpr int A_B = 112, A_K = 160, A_V = 0;
constexpr bool tile_v(int f, int t) { return t < 2 || (t == 2 && f < 4); }
constexpr int tile_vi(int f, int t) { return t * 8 + f; }
constexpr int tile_a(int f, int t) { return 4 * (t >= 3 ? (t - 3) * 8 + f : 24 + (f - 4)); }

#define PF_WO 8
enum { OX = 1000, OK_ = 2000, OP = 3000, OV = 4000, OW = 5000 };
struct ASched {
  int op[16][24];
  bool bar[16];
};
#define SCHV 0
#define X6(q) OX + 6 * q, OX + 6 * q + 1, OX + 6 * q + 2, OX + 6 * q + 3, OX + 6 * q + 4, OX + 6 * q + 5
#define K4(t) OK_ + 4 * t, OK_ + 4 * t + 1, OK_ + 4 * t + 2, OK_ + 4 * t + 3
#define V4(j) OV + j, OV + j + 1, OV + j + 2, OV + j + 3
constexpr int A_BURST[6][12] = {{X6(1), OP + 2, OP + 3}, {X6(2), OP + 4, OP + 5}, {K4(0), K4(1)},
                                {X6(3), OP + 6, OP + 7}, {K4(2), K4(3)},          {K4(4), K4(5)}};
constexpr int A_TAIL[3][8] = {{V4(0), V4(4)}, {OV + 8, OV + 9, OV + 10}, {OV + 16, OV + 17, OV + 18}};
constexpr int A_KST[6] = {8, 9, 12, 13, 14, 15};
constexpr int B_V[] = {19, 20, 21, 22, 23, 11, 12, 13, 14, 15};
constexpr int B_NV = 10, B_PFT = 4;
constexpr ASched a_make() {
  ASched a{};
  for (int s = 0; s <= 12; s += 2)
    for (int i = 0; i < 16; ++i) a.op[s][(i >> 1) * 3 + (i & 1)] = OW + 8 * (s + 2) + i;
  for (int s = 1; s <= 11; s += 2) {
    a.bar[s] = true;
    for (int i = 0; i < (int)(sizeof(A_BURST[0]) / sizeof(int)); ++i) a.op[s][i] = A_BURST[s >> 1][i];
  }
  for (int s = 13; s < 16; ++s) {
    int n = 0;
    while (n < 8 && A_TAIL[s - 13][n]) ++n;
    for (int i = 0; i < n; ++i) a.op[s][3 * (i * 8 / n)] = A_TAIL[s - 13][i];
  }
  return a;
}
constexpr ASched A_S = a_make();
constexpr int A_NP = 3 + 6 + 16;
constexpr int a_seq(int op) {
  if (op >= OW && op < OW + 16) return 9 + op - OW;
  int c = A_NP;
  for (int s = 0; s < 16; ++s)
    for (int k = 0; k < 24; ++k)
      if (A_S.op[s][k]) {
        if (A_S.op[s][k] == op) return c;
        ++c;
      }
  return -1;
}
constexpr int a_issued(int s, int k) {
  int c = A_NP;
  for (int i = 0; i < 16; ++i)
    for (int j = 0; j < 24; ++j)
      if (A_S.op[i][j] && (i < s || (i == s && j < k))) ++c;
  return c;
}
constexpr int a_kt(int s) {
  for (int k = 0; k < 6; ++k)
    if (A_KST[k] == s) return k;
  return -1;
}
constexpr int imax(int a, int b) { return a > b ? a : b; }
constexpr int a_need(int s) {
  int n = 0;
  for (int i = 0; i < 8; ++i) n = imax(n, a_seq(OW + 8 * (s + 1) + i));
  if (s == 2 || s == 6 || s == 10)
    for (int i = 0; i < 6; ++i) n = imax(n, a_seq(OX + 6 * ((s + 2) / 4) + i));
  if (a_kt(s + 1) >= 0)
    for (int i = 0; i < 4; ++i) n = imax(n, a_seq(OK_ + 4 * a_kt(s + 1) + i));
  if (s == 14)
    for (int i = 2; i < 8; ++i) n = imax(n, a_seq(OP + i));
  return n;
}
constexpr int a_wait(int s) { return a_issued(s, 21) - a_need(s) - 1; }
constexpr bool a_check() {
  for (int i = 0; i < 24; ++i)
    if (a_seq(OK_ + i) < 0) return false;
  for (int i = 6; i < 24; ++i)
    if (a_seq(OX + i) < 0) return false;
  for (int i = 0; i < 128; ++i)
    if (a_seq(OW + i) < 0 || (i >= 32 && a_seq(OW + i) < a_issued(i / 8 - 3, 0))) return false;
  int nv = B_NV;
  for (int i = 0; i < 24; ++i)
    if (a_seq(OV + i) >= 0) {
      ++nv;
      if (i >= 11 && i < 16 && a_seq(OV + i) < a_issued(A_KST[5] + 1, 0)) return false;
      if (i >= 16 && a_seq(OV + i) < a_issued(7, 0)) return false;
    }
  for (int s = 0; s < 15; ++s)
    if (a_wait(s) < 0 || a_wait(s) > 63) return false;
  return nv == 24 && A_S.bar[3] && A_S.bar[7] && A_S.bar[11];
}
static_assert(a_check(), "phase A schedule");

DI unsigned aperm(const u32x4 (&W)[8], int f, int p) {
  return (f & 1) ? perm_hi(W[2 * p][f >> 1], W[2 * p + 1][f >> 1]) : perm_lo(W[2 * p][f >> 1], W[2 * p + 1][f >> 1]);
}

__global__ __launch_bounds__(256, 1) void k1_fused(Ptrs p, u32x4* __restrict__ part
) {
  extern __shared__ __attribute__((aligned(16))) char lds[];
  asm volatile("" ::: "a0", "a255");
  const int bid = blockIdx.x;
  const int xcd = bid & 7, idx = bid >> 3;
  const int br = xcd >> 2, hg = (xcd & 3) * 2 + (idx & 1), b = idx >> 1;
  const int tid = threadIdx.x, lane = tid & 63;
  const int w = __builtin_amdgcn_readfirstlane(tid >> 6);
  const int q = lane & 15, g = lane >> 4;
  const int cchunk = q;
  const int lb = (int)(unsigned)(size_t)(LDSP char*)lds;
  STAMP(0);

  const int head = hg * 4 + w;
  const size_t kvoff = ((size_t)b * NH + head) * SQ * DH;
  const rsrc_t rx = __builtin_amdgcn_make_buffer_rsrc((void*)(p.x[br] + (size_t)b * SQ * DM), 0, SQ * DM * 2, RSRC_W3);
  const rsrc_t rk = __builtin_amdgcn_make_buffer_rsrc((void*)(p.k[br] + kvoff), 0, SQ * DH * 2, RSRC_W3);
  const rsrc_t rv = __builtin_amdgcn_make_buffer_rsrc((void*)(p.v[br] + kvoff), 0, SQ * DH * 2, RSRC_W3);
  const rsrc_t rq = __builtin_amdgcn_make_buffer_rsrc((void*)p.wq[br], 0, DM * NH * DH * 2, RSRC_W3);
  const rsrc_t ro = __builtin_amdgcn_make_buffer_rsrc((void*)(p.wo[br] + (size_t)hg * 512 * DM), 0, 512 * DM * 2, RSRC_W3);

  f32x4 accv[20];
  u32x4 W[4][8];
  u32x4 A[2];

  auto gmf = [&](auto FI, auto TI, auto BI, auto ZI, const u32x4& Af) {
    constexpr int f = FI, t = TI, bq = A_B + 24 * BI + 4 * t;
    constexpr bool z = ZI;
    if constexpr (tile_v(f, t)) mf_v_va<bq, z>(accv[tile_vi(f, t)], Af);
    else mf_a_va<tile_a(f, t), bq, z>(Af);
  };
  auto gpk = [&](auto PI, auto TI, auto RI) -> unsigned {
    constexpr int pp = PI, t = TI, r = RI;
    if constexpr (tile_v(2 * pp, t)) return pk_bf16(accv[tile_vi(2 * pp, t)][r], accv[tile_vi(2 * pp + 1, t)][r]);
    else return pk_acc<tile_a(2 * pp, t) + r, tile_a(2 * pp + 1, t) + r>();
  };

  const int vx = g * 1024 + ((q ^ g ^ (4 * w)) << 4);
  const int vwq = (8 * g) * 8192 + (hg * 512 + w * 128 + 8 * cchunk) * 2;
  nop<4>();
  const int vk = lane * 16;
  auto xdma = [&](auto II) {
    constexpr int qt = II / 6, i = II % 6;
    dma_x(lb + qt * XQ_BYTES + (4 * i + w) * 1024, rx, vx, (4 * i + w) * 4096 + qt * 256);
  };
  const int vpf = ((lane >> 5) * 32 + 2 * b + (w >> 1)) * 8192 + hg * 1024 + (w & 1) * 512 + (lane & 31) * 16;
  u32x4 pfd = u32x4{0, 0, 0, 0};
  nop<4>();
  STAMP(8);
  {
    unsigned long long pc;
    int len;
    asm volatile("s_getpc_b64 %0\n.Lk1_pc:\n\ts_mov_b32 %1, .Lk2_end-.Lk1_pc" : "=s"(pc), "=s"(len));
    const rsrc_t rc = __builtin_amdgcn_make_buffer_rsrc((void*)pc, 0, len, RSRC_W3);
    nop<4>();
    ld_pfv(pfd, rc, vk, (idx * 4 + w) * 1024);
  }
  ld_pfv(pfd, rq, vpf, 0);
  ld_pfv(pfd, rq, vpf, 64 * 8192);
  sfor<6>([&](auto I) { xdma(I); });
  STAMP(9);
  wait_vm<7>();
  STAMP(10);
  sfor<16>([&](auto I) { ld_w<0>(W[I >> 3][I & 7], rq, vwq, (32 * (I >> 3) + (I & 7)) * 8192); });
  int xa[2][4];
#pragma unroll
  for (int h = 0; h < 2; ++h)
#pragma unroll
    for (int j = 0; j < 4; ++j) xa[h][j] = lb + h * 2 * XQ_BYTES + q * 256 + (((4 * j + g) ^ q) << 4);
  const int kst = lb + V1_BASE + w * 16384 + 16384 - 16 * 272;
  const int kwa = kst + g * 272 + q * 16, kra = kst + q * 272 + g * 64;
  const int vv0 = g * 256 + ((q ^ (((g >> 1) << 2) | (g & 1))) << 4);
  const int vv1 = vv0 ^ 128;
  auto vdma = [&](auto JI) {
    constexpr int j = JI;
    dma_v(j < 16 ? lb + V1_BASE + w * 16384 + j * 1024 : lb + w * 8192 + (j - 16) * 1024, rv, (j & 1) ? vv1 : vv0, j * 1024);
  };
  STAMP(11);
  SCHED;
  wait_vm<A_NP - 9>();
  barrier();
  STAMP(1);

  auto xrd = [&](auto SI, auto TI) {
    constexpr int s = SI, t = TI;
    lds_rd_a<A_B + 24 * (s & 1) + 4 * t, ((s >> 2) & 1) * XQ_BYTES + t * 4096>(xa[s >> 3][s & 3]);
  };
  sfor<6>([&](auto T) { xrd(IC<0>{}, T); });
  wait_vm<8>();
  SCHED;
#pragma unroll
  for (int pp = 0; pp < 4; ++pp) A[0][pp] = aperm(W[0], 0, pp);
  SCHED;
  wait_lgkm0();
  nop<0>();
  SCHED;
  sfor<128>([&](auto NI) {
    constexpr int n = NI, s = n >> 3, f = n & 7;
    constexpr int kt = a_kt(s), ktc = kt < 0 ? 0 : kt;
    if constexpr (f == 0 && A_S.bar[s]) {
      barrier();
      SCHED;
    }
    sfor<6>([&](auto TI) {
      constexpr int t = TI;
      gmf(IC<f>{}, TI, IC<(s & 1)>{}, std::bool_constant<s == 0>{}, A[n & 1]);
      SCHED;
      if constexpr (t == 0 && f == 7 && s < 15) wait_vm<a_wait(s)>();
      constexpr int op = (t & 1) ? 0 : A_S.op[s][3 * f + t / 2], j = op % 1000;
      if constexpr (op >= OW) ld_w<0>(W[(j >> 3) & 3][j & 7], rq, vwq, (32 * (j >> 3) + (j & 7)) * 8192);
      else if constexpr (op >= OV) vdma(IC<j>{});
      else if constexpr (op >= OP) ld_pfv(pfd, rq, vpf, j * 64 * 8192);
      else if constexpr (op >= OK_) ld_k<A_K + 4 * j, (j & 3) * 1024>(rk, vk, (j >> 2) * 4096);
      else if constexpr (op >= OX) xdma(IC<j>{});
      if constexpr (t == 5) {
        if constexpr (s + 1 < 16 && f < 6) xrd(IC<s + 1>{}, IC<f>{});
        else if constexpr (f == 7) wait_lgkm0();
        else if constexpr (kt >= 0 && f == 6) lds_rd_a<A_K + 16 * ktc + 12, 48>(kra);
        else nop<0>();
      }
      if constexpr (n + 1 < 128 && t >= 1 && t <= 4) A[(n + 1) & 1][t - 1] = aperm(W[((n + 1) >> 3) & 3], (n + 1) & 7, t - 1);
      if constexpr (t == 3 && kt >= 0) {
        if constexpr (f < 4) lds_wr_a<A_K + 16 * ktc + 4 * f, f * 4 * 272>(kwa);
        else if constexpr (f < 7) lds_rd_a<A_K + 16 * ktc + 4 * (f - 4), (f - 4) * 16>(kra);
      }
      SCHED;
    });
  });
  asm volatile("" ::"v"(pfd));
  STAMP(2);

  u32x4 Qf[2][4];
  f32x4 sacc[2][6];
  u32x4 Pf[6][3];
  f32x4 oacc[2][8];
  f32x4 lacc[2];
  float mA = 0.f, mB = 0.f, li = 0.f;
  float ev[24];
  const float c = 0.08838834764831845f * 1.4426950408889634f;

  float ta[2][2];
  auto qp = [&](auto TI, auto JI) {
    constexpr int t = TI, j = JI, pp = (j & 15) >> 2, s = j & 3, i = j > 0 ? j - 1 : 0;
    if constexpr (j < 16) {
      if constexpr (tile_v(2 * pp, t)) Qf[t & 1][s][pp] = gpk(IC<pp>{}, TI, IC<s>{});
      else acc_rd2<tile_a(2 * pp, t) + s, tile_a(2 * pp + 1, t) + s>(ta[j & 1][0], ta[j & 1][1]);
    }
    if constexpr (j > 0 && !tile_v(2 * (i >> 2), t)) Qf[t & 1][i & 3][i >> 2] = pk_bf16(ta[i & 1][0], ta[i & 1][1]);
  };
  auto sm = [&](auto TI, auto JI) {
    constexpr int t = TI, j = JI;
    const f32x4(&S)[6] = sacc[t & 1];
    auto v = [&](int i) -> float { return S[i >> 2][i & 3]; };
    if constexpr (j == 0) {
      mA = max3(v(0), v(1), v(2));
      mB = max3(v(3), v(4), v(5));
    } else if constexpr (j < 5) {
      mA = max3(mA, v(4 * j + 2), v(4 * j + 3));
      mB = max3(mB, v(4 * j + 4), v(4 * j + 5));
    } else if constexpr (j == 5) {
      mA = __builtin_elementwise_maximum(max3(mA, v(22), v(23)), mB);
      xmax16(mA);
    } else if constexpr (j == 6) xmax32s(mA, -c);
    else if constexpr (j < 7 + 24) {
      constexpr int i = j - 7;
      ev[i] = __builtin_amdgcn_exp2f(__builtin_fmaf(v(i), c, mA));
    } else {
      constexpr int i = j - 31;
      Pf[t][i >> 2][i & 3] = pk_bf16(ev[2 * i], ev[2 * i + 1]);
    }
  };
  constexpr int SM_N = 43;
  const int vo = lane * 16;
  const int oa1 = lb + V1_BASE + w * 16384 + vo, oa2 = lb + w * 8192 + vo;
  u32x4 ot;
  auto oe = [&](auto TI, auto JI) {
    constexpr int t = TI, j = JI;
    if constexpr (j == 0) {
      asm volatile("" : "+v"(lacc[t & 1]));
      li = __builtin_amdgcn_rcpf(lacc[t & 1][0]);
    }
    else {
      constexpr int i = j - 1, sp = i / 5, e = i % 5;
      if constexpr (e < 4) {
        const f32x4& o = oacc[t & 1][2 * sp + (e >> 1)];
        ot[e] = pk_bf16(o[2 * (e & 1)] * li, __builtin_fmaf(o[2 * (e & 1) + 1], li, 0.f));
      } else {
        constexpr int fi = sp * 6 + t;
        if constexpr (fi < 16) lds_wr<fi * 1024>(oa1, ot);
        else lds_wr<(fi - 16) * 1024>(oa2, ot);
      }
    }
  };
  constexpr int OE_N = 21;
  const int vtr = (4 * g + ((lane >> 2) & 3)) * 256 +
                  (((lane & 3) ^ (((g & 1) << 3) | (((lane >> 3) & 1) << 2) | ((lane >> 2) & 1))) << 4) + 8 * (lane & 1);
  const int vt1 = lb + V1_BASE + w * 16384 + vtr, vt2 = lb + w * 8192 + vtr;
  auto vrd = [&](auto JI) {
    constexpr int j = JI, f = j / 6, t3 = (j % 6) >> 1, hh = j & 1, sp = f >> 1, h = f & 1;
    constexpr int x = (sp << 6) ^ (h << 3), d = A_V + 4 * (t3 * 8 + f) + 2 * hh;
    if constexpr (t3 < 2) lds_rdtr_a<d, (32 * t3 + 16 * hh) * 256>(vt1 ^ x);
    else lds_rdtr_a<d, 16 * hh * 256>(vt2 ^ x);
  };
  const int vwoA = (8 * g + 4 * (g & 1)) * 1024 + (w * 128 + 8 * cchunk) * 2;
  const int vwoB = (8 * g - 4 * (g & 1) + 4) * 1024 + (w * 128 + 8 * cchunk) * 2;
  auto wold = [&](auto SI, auto II) {
    constexpr int S = SI, i = II;
    const int so = (((w + (S >> 2)) & 3) * 128 + 32 * (S & 3)) * 1024;
    ld_w<(i & 3) * 1024>(W[S & 3][i], ro, i < 4 ? vwoA : vwoB, so);
  };

  sfor<16>([&](auto J) { qp(IC<0>{}, J); });
  SCHED;
  nop<1>();
  sfor<6>([&](auto TI) {
    constexpr int t = TI;
    constexpr int NQ = t < 5 ? 17 : 0, NS = t > 0 ? SM_N : 0, NV = t == 5 ? 48 : 0;
    constexpr int NF = NQ + NS + NV;
    if constexpr (t == 5) wait_vm<PF_WO>();
    sfor<24>([&](auto MI) {
      constexpr int m = MI, s = m / 6, kt = m % 6;
      mf_v_av<A_K + 4 * (kt * 4 + s), s == 0>(sacc[t & 1][kt], Qf[t & 1][s]);
      SCHED;
      if constexpr (m % 8 == 2 && t < 4 && 3 * t + m / 8 < B_NV) vdma(IC<B_V[(3 * t + m / 8) < B_NV ? 3 * t + m / 8 : 0]>{});
      if constexpr (t == B_PFT && m % 3 == 0 && m / 3 < PF_WO) ld_pf<A_B + 4 * (m / 3)>(ro, vk, (32 * b + 8 * w + m / 3) * 1024);
      constexpr int lo = m * NF / 22 < NF ? m * NF / 22 : NF, hi = (m + 1) * NF / 22 < NF ? (m + 1) * NF / 22 : NF;
      sfor<hi - lo>([&](auto KI) {
        constexpr int k = lo + KI;
        if constexpr (k < NS) sm(IC<t - 1>{}, IC<k>{});
        else if constexpr (k < NS + NQ) qp(IC<t + 1>{}, IC<k - NS>{});
        else vrd(IC<k - NS - NQ>{});
      });
      SCHED;
    });
  });
  wait_lgkm0();
  STAMP(3);
  u32x4 ones = u32x4{0x3F803F80u, 0x3F803F80u, 0x3F803F80u, 0x3F803F80u};
  asm volatile("" : "+v"(ones));
  nop<1>();
  sfor<6>([&](auto TI) {
    constexpr int t = TI;
    constexpr int NS = t == 0 ? SM_N : 0, NO = t > 0 ? OE_N : 0, NW = t >= 3 ? 8 : 0;
    constexpr int NF = NS + NO + NW;
    sfor<27>([&](auto MI) {
      constexpr int m = MI, t3 = m / 9, f = m % 9;
      if constexpr (f > 0) mf_v_av<A_V + 4 * (t3 * 8 + f - 1), t3 == 0>(oacc[t & 1][f - 1], Pf[t][t3]);
      else mf_v_vv<t3 == 0>(lacc[t & 1], ones, Pf[t][t3]);
      SCHED;
      constexpr int lo = m * NF / 27, hi = (m + 1) * NF / 27;
      sfor<hi - lo>([&](auto KI) {
        constexpr int k = lo + KI;
        if constexpr (k < NS) sm(IC<5>{}, IC<k>{});
        else if constexpr (k < NS + NO) oe(IC<t - 1>{}, IC<k - NS>{});
        else wold(IC<t - 3>{}, IC<k - NS - NO>{});
      });
      SCHED;
    });
  });
  sfor<OE_N>([&](auto J) { oe(IC<5>{}, J); });
  SCHED;
  STAMP(4);

  int ob[4][2];
#pragma unroll
  for (int h = 0; h < 4; ++h) {
    const int wp = (w + h) & 3;
    ob[h][0] = lb + V1_BASE + wp * 16384 + vo;
    ob[h][1] = lb + wp * 8192 + vo;
  }
  auto ord = [&](auto SI, auto TI) {
    constexpr int S = SI, t = TI, fi = (S & 3) * 6 + t;
    if constexpr (fi < 16) lds_rd_a<A_B + 24 * (S & 1) + 4 * t, fi * 1024>(ob[S >> 2][0]);
    else lds_rd_a<A_B + 24 * (S & 1) + 4 * t, (fi - 16) * 1024>(ob[S >> 2][1]);
  };
  wait_vm<16>();
  sfor<6>([&](auto T) { ord(IC<0>{}, T); });
  SCHED;
#pragma unroll
  for (int pp = 0; pp < 4; ++pp) A[0][pp] = aperm(W[0], 0, pp);
  SCHED;
  wait_lgkm0();
  nop<0>();
  SCHED;
  sfor<128>([&](auto NI) {
    constexpr int n = NI, s = n >> 3, f = n & 7;
    if constexpr (n == 24) {
      barrier();
      SCHED;
    }
    sfor<6>([&](auto TI) {
      constexpr int t = TI;
      gmf(IC<f>{}, TI, IC<(s & 1)>{}, std::bool_constant<s == 0>{}, A[n & 1]);
      SCHED;
      if constexpr ((t == 0 || t == 2) && f < 4 && s + 3 < 16) wold(IC<s + 3>{}, IC<(2 * f + (t >> 1)) & 7>{});
      if constexpr (t == 0 && f == 7 && s < 15) wait_vm<(s <= 12 ? 16 : s == 13 ? 8 : 0)>();
      if constexpr (t == 5) {
        if constexpr (s + 1 < 16 && f < 6) ord(IC<s + 1>{}, IC<f>{});
        else if constexpr (s + 1 < 16 && f == 7) wait_lgkm0();
        else nop<0>();
      }
      if constexpr (n + 1 < 128 && t >= 1 && t <= 4) A[(n + 1) & 1][t - 1] = aperm(W[((n + 1) >> 3) & 3], (n + 1) & 7, t - 1);
      SCHED;
    });
  });
  STAMP(5);
  {
    const rsrc_t rp = __builtin_amdgcn_make_buffer_rsrc((void*)(part + (size_t)(((br * NBATCH + b) * 8 + hg) * 4 + w) * 24 * 64), 0,
                                                        24 * 1024, RSRC_W3);
    nop<7>();
    sfor<24>([&](auto I) {
      constexpr int t = I >> 2, r = I & 3;
      u32x4 o;
      sfor<4>([&](auto P) { o[(int)P] = gpk(P, IC<t>{}, IC<r>{}); });
      __builtin_amdgcn_raw_buffer_store_b128(o, rp, vo + (I & 3) * 1024, (I >> 2) * 4096, AUX_PST);
      SCHED;
      nop<1>();
      SCHED;
    });
  }
  STAMP(6);
}

__global__ __launch_bounds__(256) void k2_reduce(const u32x4* __restrict__ part, unsigned short* __restrict__ out) {
  const int i = blockIdx.x * 256 + threadIdx.x;
  const int bb = i / 6144, e = i % 6144;
  const rsrc_t rp = __builtin_amdgcn_make_buffer_rsrc((void*)(part + (size_t)bb * 8 * 6144), 0, 8 * 6144 * 16, RSRC_W3);
  u32x4 v[8];
#pragma unroll
  for (int h = 0; h < 8; ++h) v[h] = __builtin_amdgcn_raw_buffer_load_b128(rp, e * 16, h * 6144 * 16, AUX_PLD);
  float s[8];
#pragma unroll
  for (int j = 0; j < 8; ++j) s[j] = 0.f;
#pragma unroll
  for (int h = 0; h < 8; ++h)
#pragma unroll
    for (int j = 0; j < 4; ++j) {
      s[2 * j] += __builtin_bit_cast(float, v[h][j] << 16);
      s[2 * j + 1] += __builtin_bit_cast(float, v[h][j] & 0xffff0000u);
    }
  u32x4 o;
#pragma unroll
  for (int j = 0; j < 4; ++j) o[j] = pk_bf16(s[2 * j], s[2 * j + 1]);
  const int lane = e & 63, pc = e >> 6, r = pc & 3, t = (pc >> 2) % 6, w = pc / 24;
  const int q = lane & 15, g = lane >> 4;
  const int br = bb >> 4, b = bb & 15;
  *(u32x4*)(out + ((size_t)b * SQ + 16 * t + q) * 1024 + br * 512 + 128 * w + 32 * g + 8 * r) = o;
  asm volatile(".Lk2_end:");
}

extern "C" void kernel_launch(void* const* d_in, const int* in_sizes, int n_in, void* d_out, int out_size, void* d_ws,
                              size_t ws_size, hipStream_t stream) {
  Ptrs p;
  for (int br = 0; br < 2; ++br) {
    p.x[br] = (const unsigned short*)d_in[5 * br + 0];
    p.k[br] = (const unsigned short*)d_in[5 * br + 1];
    p.v[br] = (const unsigned short*)d_in[5 * br + 2];
    p.wq[br] = (const unsigned short*)d_in[5 * br + 3];
    p.wo[br] = (const unsigned short*)d_in[5 * br + 4];
  }
  u32x4* part = (u32x4*)d_ws;
  (void)hipFuncSetAttribute((const void*)k1_fused, hipFuncAttributeMaxDynamicSharedMemorySize, LDS_BYTES);
  k1_fused<<<256, 256, LDS_BYTES, stream>>>(p, part
  );
  k2_reduce<<<2 * 16 * 96 * 64 / 256, 256, 0, stream>>>(part, (unsigned short*)d_out);
}
```

```cpp
#include <hip/hip_runtime.h>
#include <hip/hip_bf16.h>
#include <utility>

typedef unsigned u32x2 __attribute__((ext_vector_type(2)));
typedef unsigned u32x4 __attribute__((ext_vector_type(4)));
typedef float f32x4 __attribute__((ext_vector_type(4)));
#define LDSP __attribute__((address_space(3)))
#define DI __device__ __forceinline__
typedef __amdgpu_buffer_rsrc_t rsrc_t;

constexpr int SQ = 96, DM = 512, NH = 32, DH = 128, NBATCH = 16;
constexpr int RSRC_W3 = 0x00020000;
#define POL_KV " nt"
#define POL_W ""
#define POL_PF ""
#define POL_X ""
#define AUX_PST 16
#define AUX_PLD 0

template <class F, int... I>
DI void sfor_impl(F&& f, std::integer_sequence<int, I...>) {
  (f(std::integral_constant<int, I>{}), ...);
}
template <int N, class F>
DI void sfor(F&& f) {
  sfor_impl(f, std::make_integer_sequence<int, N>{});
}
template <int N>
using IC = std::integral_constant<int, N>;

typedef float f32x2 __attribute__((ext_vector_type(2)));
typedef __bf16 bf16x2 __attribute__((ext_vector_type(2)));
DI unsigned pk_bf16(float lo, float hi) {
  return __builtin_bit_cast(unsigned, __builtin_convertvector(f32x2{lo, hi}, bf16x2));
}
DI unsigned perm_lo(unsigned e, unsigned o) { return __builtin_amdgcn_perm(o, e, 0x05040100u); }
DI unsigned perm_hi(unsigned e, unsigned o) { return __builtin_amdgcn_perm(o, e, 0x07060302u); }
#define SCHED __builtin_amdgcn_sched_barrier(0)

#define MF "v_mfma_f32_16x16x32_bf16 "
template <int D, int Bq, bool Z>
DI void mf_a_va(const u32x4& A) {
  if constexpr (Z) asm volatile(MF "a[%c0:%c1], %2, a[%c3:%c4], 0" ::"i"(D), "i"(D + 3), "v"(A), "i"(Bq), "i"(Bq + 3));
  else asm volatile(MF "a[%c0:%c1], %2, a[%c3:%c4], a[%c0:%c1]" ::"i"(D), "i"(D + 3), "v"(A), "i"(Bq), "i"(Bq + 3));
}
template <int Bq, bool Z>
DI void mf_v_va(f32x4& C, const u32x4& A) {
  if constexpr (Z) asm volatile(MF "%0, %1, a[%c2:%c3], 0" : "=&v"(C) : "v"(A), "i"(Bq), "i"(Bq + 3));
  else asm volatile(MF "%0, %1, a[%c2:%c3], %0" : "+v"(C) : "v"(A), "i"(Bq), "i"(Bq + 3));
}
template <int Aq, bool Z>
DI void mf_v_av(f32x4& C, const u32x4& Bv) {
  if constexpr (Z) asm volatile(MF "%0, a[%c2:%c3], %1, 0" : "=&v"(C) : "v"(Bv), "i"(Aq), "i"(Aq + 3));
  else asm volatile(MF "%0, a[%c2:%c3], %1, %0" : "+v"(C) : "v"(Bv), "i"(Aq), "i"(Aq + 3));
}
template <bool Z>
DI void mf_v_vv(f32x4& C, const u32x4& A, const u32x4& Bv) {
  if constexpr (Z) asm volatile(MF "%0, %1, %2, 0" : "=&v"(C) : "v"(A), "v"(Bv));
  else asm volatile(MF "%0, %1, %2, %0" : "+v"(C) : "v"(A), "v"(Bv));
}
template <int X, int Y>
DI unsigned pk_acc() {
  unsigned r, t0, t1;
  asm volatile("v_accvgpr_read_b32 %1, a[%c3]\n\tv_accvgpr_read_b32 %2, a[%c4]\n\tv_cvt_pk_bf16_f32 %0, %1, %2"
               : "=v"(r), "=&v"(t0), "=&v"(t1)
               : "i"(X), "i"(Y));
  return r;
}
template <int IMM>
DI void ld_w(u32x4& d, rsrc_t r, int voff, int soff) {
  asm volatile("buffer_load_dwordx4 %0, %1, %2, %3 offen offset:%c4" POL_W : "=v"(d) : "v"(voff), "s"(r), "s"(soff), "i"(IMM));
}
template <int D, int IMM>
DI void ld_k(rsrc_t r, int voff, int soff) {
  asm volatile("buffer_load_dwordx4 a[%c0:%c1], %2, %3, %4 offen offset:%c5" POL_KV ::"i"(D), "i"(D + 3), "v"(voff), "s"(r),
               "s"(soff), "i"(IMM));
}
template <int D>
DI void ld_pf(rsrc_t r, int voff, int soff) {
  asm volatile("buffer_load_dwordx4 a[%c0:%c1], %2, %3, %4 offen" POL_PF ::"i"(D), "i"(D + 3), "v"(voff), "s"(r), "s"(soff));
}
DI void ld_pfv(u32x4& d, rsrc_t r, int voff, int soff) {
  asm volatile("buffer_load_dwordx4 %0, %1, %2, %3 offen" POL_PF : "+v"(d) : "v"(voff), "s"(r), "s"(soff));
}
DI void dma_x(int m0v, rsrc_t r, int voff, int soff) {
  asm volatile("s_mov_b32 m0, %0\n\ts_nop 0\n\tbuffer_load_dwordx4 %1, %2, %3 offen" POL_X " lds" ::"s"(m0v), "v"(voff), "s"(r),
               "s"(soff)
               : "memory");
}
DI void dma_v(int m0v, rsrc_t r, int voff, int soff) {
  asm volatile("s_mov_b32 m0, %0\n\ts_nop 0\n\tbuffer_load_dwordx4 %1, %2, %3 offen" POL_KV " lds" ::"s"(m0v), "v"(voff), "s"(r),
               "s"(soff)
               : "memory");
}
template <int D, int IMM>
DI void lds_rd_a(int addr) {
  asm volatile("ds_read_b128 a[%c0:%c1], %2 offset:%c3" ::"i"(D), "i"(D + 3), "v"(addr), "i"(IMM) : "memory");
}
template <int D, int IMM>
DI void lds_rdtr_a(int addr) {
  asm volatile("ds_read_b64_tr_b16 a[%c0:%c1], %2 offset:%c3" ::"i"(D), "i"(D + 1), "v"(addr), "i"(IMM) : "memory");
}
template <int D, int IMM>
DI void lds_wr_a(int addr) {
  asm volatile("ds_write_b128 %2, a[%c0:%c1] offset:%c3" ::"i"(D), "i"(D + 3), "v"(addr), "i"(IMM) : "memory");
}
template <int IMM>
DI void lds_wr(int addr, const u32x4& d) {
  asm volatile("ds_write_b128 %0, %1 offset:%c2" ::"v"(addr), "v"(d), "i"(IMM) : "memory");
}
template <int N>
DI void wait_vm() {
  static_assert(N >= 0 && N < 64, "vmcnt");
  asm volatile("s_waitcnt vmcnt(%c0)" ::"i"(N) : "memory");
}
DI void wait_lgkm0() { asm volatile("s_waitcnt lgkmcnt(0)" ::: "memory"); }
DI void barrier() { asm volatile("s_barrier" ::: "memory"); }
template <int N>
DI void nop() {
  asm volatile("s_nop %c0" ::"i"(N));
}
DI float max3(float a, float b, float c) { return __builtin_elementwise_maximum(__builtin_elementwise_maximum(a, b), c); }
DI void xmax16(float& a) {
  float t;
  asm volatile("v_mov_b32 %1, %0\n\ts_nop 1\n\tv_permlane16_swap_b32 %0, %1\n\tv_max_f32 %0, %0, %1" : "+v"(a), "=&v"(t));
}
DI void xmax32s(float& a, float s) {
  float t;
  asm volatile("v_mov_b32 %1, %0\n\ts_nop 1\n\tv_permlane32_swap_b32 %0, %1\n\tv_max_f32 %0, %0, %1\n\tv_mul_f32 %0, %2, %0"
               : "+v"(a), "=&v"(t)
               : "s"(s));
}
template <int X, int Y>
DI void acc_rd2(float& a, float& b) {
  asm volatile("v_accvgpr_read_b32 %0, a[%c2]\n\tv_accvgpr_read_b32 %1, a[%c3]" : "=v"(a), "=v"(b) : "i"(X), "i"(Y));
}

#define STAMP(i)

struct Ptrs {
  const unsigned short* x[2];
  const unsigned short* k[2];
  const unsigned short* v[2];
  const unsigned short* wq[2];
  const unsigned short* wo[2];
};

constexpr int LDS_BYTES = 163840;
constexpr int XQ_BYTES = 24576, V1_BASE = 98304;

constexpr int A_B = 112, A_K = 160, A_V = 0;
constexpr bool tile_v(int f, int t) { return t < 2 || (t == 2 && f < 4); }
constexpr int tile_vi(int f, int t) { return t * 8 + f; }
constexpr int tile_a(int f, int t) { return 4 * (t >= 3 ? (t - 3) * 8 + f : 24 + (f - 4)); }

#define PF_WO 8
enum { OX = 1000, OK_ = 2000, OP = 3000, OV = 4000, OW = 5000 };
struct ASched {
  int op[16][24];
  bool bar[16];
};
#define SCHV 0
#define X6(q) OX + 6 * q, OX + 6 * q + 1, OX + 6 * q + 2, OX + 6 * q + 3, OX + 6 * q + 4, OX + 6 * q + 5
#define K4(t) OK_ + 4 * t, OK_ + 4 * t + 1, OK_ + 4 * t + 2, OK_ + 4 * t + 3
#define V4(j) OV + j, OV + j + 1, OV + j + 2, OV + j + 3
constexpr int A_BURST[6][12] = {{X6(1), OP + 2, OP + 3}, {X6(2), OP + 4, OP + 5}, {K4(0), K4(1)},
                                {X6(3), OP + 6, OP + 7}, {K4(2), K4(3)},          {K4(4), K4(5)}};
constexpr int A_TAIL[3][8] = {{V4(0), V4(4)}, {OV + 8, OV + 9, OV + 10}, {OV + 16, OV + 17, OV + 18}};
constexpr int A_KST[6] = {8, 9, 12, 13, 14, 15};
constexpr int B_V[] = {19, 20, 21, 22, 23, 11, 12, 13, 14, 15};
constexpr int B_NV = 10, B_PFT = 4;
constexpr ASched a_make() {
  ASched a{};
  for (int s = 0; s <= 12; s += 2)
    for (int i = 0; i < 16; ++i) a.op[s][(i >> 1) * 3 + (i & 1)] = OW + 8 * (s + 2) + i;
  for (int s = 1; s <= 11; s += 2) {
    a.bar[s] = true;
    for (int i = 0; i < (int)(sizeof(A_BURST[0]) / sizeof(int)); ++i) a.op[s][i] = A_BURST[s >> 1][i];
  }
  for (int s = 13; s < 16; ++s) {
    int n = 0;
    while (n < 8 && A_TAIL[s - 13][n]) ++n;
    for (int i = 0; i < n; ++i) a.op[s][3 * (i * 8 / n)] = A_TAIL[s - 13][i];
  }
  return a;
}
constexpr ASched A_S = a_make();
constexpr int A_NP = 3 + 6 + 16;
constexpr int a_seq(int op) {
  if (op >= OW && op < OW + 16) return 9 + op - OW;
  int c = A_NP;
  for (int s = 0; s < 16; ++s)
    for (int k = 0; k < 24; ++k)
      if (A_S.op[s][k]) {
        if (A_S.op[s][k] == op) return c;
        ++c;
      }
  return -1;
}
constexpr int a_issued(int s, int k) {
  int c = A_NP;
  for (int i = 0; i < 16; ++i)
    for (int j = 0; j < 24; ++j)
      if (A_S.op[i][j] && (i < s || (i == s && j < k))) ++c;
  return c;
}
constexpr int a_kt(int s) {
  for (int k = 0; k < 6; ++k)
    if (A_KST[k] == s) return k;
  return -1;
}
constexpr int imax(int a, int b) { return a > b ? a : b; }
constexpr int a_need(int s) {
  int n = 0;
  for (int i = 0; i < 8; ++i) n = imax(n, a_seq(OW + 8 * (s + 1) + i));
  if (s == 2 || s == 6 || s == 10)
    for (int i = 0; i < 6; ++i) n = imax(n, a_seq(OX + 6 * ((s + 2) / 4) + i));
  if (a_kt(s + 1) >= 0)
    for (int i = 0; i < 4; ++i) n = imax(n, a_seq(OK_ + 4 * a_kt(s + 1) + i));
  if (s == 14)
    for (int i = 2; i < 8; ++i) n = imax(n, a_seq(OP + i));
  return n;
}
constexpr int a_wait(int s) { return a_issued(s, 21) - a_need(s) - 1; }
constexpr bool a_check() {
  for (int i = 0; i < 24; ++i)
    if (a_seq(OK_ + i) < 0) return false;
  for (int i = 6; i < 24; ++i)
    if (a_seq(OX + i) < 0) return false;
  for (int i = 0; i < 128; ++i)
    if (a_seq(OW + i) < 0 || (i >= 32 && a_seq(OW + i) < a_issued(i / 8 - 3, 0))) return false;
  int nv = B_NV;
  for (int i = 0; i < 24; ++i)
    if (a_seq(OV + i) >= 0) {
      ++nv;
      if (i >= 11 && i < 16 && a_seq(OV + i) < a_issued(A_KST[5] + 1, 0)) return false;
      if (i >= 16 && a_seq(OV + i) < a_issued(7, 0)) return false;
    }
  for (int s = 0; s < 15; ++s)
    if (a_wait(s) < 0 || a_wait(s) > 63) return false;
  return nv == 24 && A_S.bar[3] && A_S.bar[7] && A_S.bar[11];
}
static_assert(a_check(), "phase A schedule");

DI unsigned aperm(const u32x4 (&W)[8], int f, int p) {
  return (f & 1) ? perm_hi(W[2 * p][f >> 1], W[2 * p + 1][f >> 1]) : perm_lo(W[2 * p][f >> 1], W[2 * p + 1][f >> 1]);
}

__global__ __launch_bounds__(256, 1) void k1_fused(Ptrs p, u32x4* part
) {
  extern __shared__ __attribute__((aligned(16))) char lds[];
  asm volatile("" ::: "a0", "a255");
  const int bid = blockIdx.x;
  const int xcd = bid & 7, idx = bid >> 3;
  const int br = xcd >> 2, hg = (xcd & 3) * 2 + (idx & 1), b = idx >> 1;
  const int tid = threadIdx.x, lane = tid & 63;
  const int w = __builtin_amdgcn_readfirstlane(tid >> 6);
  const int q = lane & 15, g = lane >> 4;
  const int cchunk = q;
  const int lb = (int)(unsigned)(size_t)(LDSP char*)lds;
  STAMP(0);

  const int vk = lane * 16;
  u32x4 pfd = u32x4{0, 0, 0, 0};
  {
    unsigned long long pc;
    int len;
    asm volatile("s_getpc_b64 %0\n.Lk1_pc:\n\ts_mov_b32 %1, .Lk2_end-.Lk1_pc" : "=s"(pc), "=s"(len));
    const rsrc_t rc = __builtin_amdgcn_make_buffer_rsrc((void*)pc, 0, len, RSRC_W3);
    nop<4>();
    ld_pfv(pfd, rc, vk, (idx * 4 + w) * 1024);
  }
  const unsigned short *px = p.x[br], *pk = p.k[br], *pv = p.v[br], *pq = p.wq[br], *po = p.wo[br];
  asm volatile("" : "+s"(px), "+s"(pk), "+s"(pv), "+s"(pq), "+s"(po), "+s"(part));
  const int head = hg * 4 + w;
  const size_t kvoff = ((size_t)b * NH + head) * SQ * DH;
  const rsrc_t rx = __builtin_amdgcn_make_buffer_rsrc((void*)(px + (size_t)b * SQ * DM), 0, SQ * DM * 2, RSRC_W3);
  const rsrc_t rk = __builtin_amdgcn_make_buffer_rsrc((void*)(pk + kvoff), 0, SQ * DH * 2, RSRC_W3);
  const rsrc_t rv = __builtin_amdgcn_make_buffer_rsrc((void*)(pv + kvoff), 0, SQ * DH * 2, RSRC_W3);
  const rsrc_t rq = __builtin_amdgcn_make_buffer_rsrc((void*)pq, 0, DM * NH * DH * 2, RSRC_W3);
  const rsrc_t ro = __builtin_amdgcn_make_buffer_rsrc((void*)(po + (size_t)hg * 512 * DM), 0, 512 * DM * 2, RSRC_W3);

  f32x4 accv[20];
  u32x4 W[4][8];
  u32x4 A[2];

  auto gmf = [&](auto FI, auto TI, auto BI, auto ZI, const u32x4& Af) {
    constexpr int f = FI, t = TI, bq = A_B + 24 * BI + 4 * t;
    constexpr bool z = ZI;
    if constexpr (tile_v(f, t)) mf_v_va<bq, z>(accv[tile_vi(f, t)], Af);
    else mf_a_va<tile_a(f, t), bq, z>(Af);
  };
  auto gpk = [&](auto PI, auto TI, auto RI) -> unsigned {
    constexpr int pp = PI, t = TI, r = RI;
    if constexpr (tile_v(2 * pp, t)) return pk_bf16(accv[tile_vi(2 * pp, t)][r], accv[tile_vi(2 * pp + 1, t)][r]);
    else return pk_acc<tile_a(2 * pp, t) + r, tile_a(2 * pp + 1, t) + r>();
  };

  const int vx = g * 1024 + ((q ^ g ^ (4 * w)) << 4);
  const int vwq = (8 * g) * 8192 + (hg * 512 + w * 128 + 8 * cchunk) * 2;
  auto xdma = [&](auto II) {
    constexpr int qt = II / 6, i = II % 6;
    dma_x(lb + qt * XQ_BYTES + (4 * i + w) * 1024, rx, vx, (4 * i + w) * 4096 + qt * 256);
  };
  const int vpf = ((lane >> 5) * 32 + 2 * b + (w >> 1)) * 8192 + hg * 1024 + (w & 1) * 512 + (lane & 31) * 16;
  nop<4>();
  STAMP(8);
  ld_pfv(pfd, rq, vpf, 0);
  ld_pfv(pfd, rq, vpf, 64 * 8192);
  sfor<6>([&](auto I) { xdma(I); });
  STAMP(9);
  wait_vm<7>();
  STAMP(10);
  sfor<16>([&](auto I) { ld_w<0>(W[I >> 3][I & 7], rq, vwq, (32 * (I >> 3) + (I & 7)) * 8192); });
  int xa[2][4];
#pragma unroll
  for (int h = 0; h < 2; ++h)
#pragma unroll
    for (int j = 0; j < 4; ++j) xa[h][j] = lb + h * 2 * XQ_BYTES + q * 256 + (((4 * j + g) ^ q) << 4);
  const int kst = lb + V1_BASE + w * 16384 + 16384 - 16 * 272;
  const int kwa = kst + g * 272 + q * 16, kra = kst + q * 272 + g * 64;
  const int vv0 = g * 256 + ((q ^ (((g >> 1) << 2) | (g & 1))) << 4);
  const int vv1 = vv0 ^ 128;
  auto vdma = [&](auto JI) {
    constexpr int j = JI;
    dma_v(j < 16 ? lb + V1_BASE + w * 16384 + j * 1024 : lb + w * 8192 + (j - 16) * 1024, rv, (j & 1) ? vv1 : vv0, j * 1024);
  };
  STAMP(11);
  SCHED;
  wait_vm<A_NP - 9>();
  barrier();
  STAMP(1);

  auto xrd = [&](auto SI, auto TI) {
    constexpr int s = SI, t = TI;
    lds_rd_a<A_B + 24 * (s & 1) + 4 * t, ((s >> 2) & 1) * XQ_BYTES + t * 4096>(xa[s >> 3][s & 3]);
  };
  sfor<6>([&](auto T) { xrd(IC<0>{}, T); });
  wait_vm<8>();
  SCHED;
#pragma unroll
  for (int pp = 0; pp < 4; ++pp) A[0][pp] = aperm(W[0], 0, pp);
  SCHED;
  wait_lgkm0();
  nop<0>();
  SCHED;
  sfor<128>([&](auto NI) {
    constexpr int n = NI, s = n >> 3, f = n & 7;
    constexpr int kt = a_kt(s), ktc = kt < 0 ? 0 : kt;
    if constexpr (f == 0 && A_S.bar[s]) {
      barrier();
      SCHED;
    }
    sfor<6>([&](auto TI) {
      constexpr int t = TI;
      gmf(IC<f>{}, TI, IC<(s & 1)>{}, std::bool_constant<s == 0>{}, A[n & 1]);
      SCHED;
      if constexpr (t == 0 && f == 7 && s < 15) wait_vm<a_wait(s)>();
      constexpr int op = (t & 1) ? 0 : A_S.op[s][3 * f + t / 2], j = op % 1000;
      if constexpr (op >= OW) ld_w<0>(W[(j >> 3) & 3][j & 7], rq, vwq, (32 * (j >> 3) + (j & 7)) * 8192);
      else if constexpr (op >= OV) vdma(IC<j>{});
      else if constexpr (op >= OP) ld_pfv(pfd, rq, vpf, j * 64 * 8192);
      else if constexpr (op >= OK_) ld_k<A_K + 4 * j, (j & 3) * 1024>(rk, vk, (j >> 2) * 4096);
      else if constexpr (op >= OX) xdma(IC<j>{});
      if constexpr (t == 5) {
        if constexpr (s + 1 < 16 && f < 6) xrd(IC<s + 1>{}, IC<f>{});
        else if constexpr (f == 7) wait_lgkm0();
        else if constexpr (kt >= 0 && f == 6) lds_rd_a<A_K + 16 * ktc + 12, 48>(kra);
        else nop<0>();
      }
      if constexpr (n + 1 < 128 && t >= 1 && t <= 4) A[(n + 1) & 1][t - 1] = aperm(W[((n + 1) >> 3) & 3], (n + 1) & 7, t - 1);
      if constexpr (t == 3 && kt >= 0) {
        if constexpr (f < 4) lds_wr_a<A_K + 16 * ktc + 4 * f, f * 4 * 272>(kwa);
        else if constexpr (f < 7) lds_rd_a<A_K + 16 * ktc + 4 * (f - 4), (f - 4) * 16>(kra);
      }
      SCHED;
    });
  });
  asm volatile("" ::"v"(pfd));
  STAMP(2);

  u32x4 Qf[2][4];
  f32x4 sacc[2][6];
  u32x4 Pf[6][3];
  f32x4 oacc[2][8];
  f32x4 lacc[2];
  float mA = 0.f, mB = 0.f, li = 0.f;
  float ev[24];
  const float c = 0.08838834764831845f * 1.4426950408889634f;

  float ta[2][2];
  auto qp = [&](auto TI, auto JI) {
    constexpr int t = TI, j = JI, pp = (j & 15) >> 2, s = j & 3, i = j > 0 ? j - 1 : 0;
    if constexpr (j < 16) {
      if constexpr (tile_v(2 * pp, t)) Qf[t & 1][s][pp] = gpk(IC<pp>{}, TI, IC<s>{});
      else acc_rd2<tile_a(2 * pp, t) + s, tile_a(2 * pp + 1, t) + s>(ta[j & 1][0], ta[j & 1][1]);
    }
    if constexpr (j > 0 && !tile_v(2 * (i >> 2), t)) Qf[t & 1][i & 3][i >> 2] = pk_bf16(ta[i & 1][0], ta[i & 1][1]);
  };
  auto sm = [&](auto TI, auto JI) {
    constexpr int t = TI, j = JI;
    const f32x4(&S)[6] = sacc[t & 1];
    auto v = [&](int i) -> float { return S[i >> 2][i & 3]; };
    if constexpr (j == 0) {
      mA = max3(v(0), v(1), v(2));
      mB = max3(v(3), v(4), v(5));
    } else if constexpr (j < 5) {
      mA = max3(mA, v(4 * j + 2), v(4 * j + 3));
      mB = max3(mB, v(4 * j + 4), v(4 * j + 5));
    } else if constexpr (j == 5) {
      mA = __builtin_elementwise_maximum(max3(mA, v(22), v(23)), mB);
      xmax16(mA);
    } else if constexpr (j == 6) xmax32s(mA, -c);
    else if constexpr (j < 7 + 24) {
      constexpr int i = j - 7;
      ev[i] = __builtin_amdgcn_exp2f(__builtin_fmaf(v(i), c, mA));
    } else {
      constexpr int i = j - 31;
      Pf[t][i >> 2][i & 3] = pk_bf16(ev[2 * i], ev[2 * i + 1]);
    }
  };
  constexpr int SM_N = 43;
  const int vo = lane * 16;
  const int oa1 = lb + V1_BASE + w * 16384 + vo, oa2 = lb + w * 8192 + vo;
  u32x4 ot;
  auto oe = [&](auto TI, auto JI) {
    constexpr int t = TI, j = JI;
    if constexpr (j == 0) {
      asm volatile("" : "+v"(lacc[t & 1]));
      li = __builtin_amdgcn_rcpf(lacc[t & 1][0]);
    }
    else {
      constexpr int i = j - 1, sp = i / 5, e = i % 5;
      if constexpr (e < 4) {
        const f32x4& o = oacc[t & 1][2 * sp + (e >> 1)];
        ot[e] = pk_bf16(o[2 * (e & 1)] * li, __builtin_fmaf(o[2 * (e & 1) + 1], li, 0.f));
      } else {
        constexpr int fi = sp * 6 + t;
        if constexpr (fi < 16) lds_wr<fi * 1024>(oa1, ot);
        else lds_wr<(fi - 16) * 1024>(oa2, ot);
      }
    }
  };
  constexpr int OE_N = 21;
  const int vtr = (4 * g + ((lane >> 2) & 3)) * 256 +
                  (((lane & 3) ^ (((g & 1) << 3) | (((lane >> 3) & 1) << 2) | ((lane >> 2) & 1))) << 4) + 8 * (lane & 1);
  const int vt1 = lb + V1_BASE + w * 16384 + vtr, vt2 = lb + w * 8192 + vtr;
  auto vrd = [&](auto JI) {
    constexpr int j = JI, f = j / 6, t3 = (j % 6) >> 1, hh = j & 1, sp = f >> 1, h = f & 1;
    constexpr int x = (sp << 6) ^ (h << 3), d = A_V + 4 * (t3 * 8 + f) + 2 * hh;
    if constexpr (t3 < 2) lds_rdtr_a<d, (32 * t3 + 16 * hh) * 256>(vt1 ^ x);
    else lds_rdtr_a<d, 16 * hh * 256>(vt2 ^ x);
  };
  const int vwoA = (8 * g + 4 * (g & 1)) * 1024 + (w * 128 + 8 * cchunk) * 2;
  const int vwoB = (8 * g - 4 * (g & 1) + 4) * 1024 + (w * 128 + 8 * cchunk) * 2;
  auto wold = [&](auto SI, auto II) {
    constexpr int S = SI, i = II;
    const int so = (((w + (S >> 2)) & 3) * 128 + 32 * (S & 3)) * 1024;
    ld_w<(i & 3) * 1024>(W[S & 3][i], ro, i < 4 ? vwoA : vwoB, so);
  };

  sfor<16>([&](auto J) { qp(IC<0>{}, J); });
  SCHED;
  nop<1>();
  sfor<6>([&](auto TI) {
    constexpr int t = TI;
    constexpr int NQ = t < 5 ? 17 : 0, NS = t > 0 ? SM_N : 0, NV = t == 5 ? 48 : 0;
    constexpr int NF = NQ + NS + NV;
    if constexpr (t == 5) wait_vm<PF_WO>();
    sfor<24>([&](auto MI) {
      constexpr int m = MI, s = m / 6, kt = m % 6;
      mf_v_av<A_K + 4 * (kt * 4 + s), s == 0>(sacc[t & 1][kt], Qf[t & 1][s]);
      SCHED;
      if constexpr (m % 8 == 2 && t < 4 && 3 * t + m / 8 < B_NV) vdma(IC<B_V[(3 * t + m / 8) < B_NV ? 3 * t + m / 8 : 0]>{});
      if constexpr (t == B_PFT && m % 3 == 0 && m / 3 < PF_WO) ld_pf<A_B + 4 * (m / 3)>(ro, vk, (32 * b + 8 * w + m / 3) * 1024);
      constexpr int lo = m * NF / 22 < NF ? m * NF / 22 : NF, hi = (m + 1) * NF / 22 < NF ? (m + 1) * NF / 22 : NF;
      sfor<hi - lo>([&](auto KI) {
        constexpr int k = lo + KI;
        if constexpr (k < NS) sm(IC<t - 1>{}, IC<k>{});
        else if constexpr (k < NS + NQ) qp(IC<t + 1>{}, IC<k - NS>{});
        else vrd(IC<k - NS - NQ>{});
      });
      SCHED;
    });
  });
  wait_lgkm0();
  STAMP(3);
  u32x4 ones = u32x4{0x3F803F80u, 0x3F803F80u, 0x3F803F80u, 0x3F803F80u};
  asm volatile("" : "+v"(ones));
  nop<1>();
  sfor<6>([&](auto TI) {
    constexpr int t = TI;
    constexpr int NS = t == 0 ? SM_N : 0, NO = t > 0 ? OE_N : 0, NW = t >= 3 ? 8 : 0;
    constexpr int NF = NS + NO + NW;
    sfor<27>([&](auto MI) {
      constexpr int m = MI, t3 = m / 9, f = m % 9;
      if constexpr (f > 0) mf_v_av<A_V + 4 * (t3 * 8 + f - 1), t3 == 0>(oacc[t & 1][f - 1], Pf[t][t3]);
      else mf_v_vv<t3 == 0>(lacc[t & 1], ones, Pf[t][t3]);
      SCHED;
      constexpr int lo = m * NF / 27, hi = (m + 1) * NF / 27;
      sfor<hi - lo>([&](auto KI) {
        constexpr int k = lo + KI;
        if constexpr (k < NS) sm(IC<5>{}, IC<k>{});
        else if constexpr (k < NS + NO) oe(IC<t - 1>{}, IC<k - NS>{});
        else wold(IC<t - 3>{}, IC<k - NS - NO>{});
      });
      SCHED;
    });
  });
  sfor<OE_N>([&](auto J) { oe(IC<5>{}, J); });
  SCHED;
  STAMP(4);

  int ob[4][2];
#pragma unroll
  for (int h = 0; h < 4; ++h) {
    const int wp = (w + h) & 3;
    ob[h][0] = lb + V1_BASE + wp * 16384 + vo;
    ob[h][1] = lb + wp * 8192 + vo;
  }
  auto ordx = [&](auto SI, auto TI, auto DI_) {
    constexpr int S = SI, t = TI, d = DI_, fi = (S & 3) * 6 + t;
    if constexpr (fi < 16) lds_rd_a<d, fi * 1024>(ob[S >> 2][0]);
    else lds_rd_a<d, (fi - 16) * 1024>(ob[S >> 2][1]);
  };
  auto ord = [&](auto SI, auto TI) { ordx(SI, TI, IC<A_B + 24 * (SI & 1) + 4 * TI>{}); };
  constexpr int TG[3][2] = {{4, 5}, {2, 3}, {0, 1}};
  u32x4 AT[3][8];
  auto tperm = [&](auto KI) {
    constexpr int k = KI, m = k >> 1, d = 2 * (k & 1);
    AT[m >> 3][m & 7][d] = aperm(W[(13 + (m >> 3)) & 3], m & 7, d);
    AT[m >> 3][m & 7][d + 1] = aperm(W[(13 + (m >> 3)) & 3], m & 7, d + 1);
  };
  auto tord = [&](auto GI, auto JI) {
    constexpr int G = GI, jj = JI;
    ordx(IC<13 + jj / 2>{}, IC<TG[G][jj & 1]>{}, IC<A_B + 24 * ((G & 1) ^ 1) + 4 * jj>{});
  };
  wait_vm<16>();
  sfor<6>([&](auto T) { ord(IC<0>{}, T); });
  SCHED;
#pragma unroll
  for (int pp = 0; pp < 4; ++pp) A[0][pp] = aperm(W[0], 0, pp);
  SCHED;
  wait_lgkm0();
  nop<0>();
  SCHED;
  sfor<104>([&](auto NI) {
    constexpr int n = NI, s = n >> 3, f = n & 7;
    if constexpr (n == 24) {
      barrier();
      SCHED;
    }
    sfor<6>([&](auto TI) {
      constexpr int t = TI;
      gmf(IC<f>{}, TI, IC<(s & 1)>{}, std::bool_constant<s == 0>{}, A[n & 1]);
      SCHED;
      if constexpr ((t == 0 || t == 2) && f < 4) wold(IC<s + 3>{}, IC<(2 * f + (t >> 1)) & 7>{});
      if constexpr (t == 0 && f == 7) wait_vm<16>();
      if constexpr (t == 5) {
        if constexpr (f < 6) {
          if constexpr (s < 12) ord(IC<s + 1>{}, IC<f>{});
          else tord(IC<0>{}, IC<f>{});
        } else if constexpr (f == 7) wait_lgkm0();
        else nop<0>();
      }
      if constexpr (n < 103) {
        if constexpr (t >= 1 && t <= 4) A[(n + 1) & 1][t - 1] = aperm(W[((n + 1) >> 3) & 3], (n + 1) & 7, t - 1);
      } else if constexpr (t & 1) tperm(IC<t / 2>{});
      SCHED;
    });
  });
  const rsrc_t rp = __builtin_amdgcn_make_buffer_rsrc((void*)(part + (size_t)(((br * NBATCH + b) * 8 + hg) * 4 + w) * 24 * 64), 0,
                                                      24 * 1024, RSRC_W3);
  u32x4 so[2];
  auto stp = [&](auto GI, auto KI) {
    constexpr int G = GI, pc = KI / 6, jj = KI % 6, t = TG[G][pc >> 2], r = pc & 3, i = jj > 0 ? jj - 1 : 0;
    if constexpr (jj < 4) {
      if constexpr (tile_v(2 * jj, t)) so[pc & 1][jj] = gpk(IC<jj>{}, IC<t>{}, IC<r>{});
      else acc_rd2<tile_a(2 * jj, t) + r, tile_a(2 * jj + 1, t) + r>(ta[jj & 1][0], ta[jj & 1][1]);
    }
    if constexpr (jj > 0 && jj < 5 && !tile_v(2 * i, t)) so[pc & 1][i] = pk_bf16(ta[i & 1][0], ta[i & 1][1]);
    if constexpr (jj == 5) {
      __builtin_amdgcn_raw_buffer_store_b128(so[pc & 1], rp, vo + r * 1024, t * 4096, AUX_PST);
      SCHED;
      nop<1>();
    }
  };
  sfor<3>([&](auto GI) {
    constexpr int G = GI;
    sfor<48>([&](auto UI) {
      constexpr int u = UI, sp = u >> 4, f = (u >> 1) & 7, tt = u & 1, t = TG[G][tt];
      constexpr int bq = A_B + 24 * ((G & 1) ^ 1) + 4 * (2 * sp + tt);
      if constexpr (tile_v(f, t)) mf_v_va<bq, false>(accv[tile_vi(f, t)], AT[sp][f]);
      else mf_a_va<tile_a(f, t), bq, false>(AT[sp][f]);
      SCHED;
      if constexpr (G == 0 && u + 3 < 48) {
        if constexpr (u + 3 == 16) wait_vm<8>();
        if constexpr (u + 3 == 32) wait_vm<0>();
        tperm(IC<u + 3>{});
      }
      if constexpr (G == 0 && u == 45) nop<0>();
      if constexpr (G < 2 && u % 6 == 4 && u / 6 < 6) tord(IC<(G < 2 ? G + 1 : 0)>{}, IC<u / 6>{});
      if constexpr (G < 2 && u == 47) wait_lgkm0();
      if constexpr (G > 0 && u >= 2) {
        constexpr int lo = (u - 2) * 48 / 46, hi = (u - 1) * 48 / 46;
        sfor<hi - lo>([&](auto KI) { stp(IC<(G > 0 ? G - 1 : 0)>{}, IC<lo + KI>{}); });
      }
      SCHED;
    });
  });
  STAMP(5);
  nop<7>();
  sfor<48>([&](auto KI) {
    stp(IC<2>{}, KI);
    SCHED;
  });
  STAMP(6);
}

__global__ __launch_bounds__(256) void k2_reduce(const u32x4* __restrict__ part, unsigned short* __restrict__ out) {
  const int i = blockIdx.x * 256 + threadIdx.x;
  const int bb = i / 6144, e = i % 6144;
  const rsrc_t rp = __builtin_amdgcn_make_buffer_rsrc((void*)(part + (size_t)bb * 8 * 6144), 0, 8 * 6144 * 16, RSRC_W3);
  u32x4 v[8];
#pragma unroll
  for (int h = 0; h < 8; ++h) v[h] = __builtin_amdgcn_raw_buffer_load_b128(rp, e * 16, h * 6144 * 16, AUX_PLD);
  float s[8];
#pragma unroll
  for (int j = 0; j < 8; ++j) s[j] = 0.f;
#pragma unroll
  for (int h = 0; h < 8; ++h)
#pragma unroll
    for (int j = 0; j < 4; ++j) {
      s[2 * j] += __builtin_bit_cast(float, v[h][j] << 16);
      s[2 * j + 1] += __builtin_bit_cast(float, v[h][j] & 0xffff0000u);
    }
  u32x4 o;
#pragma unroll
  for (int j = 0; j < 4; ++j) o[j] = pk_bf16(s[2 * j], s[2 * j + 1]);
  const int lane = e & 63, pc = e >> 6, r = pc & 3, t = (pc >> 2) % 6, w = pc / 24;
  const int q = lane & 15, g = lane >> 4;
  const int br = bb >> 4, b = bb & 15;
  *(u32x4*)(out + ((size_t)b * SQ + 16 * t + q) * 1024 + br * 512 + 128 * w + 32 * g + 8 * r) = o;
  asm volatile(".Lk2_end:");
}

extern "C" void kernel_launch(void* const* d_in, const int* in_sizes, int n_in, void* d_out, int out_size, void* d_ws,
                              size_t ws_size, hipStream_t stream) {
  Ptrs p;
  for (int br = 0; br < 2; ++br) {
    p.x[br] = (const unsigned short*)d_in[5 * br + 0];
    p.k[br] = (const unsigned short*)d_in[5 * br + 1];
    p.v[br] = (const unsigned short*)d_in[5 * br + 2];
    p.wq[br] = (const unsigned short*)d_in[5 * br + 3];
    p.wo[br] = (const unsigned short*)d_in[5 * br + 4];
  }
  u32x4* part = (u32x4*)d_ws;
  (void)hipFuncSetAttribute((const void*)k1_fused, hipFuncAttributeMaxDynamicSharedMemorySize, LDS_BYTES);
  k1_fused<<<256, 256, LDS_BYTES, stream>>>(p, part
  );
  k2_reduce<<<2 * 16 * 96 * 64 / 256, 256, 0, stream>>>(part, (unsigned short*)d_out);
}
```

```cpp
#include <hip/hip_runtime.h>
#include <hip/hip_bf16.h>
#include <utility>

typedef unsigned u32x2 __attribute__((ext_vector_type(2)));
typedef unsigned u32x4 __attribute__((ext_vector_type(4)));
typedef float f32x4 __attribute__((ext_vector_type(4)));
#define LDSP __attribute__((address_space(3)))
#define DI __device__ __forceinline__
typedef __amdgpu_buffer_rsrc_t rsrc_t;

constexpr int SQ = 96, DM = 512, NH = 32, DH = 128, NBATCH = 16;
constexpr int RSRC_W3 = 0x00020000;
#define POL_KV " nt"
#define POL_W ""
#define POL_PF ""
#define POL_X ""
#define AUX_PST 16
#define AUX_PLD 0

template <class F, int... I>
DI void sfor_impl(F&& f, std::integer_sequence<int, I...>) {
  (f(std::integral_constant<int, I>{}), ...);
}
template <int N, class F>
DI void sfor(F&& f) {
  sfor_impl(f, std::make_integer_sequence<int, N>{});
}
template <int N>
using IC = std::integral_constant<int, N>;

typedef float f32x2 __attribute__((ext_vector_type(2)));
typedef __bf16 bf16x2 __attribute__((ext_vector_type(2)));
DI unsigned pk_bf16(float lo, float hi) {
  return __builtin_bit_cast(unsigned, __builtin_convertvector(f32x2{lo, hi}, bf16x2));
}
DI unsigned perm_lo(unsigned e, unsigned o) { return __builtin_amdgcn_perm(o, e, 0x05040100u); }
DI unsigned perm_hi(unsigned e, unsigned o) { return __builtin_amdgcn_perm(o, e, 0x07060302u); }
#define SCHED __builtin_amdgcn_sched_barrier(0)

#define MF "v_mfma_f32_16x16x32_bf16 "
template <int D, int Bq, bool Z>
DI void mf_a_va(const u32x4& A) {
  if constexpr (Z) asm volatile(MF "a[%c0:%c1], %2, a[%c3:%c4], 0" ::"i"(D), "i"(D + 3), "v"(A), "i"(Bq), "i"(Bq + 3));
  else asm volatile(MF "a[%c0:%c1], %2, a[%c3:%c4], a[%c0:%c1]" ::"i"(D), "i"(D + 3), "v"(A), "i"(Bq), "i"(Bq + 3));
}
template <int Bq, bool Z>
DI void mf_v_va(f32x4& C, const u32x4& A) {
  if constexpr (Z) asm volatile(MF "%0, %1, a[%c2:%c3], 0" : "=&v"(C) : "v"(A), "i"(Bq), "i"(Bq + 3));
  else asm volatile(MF "%0, %1, a[%c2:%c3], %0" : "+v"(C) : "v"(A), "i"(Bq), "i"(Bq + 3));
}
template <int Aq, bool Z>
DI void mf_v_av(f32x4& C, const u32x4& Bv) {
  if constexpr (Z) asm volatile(MF "%0, a[%c2:%c3], %1, 0" : "=&v"(C) : "v"(Bv), "i"(Aq), "i"(Aq + 3));
  else asm volatile(MF "%0, a[%c2:%c3], %1, %0" : "+v"(C) : "v"(Bv), "i"(Aq), "i"(Aq + 3));
}
template <bool Z>
DI void mf_v_vv(f32x4& C, const u32x4& A, const u32x4& Bv) {
  if constexpr (Z) asm volatile(MF "%0, %1, %2, 0" : "=&v"(C) : "v"(A), "v"(Bv));
  else asm volatile(MF "%0, %1, %2, %0" : "+v"(C) : "v"(A), "v"(Bv));
}
template <int X, int Y>
DI unsigned pk_acc() {
  unsigned r, t0, t1;
  asm volatile("v_accvgpr_read_b32 %1, a[%c3]\n\tv_accvgpr_read_b32 %2, a[%c4]\n\tv_cvt_pk_bf16_f32 %0, %1, %2"
               : "=v"(r), "=&v"(t0), "=&v"(t1)
               : "i"(X), "i"(Y));
  return r;
}
template <int IMM>
DI void ld_w(u32x4& d, rsrc_t r, int voff, int soff) {
  asm volatile("buffer_load_dwordx4 %0, %1, %2, %3 offen offset:%c4" POL_W : "=v"(d) : "v"(voff), "s"(r), "s"(soff), "i"(IMM));
}
template <int D, int IMM>
DI void ld_k(rsrc_t r, int voff, int soff) {
  asm volatile("buffer_load_dwordx4 a[%c0:%c1], %2, %3, %4 offen offset:%c5" POL_KV ::"i"(D), "i"(D + 3), "v"(voff), "s"(r),
               "s"(soff), "i"(IMM));
}
template <int D>
DI void ld_pf(rsrc_t r, int voff, int soff) {
  asm volatile("buffer_load_dwordx4 a[%c0:%c1], %2, %3, %4 offen" POL_PF ::"i"(D), "i"(D + 3), "v"(voff), "s"(r), "s"(soff));
}
DI void ld_pfv(u32x4& d, rsrc_t r, int voff, int soff) {
  asm volatile("buffer_load_dwordx4 %0, %1, %2, %3 offen" POL_PF : "+v"(d) : "v"(voff), "s"(r), "s"(soff));
}
DI void dma_x(int m0v, rsrc_t r, int voff, int soff) {
  asm volatile("s_mov_b32 m0, %0\n\ts_nop 0\n\tbuffer_load_dwordx4 %1, %2, %3 offen" POL_X " lds" ::"s"(m0v), "v"(voff), "s"(r),
               "s"(soff)
               : "memory");
}
DI void dma_v(int m0v, rsrc_t r, int voff, int soff) {
  asm volatile("s_mov_b32 m0, %0\n\ts_nop 0\n\tbuffer_load_dwordx4 %1, %2, %3 offen" POL_KV " lds" ::"s"(m0v), "v"(voff), "s"(r),
               "s"(soff)
               : "memory");
}
template <int D, int IMM>
DI void lds_rd_a(int addr) {
  asm volatile("ds_read_b128 a[%c0:%c1], %2 offset:%c3" ::"i"(D), "i"(D + 3), "v"(addr), "i"(IMM) : "memory");
}
template <int D, int IMM>
DI void lds_rdtr_a(int addr) {
  asm volatile("ds_read_b64_tr_b16 a[%c0:%c1], %2 offset:%c3" ::"i"(D), "i"(D + 1), "v"(addr), "i"(IMM) : "memory");
}
template <int D, int IMM>
DI void lds_wr_a(int addr) {
  asm volatile("ds_write_b128 %2, a[%c0:%c1] offset:%c3" ::"i"(D), "i"(D + 3), "v"(addr), "i"(IMM) : "memory");
}
template <int IMM>
DI void lds_wr(int addr, const u32x4& d) {
  asm volatile("ds_write_b128 %0, %1 offset:%c2" ::"v"(addr), "v"(d), "i"(IMM) : "memory");
}
template <int N>
DI void wait_vm() {
  static_assert(N >= 0 && N < 64, "vmcnt");
  asm volatile("s_waitcnt vmcnt(%c0)" ::"i"(N) : "memory");
}
DI void wait_lgkm0() { asm volatile("s_waitcnt lgkmcnt(0)" ::: "memory"); }
DI void barrier() { asm volatile("s_barrier" ::: "memory"); }
template <int N>
DI void nop() {
  asm volatile("s_nop %c0" ::"i"(N));
}
DI float max3(float a, float b, float c) { return __builtin_elementwise_maximum(__builtin_elementwise_maximum(a, b), c); }
DI void xmax16(float& a) {
  float t;
  asm volatile("v_mov_b32 %1, %0\n\ts_nop 1\n\tv_permlane16_swap_b32 %0, %1\n\tv_max_f32 %0, %0, %1" : "+v"(a), "=&v"(t));
}
DI void xmax32s(float& a, float s) {
  float t;
  asm volatile("v_mov_b32 %1, %0\n\ts_nop 1\n\tv_permlane32_swap_b32 %0, %1\n\tv_max_f32 %0, %0, %1\n\tv_mul_f32 %0, %2, %0"
               : "+v"(a), "=&v"(t)
               : "s"(s));
}
template <int X, int Y>
DI void acc_rd2(float& a, float& b) {
  asm volatile("v_accvgpr_read_b32 %0, a[%c2]\n\tv_accvgpr_read_b32 %1, a[%c3]" : "=v"(a), "=v"(b) : "i"(X), "i"(Y));
}

#define STAMP(i)

#define HALFX(n) (n)
struct Ptrs {
  const unsigned short* x[2];
  const unsigned short* k[2];
  const unsigned short* v[2];
  const unsigned short* wq[2];
  const unsigned short* wo[2];
};

constexpr int LDS_BYTES = 163840;
constexpr int XQ_BYTES = 24576, V1_BASE = 98304;

constexpr int A_B = 112, A_K = 160, A_V = 0;
constexpr bool tile_v(int f, int t) { return t < 2 || (t == 2 && f < 4); }
constexpr int tile_vi(int f, int t) { return t * 8 + f; }
constexpr int tile_a(int f, int t) { return 4 * (t >= 3 ? (t - 3) * 8 + f : 24 + (f - 4)); }

#define PF_WO 8
enum { OX = 1000, OK_ = 2000, OP = 3000, OV = 4000, OW = 5000 };
struct ASched {
  int op[16][24];
  bool bar[16];
};
#define SCHV 0
#define STAG 16
#define X6(q) OX + 6 * q, OX + 6 * q + 1, OX + 6 * q + 2, OX + 6 * q + 3, OX + 6 * q + 4, OX + 6 * q + 5
#define K4(t) OK_ + 4 * t, OK_ + 4 * t + 1, OK_ + 4 * t + 2, OK_ + 4 * t + 3
#define V4(j) OV + j, OV + j + 1, OV + j + 2, OV + j + 3
constexpr int A_BURST[6][12] = {{X6(1), OP + 2, OP + 3}, {X6(2), OP + 4, OP + 5}, {K4(0), K4(1)},
                                {X6(3), OP + 6, OP + 7}, {K4(2), K4(3)},          {K4(4), K4(5)}};
constexpr int A_TAIL[3][8] = {{V4(0), V4(4)}, {OV + 8, OV + 9, OV + 10}, {OV + 16, OV + 17, OV + 18}};
constexpr int A_KST[6] = {8, 9, 12, 13, 14, 15};
constexpr int B_V[] = {19, 20, 21, 22, 23, 11, 12, 13, 14, 15};
constexpr int B_NV = 10, B_PFT = 4;
constexpr ASched a_make() {
  ASched a{};
  for (int s = 0; s <= 12; s += 2)
    for (int i = 0; i < 16; ++i) a.op[s][(i >> 1) * 3 + (i & 1)] = OW + 8 * (s + 2) + i;
  for (int s = 1; s <= 11; s += 2) {
    a.bar[s] = true;
    for (int i = 0; i < (int)(sizeof(A_BURST[0]) / sizeof(int)); ++i) a.op[s][i] = A_BURST[s >> 1][i];
  }
  for (int s = 13; s < 16; ++s) {
    int n = 0;
    while (n < 8 && A_TAIL[s - 13][n]) ++n;
    for (int i = 0; i < n; ++i) a.op[s][3 * (i * 8 / n)] = A_TAIL[s - 13][i];
  }
  return a;
}
constexpr ASched A_S = a_make();
constexpr int A_NP = 3 + 6 + 16;
constexpr int a_seq(int op) {
  if (op >= OW && op < OW + 16) return 9 + op - OW;
  int c = A_NP;
  for (int s = 0; s < 16; ++s)
    for (int k = 0; k < 24; ++k)
      if (A_S.op[s][k]) {
        if (A_S.op[s][k] == op) return c;
        ++c;
      }
  return -1;
}
constexpr int a_issued(int s, int k) {
  int c = A_NP;
  for (int i = 0; i < 16; ++i)
    for (int j = 0; j < 24; ++j)
      if (A_S.op[i][j] && (i < s || (i == s && j < k))) ++c;
  return c;
}
constexpr int a_kt(int s) {
  for (int k = 0; k < 6; ++k)
    if (A_KST[k] == s) return k;
  return -1;
}
constexpr int imax(int a, int b) { return a > b ? a : b; }
constexpr int a_need(int s) {
  int n = 0;
  for (int i = 0; i < 8; ++i) n = imax(n, a_seq(OW + 8 * (s + 1) + i));
  if (s == 2 || s == 6 || s == 10)
    for (int i = 0; i < 6; ++i) n = imax(n, a_seq(OX + 6 * ((s + 2) / 4) + i));
  if (a_kt(s + 1) >= 0)
    for (int i = 0; i < 4; ++i) n = imax(n, a_seq(OK_ + 4 * a_kt(s + 1) + i));
  if (s == 14)
    for (int i = 2; i < 8; ++i) n = imax(n, a_seq(OP + i));
  return n;
}
constexpr int a_wait(int s) { return a_issued(s, 21) - a_need(s) - 1; }
constexpr int a_wait_w(int ws, int s, int k) {
  int n = 0;
  for (int i = 0; i < 8; ++i) n = imax(n, a_seq(OW + 8 * ws + i));
  return a_issued(s, k) - n - 1;
}
constexpr bool a_check() {
  for (int i = 0; i < 24; ++i)
    if (a_seq(OK_ + i) < 0) return false;
  for (int i = 6; i < 24; ++i)
    if (a_seq(OX + i) < 0) return false;
  for (int i = 0; i < 128; ++i)
    if (a_seq(OW + i) < 0 || (i >= 32 && a_seq(OW + i) < a_issued(i / 8 - 3, 0))) return false;
  int nv = B_NV;
  for (int i = 0; i < 24; ++i)
    if (a_seq(OV + i) >= 0) {
      ++nv;
      if (i >= 11 && i < 16 && a_seq(OV + i) < a_issued(A_KST[5] + 1, 0)) return false;
      if (i >= 16 && a_seq(OV + i) < a_issued(7, 0)) return false;
    }
  for (int s = 0; s < 15; ++s)
    if (a_wait(s) < 0 || a_wait(s) > 63) return false;
  return nv == 24 && A_S.bar[3] && A_S.bar[7] && A_S.bar[11];
}
static_assert(a_check(), "phase A schedule");

DI unsigned aperm(const u32x4 (&W)[8], int f, int p) {
  return (f & 1) ? perm_hi(W[2 * p][f >> 1], W[2 * p + 1][f >> 1]) : perm_lo(W[2 * p][f >> 1], W[2 * p + 1][f >> 1]);
}

__global__ __launch_bounds__(256, 1) void k1_fused(Ptrs p, u32x4* part
) {
  extern __shared__ __attribute__((aligned(16))) char lds[];
  asm volatile("" ::: "a0", "a255");
  const int bid = blockIdx.x;
  const int xcd = bid & 7, idx = bid >> 3;
  const int br = xcd >> 2, hg = (xcd & 3) * 2 + (idx & 1), b = idx >> 1;
  const int tid = threadIdx.x, lane = tid & 63;
  const int w = __builtin_amdgcn_readfirstlane(tid >> 6);
  const int q = lane & 15, g = lane >> 4;
  const int cchunk = q;
  const int lb = (int)(unsigned)(size_t)(LDSP char*)lds;
  STAMP(0);

  const int vk = lane * 16;
  u32x4 pfd = u32x4{0, 0, 0, 0};
  {
    unsigned long long pc;
    int len;
    asm volatile("s_getpc_b64 %0\n.Lk1_pc:\n\ts_mov_b32 %1, .Lk2_end-.Lk1_pc" : "=s"(pc), "=s"(len));
    const rsrc_t rc = __builtin_amdgcn_make_buffer_rsrc((void*)pc, 0, len, RSRC_W3);
    nop<4>();
    ld_pfv(pfd, rc, vk, (idx * 4 + w) * 1024);
  }
  const unsigned short *px = p.x[br], *pk = p.k[br], *pv = p.v[br], *pq = p.wq[br], *po = p.wo[br];
  asm volatile("" : "+s"(px), "+s"(pk), "+s"(pv), "+s"(pq), "+s"(po), "+s"(part));
  const int head = hg * 4 + w;
  const size_t kvoff = ((size_t)b * NH + head) * SQ * DH;
  const rsrc_t rx = __builtin_amdgcn_make_buffer_rsrc((void*)(px + (size_t)b * SQ * DM), 0, HALFX(SQ * DM * 2), RSRC_W3);
  const rsrc_t rk = __builtin_amdgcn_make_buffer_rsrc((void*)(pk + kvoff), 0, HALFX(SQ * DH * 2), RSRC_W3);
  const rsrc_t rv = __builtin_amdgcn_make_buffer_rsrc((void*)(pv + kvoff), 0, HALFX(SQ * DH * 2), RSRC_W3);
  const rsrc_t rq = __builtin_amdgcn_make_buffer_rsrc((void*)pq, 0, DM * NH * DH * 2, RSRC_W3);
  const rsrc_t ro = __builtin_amdgcn_make_buffer_rsrc((void*)(po + (size_t)hg * 512 * DM), 0, 512 * DM * 2, RSRC_W3);

  f32x4 accv[20];
  u32x4 W[4][8];
  u32x4 A[2];

  auto gmf = [&](auto FI, auto TI, auto BI, auto ZI, const u32x4& Af) {
    constexpr int f = FI, t = TI, bq = A_B + 24 * BI + 4 * t;
    constexpr bool z = ZI;
    if constexpr (tile_v(f, t)) mf_v_va<bq, z>(accv[tile_vi(f, t)], Af);
    else mf_a_va<tile_a(f, t), bq, z>(Af);
  };
  auto gpk = [&](auto PI, auto TI, auto RI) -> unsigned {
    constexpr int pp = PI, t = TI, r = RI;
    if constexpr (tile_v(2 * pp, t)) return pk_bf16(accv[tile_vi(2 * pp, t)][r], accv[tile_vi(2 * pp + 1, t)][r]);
    else return pk_acc<tile_a(2 * pp, t) + r, tile_a(2 * pp + 1, t) + r>();
  };

  const int vx = g * 1024 + ((q ^ g ^ (4 * w)) << 4);
  const int vwq = (8 * g) * 8192 + (hg * 512 + w * 128 + 8 * cchunk) * 2;
  auto xdma = [&](auto II) {
    constexpr int qt = II / 6, i = II % 6;
    dma_x(lb + qt * XQ_BYTES + (4 * i + w) * 1024, rx, vx, (4 * i + w) * 4096 + qt * 256);
  };
  const int vpf = ((lane >> 5) * 32 + 2 * b + (w >> 1)) * 8192 + hg * 1024 + (w & 1) * 512 + (lane & 31) * 16;
  nop<4>();
  STAMP(8);
  ld_pfv(pfd, rq, vpf, 0);
  ld_pfv(pfd, rq, vpf, 64 * 8192);
  sfor<6>([&](auto I) { xdma(I); });
  STAMP(9);
  wait_vm<7>();
  STAMP(10);
  sfor<16>([&](auto I) { ld_w<0>(W[I >> 3][I & 7], rq, vwq, (32 * (I >> 3) + (I & 7)) * 8192); });
  int xa[2][4];
#pragma unroll
  for (int h = 0; h < 2; ++h)
#pragma unroll
    for (int j = 0; j < 4; ++j) xa[h][j] = lb + h * 2 * XQ_BYTES + q * 256 + (((4 * j + g) ^ q) << 4);
  const int kst = lb + V1_BASE + w * 16384 + 16384 - 16 * 272;
  const int kwa = kst + g * 272 + q * 16, kra = kst + q * 272 + g * 64;
  const int vv0 = g * 256 + ((q ^ (((g >> 1) << 2) | (g & 1))) << 4);
  const int vv1 = vv0 ^ 128;
  auto vdma = [&](auto JI) {
    constexpr int j = JI;
    dma_v(j < 16 ? lb + V1_BASE + w * 16384 + j * 1024 : lb + w * 8192 + (j - 16) * 1024, rv, (j & 1) ? vv1 : vv0, j * 1024);
  };
  STAMP(11);
  SCHED;
  if (b & 1) __builtin_amdgcn_s_sleep(STAG);
  wait_vm<A_NP - 9>();
  barrier();
  STAMP(1);

  constexpr int TG[3][2] = {{4, 5}, {2, 3}, {0, 1}};
  u32x4 AT[3][8];
  auto tperm = [&](auto KI) {
    constexpr int k = KI, m = k >> 1, d = 2 * (k & 1);
    AT[m >> 3][m & 7][d] = aperm(W[(13 + (m >> 3)) & 3], m & 7, d);
    AT[m >> 3][m & 7][d + 1] = aperm(W[(13 + (m >> 3)) & 3], m & 7, d + 1);
  };
  auto tmf = [&](auto GI, auto UI) {
    constexpr int G = GI, u = UI, sp = u >> 4, f = (u >> 1) & 7, tt = u & 1, t = TG[G][tt];
    constexpr int bq = A_B + 24 * ((G & 1) ^ 1) + 4 * (2 * sp + tt);
    if constexpr (tile_v(f, t)) mf_v_va<bq, false>(accv[tile_vi(f, t)], AT[sp][f]);
    else mf_a_va<tile_a(f, t), bq, false>(AT[sp][f]);
  };
  u32x4 Qf[6][4];
  float ta[2][2];
  auto qp = [&](auto II, auto JI) {
    constexpr int qi = II, t = TG[qi >> 1][qi & 1], j = JI, pp = (j & 15) >> 2, s = j & 3, i = j > 0 ? j - 1 : 0;
    if constexpr (j < 16) {
      if constexpr (tile_v(2 * pp, t)) Qf[qi][s][pp] = gpk(IC<pp>{}, IC<t>{}, IC<s>{});
      else acc_rd2<tile_a(2 * pp, t) + s, tile_a(2 * pp + 1, t) + s>(ta[j & 1][0], ta[j & 1][1]);
    }
    if constexpr (j > 0 && !tile_v(2 * (i >> 2), t)) Qf[qi][i & 3][i >> 2] = pk_bf16(ta[i & 1][0], ta[i & 1][1]);
  };

  auto xrdx = [&](auto SI, auto TI, auto DI_) {
    constexpr int s = SI, t = TI;
    lds_rd_a<DI_, ((s >> 2) & 1) * XQ_BYTES + t * 4096>(xa[s >> 3][s & 3]);
  };
  auto xrd = [&](auto SI, auto TI) { xrdx(SI, TI, IC<A_B + 24 * (SI & 1) + 4 * TI>{}); };
  auto txrd = [&](auto GI, auto JI) {
    constexpr int G = GI, jj = JI;
    xrdx(IC<13 + jj / 2>{}, IC<TG[G][jj & 1]>{}, IC<A_B + 24 * ((G & 1) ^ 1) + 4 * jj>{});
  };
  auto aop = [&](auto OI) {
    constexpr int op = OI, j = op % 1000;
    if constexpr (op >= OW) ld_w<0>(W[(j >> 3) & 3][j & 7], rq, vwq, (32 * (j >> 3) + (j & 7)) * 8192);
    else if constexpr (op >= OV) vdma(IC<j>{});
    else if constexpr (op >= OP) ld_pfv(pfd, rq, vpf, j * 64 * 8192);
    else if constexpr (op >= OK_) ld_k<A_K + 4 * j, (j & 3) * 1024>(rk, vk, (j >> 2) * 4096);
    else if constexpr (op >= OX) xdma(IC<j>{});
  };
  sfor<6>([&](auto T) { xrd(IC<0>{}, T); });
  wait_vm<8>();
  SCHED;
#pragma unroll
  for (int pp = 0; pp < 4; ++pp) A[0][pp] = aperm(W[0], 0, pp);
  SCHED;
  wait_lgkm0();
  nop<0>();
  SCHED;
  sfor<104>([&](auto NI) {
    constexpr int n = NI, s = n >> 3, f = n & 7;
    constexpr int kt = a_kt(s), ktc = kt < 0 ? 0 : kt;
    if constexpr (f == 0 && A_S.bar[s]) {
      barrier();
      SCHED;
    }
    sfor<6>([&](auto TI) {
      constexpr int t = TI;
      gmf(IC<f>{}, TI, IC<(s & 1)>{}, std::bool_constant<s == 0>{}, A[n & 1]);
      SCHED;
      if constexpr (t == 0 && f == 7) wait_vm<a_wait(s)>();
      aop(IC<((t & 1) ? 0 : A_S.op[s][3 * f + t / 2])>{});
      if constexpr (t == 5) {
        if constexpr (f < 6) {
          if constexpr (s < 12) xrd(IC<s + 1>{}, IC<f>{});
          else txrd(IC<0>{}, IC<f>{});
        } else if constexpr (f == 7) wait_lgkm0();
        else if constexpr (kt >= 0 && f == 6) lds_rd_a<A_K + 16 * ktc + 12, 48>(kra);
        else nop<0>();
      }
      if constexpr (n < 103) {
        if constexpr (t >= 1 && t <= 4) A[(n + 1) & 1][t - 1] = aperm(W[((n + 1) >> 3) & 3], (n + 1) & 7, t - 1);
      } else if constexpr (t & 1) tperm(IC<t / 2>{});
      if constexpr (t == 3 && kt >= 0) {
        if constexpr (f < 4) lds_wr_a<A_K + 16 * ktc + 4 * f, f * 4 * 272>(kwa);
        else if constexpr (f < 7) lds_rd_a<A_K + 16 * ktc + 4 * (f - 4), (f - 4) * 16>(kra);
      }
      SCHED;
    });
  });
  sfor<3>([&](auto GI) {
    constexpr int G = GI, s = 13 + G;
    constexpr int kt = a_kt(s), ktc = kt < 0 ? 0 : kt;
    sfor<48>([&](auto UI) {
      constexpr int u = UI;
      tmf(GI, UI);
      SCHED;
      if constexpr (u == 42 && s < 15) wait_vm<a_wait(s)>();
      if constexpr (G == 0 && u == 13) wait_vm<a_wait_w(14, 13, 7)>();
      if constexpr (G == 0 && u == 29) wait_vm<a_wait_w(15, 13, 15)>();
      aop(IC<((u & 1) ? 0 : A_S.op[s][u / 2])>{});
      if constexpr (G == 0 && u + 3 < 48) tperm(IC<u + 3>{});
      if constexpr (G == 0 && u == 45) nop<0>();
      if constexpr (G < 2 && u % 6 == 5 && u / 6 < 6) txrd(IC<(G < 2 ? G + 1 : 0)>{}, IC<u / 6>{});
      if constexpr (kt >= 0 && u % 6 == 3) {
        if constexpr (u / 6 < 4) lds_wr_a<A_K + 16 * ktc + 4 * (u / 6), (u / 6) * 4 * 272>(kwa);
        else if constexpr (u / 6 < 7) lds_rd_a<A_K + 16 * ktc + 4 * (u / 6 - 4), (u / 6 - 4) * 16>(kra);
      }
      if constexpr (kt >= 0 && u == 41) lds_rd_a<A_K + 16 * ktc + 12, 48>(kra);
      if constexpr (u == 47) wait_lgkm0();
      if constexpr (G > 0 && u >= 2) {
        constexpr int lo = (u - 2) * 34 / 45 < 34 ? (u - 2) * 34 / 45 : 34, hi = (u - 1) * 34 / 45 < 34 ? (u - 1) * 34 / 45 : 34;
        sfor<hi - lo>([&](auto KI) { qp(IC<2 * (G > 0 ? G - 1 : 0) + (lo + KI) / 17>{}, IC<(lo + KI) % 17>{}); });
      }
      SCHED;
    });
  });
  asm volatile("" ::"v"(pfd));
  STAMP(2);

  f32x4 sacc[2][6];
  u32x4 Pf[6][3];
  f32x4 oacc[2][8];
  f32x4 lacc[2];
  float mA = 0.f, mB = 0.f, li = 0.f;
  float ev[24];
  const float c = 0.08838834764831845f * 1.4426950408889634f;

  auto sm = [&](auto TI, auto JI) {
    constexpr int t = TI, j = JI;
    const f32x4(&S)[6] = sacc[t & 1];
    auto v = [&](int i) -> float { return S[i >> 2][i & 3]; };
    if constexpr (j == 0) {
      mA = max3(v(0), v(1), v(2));
      mB = max3(v(3), v(4), v(5));
    } else if constexpr (j < 5) {
      mA = max3(mA, v(4 * j + 2), v(4 * j + 3));
      mB = max3(mB, v(4 * j + 4), v(4 * j + 5));
    } else if constexpr (j == 5) {
      mA = __builtin_elementwise_maximum(max3(mA, v(22), v(23)), mB);
      xmax16(mA);
    } else if constexpr (j == 6) xmax32s(mA, -c);
    else if constexpr (j < 7 + 24) {
      constexpr int i = j - 7;
      ev[i] = __builtin_amdgcn_exp2f(__builtin_fmaf(v(i), c, mA));
    } else {
      constexpr int i = j - 31;
      Pf[t][i >> 2][i & 3] = pk_bf16(ev[2 * i], ev[2 * i + 1]);
    }
  };
  constexpr int SM_N = 43;
  const int vo = lane * 16;
  const int oa1 = lb + V1_BASE + w * 16384 + vo, oa2 = lb + w * 8192 + vo;
  u32x4 ot;
  auto oe = [&](auto TI, auto JI) {
    constexpr int t = TI, j = JI;
    if constexpr (j == 0) {
      asm volatile("" : "+v"(lacc[t & 1]));
      li = __builtin_amdgcn_rcpf(lacc[t & 1][0]);
    }
    else {
      constexpr int i = j - 1, sp = i / 5, e = i % 5;
      if constexpr (e < 4) {
        const f32x4& o = oacc[t & 1][2 * sp + (e >> 1)];
        ot[e] = pk_bf16(o[2 * (e & 1)] * li, __builtin_fmaf(o[2 * (e & 1) + 1], li, 0.f));
      } else {
        constexpr int fi = sp * 6 + TG[t >> 1][t & 1];
        if constexpr (fi < 16) lds_wr<fi * 1024>(oa1, ot);
        else lds_wr<(fi - 16) * 1024>(oa2, ot);
      }
    }
  };
  constexpr int OE_N = 21;
  const int vtr = (4 * g + ((lane >> 2) & 3)) * 256 +
                  (((lane & 3) ^ (((g & 1) << 3) | (((lane >> 3) & 1) << 2) | ((lane >> 2) & 1))) << 4) + 8 * (lane & 1);
  const int vt1 = lb + V1_BASE + w * 16384 + vtr, vt2 = lb + w * 8192 + vtr;
  auto vrd = [&](auto JI) {
    constexpr int j = JI, f = j / 6, t3 = (j % 6) >> 1, hh = j & 1, sp = f >> 1, h = f & 1;
    constexpr int x = (sp << 6) ^ (h << 3), d = A_V + 4 * (t3 * 8 + f) + 2 * hh;
    if constexpr (t3 < 2) lds_rdtr_a<d, (32 * t3 + 16 * hh) * 256>(vt1 ^ x);
    else lds_rdtr_a<d, 16 * hh * 256>(vt2 ^ x);
  };
  const int vwoA = (8 * g + 4 * (g & 1)) * 1024 + (w * 128 + 8 * cchunk) * 2;
  const int vwoB = (8 * g - 4 * (g & 1) + 4) * 1024 + (w * 128 + 8 * cchunk) * 2;
  auto wold = [&](auto SI, auto II) {
    constexpr int S = SI, i = II;
    const int so = (((w + (S >> 2)) & 3) * 128 + 32 * (S & 3)) * 1024;
    ld_w<(i & 3) * 1024>(W[S & 3][i], ro, i < 4 ? vwoA : vwoB, so);
  };

  sfor<6>([&](auto TI) {
    constexpr int t = TI;
    constexpr int NQ = t == 0 ? 34 : 0, NS = t > 0 ? SM_N : 0, NV = t == 5 ? 48 : 0;
    constexpr int NF = NQ + NS + NV, M0 = t == 0 ? 2 : 0;
    if constexpr (t == 5) wait_vm<PF_WO>();
    sfor<24>([&](auto MI) {
      constexpr int m = MI, s = m / 6, kt = m % 6;
      mf_v_av<A_K + 4 * (kt * 4 + s), s == 0>(sacc[t & 1][kt], Qf[t][s]);
      SCHED;
      if constexpr (m % 8 == 2 && t < 4 && 3 * t + m / 8 < B_NV) vdma(IC<B_V[(3 * t + m / 8) < B_NV ? 3 * t + m / 8 : 0]>{});
      if constexpr (t == B_PFT && m % 3 == 0 && m / 3 < PF_WO) ld_pf<A_B + 4 * (m / 3)>(ro, vk, (32 * b + 8 * w + m / 3) * 1024);
      constexpr int m0 = m < M0 ? 0 : m - M0, m1 = m + 1 < M0 ? 0 : m + 1 - M0;
      constexpr int lo = m0 * NF / 22 < NF ? m0 * NF / 22 : NF, hi = m1 * NF / 22 < NF ? m1 * NF / 22 : NF;
      sfor<hi - lo>([&](auto KI) {
        constexpr int k = lo + KI;
        if constexpr (k < NS) sm(IC<t - 1>{}, IC<k>{});
        else if constexpr (k < NS + NQ) qp(IC<4 + (k - NS) / 17>{}, IC<(k - NS) % 17>{});
        else vrd(IC<k - NS - NQ>{});
      });
      SCHED;
    });
  });
  wait_lgkm0();
  STAMP(3);
  u32x4 ones = u32x4{0x3F803F80u, 0x3F803F80u, 0x3F803F80u, 0x3F803F80u};
  asm volatile("" : "+v"(ones));
  nop<1>();
  sfor<6>([&](auto TI) {
    constexpr int t = TI;
    constexpr int NS = t == 0 ? SM_N : 0, NO = t > 0 ? OE_N : 0, NW = t >= 3 ? 8 : 0;
    constexpr int NF = NS + NO + NW;
    sfor<27>([&](auto MI) {
      constexpr int m = MI, t3 = m / 9, f = m % 9;
      if constexpr (f > 0) mf_v_av<A_V + 4 * (t3 * 8 + f - 1), t3 == 0>(oacc[t & 1][f - 1], Pf[t][t3]);
      else mf_v_vv<t3 == 0>(lacc[t & 1], ones, Pf[t][t3]);
      SCHED;
      constexpr int lo = m * NF / 27, hi = (m + 1) * NF / 27;
      sfor<hi - lo>([&](auto KI) {
        constexpr int k = lo + KI;
        if constexpr (k < NS) sm(IC<5>{}, IC<k>{});
        else if constexpr (k < NS + NO) oe(IC<t - 1>{}, IC<k - NS>{});
        else wold(IC<t - 3>{}, IC<k - NS - NO>{});
      });
      SCHED;
    });
  });
  sfor<OE_N>([&](auto J) { oe(IC<5>{}, J); });
  SCHED;
  STAMP(4);

  int ob[4][2];
#pragma unroll
  for (int h = 0; h < 4; ++h) {
    const int wp = (w + h) & 3;
    ob[h][0] = lb + V1_BASE + wp * 16384 + vo;
    ob[h][1] = lb + wp * 8192 + vo;
  }
  auto ordx = [&](auto SI, auto TI, auto DI_) {
    constexpr int S = SI, t = TI, d = DI_, fi = (S & 3) * 6 + t;
    if constexpr (fi < 16) lds_rd_a<d, fi * 1024>(ob[S >> 2][0]);
    else lds_rd_a<d, (fi - 16) * 1024>(ob[S >> 2][1]);
  };
  auto ord = [&](auto SI, auto TI) { ordx(SI, TI, IC<A_B + 24 * (SI & 1) + 4 * TI>{}); };
  auto tord = [&](auto GI, auto JI) {
    constexpr int G = GI, jj = JI;
    ordx(IC<13 + jj / 2>{}, IC<TG[G][jj & 1]>{}, IC<A_B + 24 * ((G & 1) ^ 1) + 4 * jj>{});
  };
  wait_vm<16>();
  sfor<6>([&](auto T) { ord(IC<0>{}, T); });
  SCHED;
#pragma unroll
  for (int pp = 0; pp < 4; ++pp) A[0][pp] = aperm(W[0], 0, pp);
  SCHED;
  wait_lgkm0();
  nop<0>();
  SCHED;
  sfor<104>([&](auto NI) {
    constexpr int n = NI, s = n >> 3, f = n & 7;
    if constexpr (n == 24) {
      barrier();
      SCHED;
    }
    sfor<6>([&](auto TI) {
      constexpr int t = TI;
      gmf(IC<f>{}, TI, IC<(s & 1)>{}, std::bool_constant<s == 0>{}, A[n & 1]);
      SCHED;
      if constexpr ((t == 0 || t == 2) && f < 4) wold(IC<s + 3>{}, IC<(2 * f + (t >> 1)) & 7>{});
      if constexpr (t == 0 && f == 7) wait_vm<16>();
      if constexpr (t == 5) {
        if constexpr (f < 6) {
          if constexpr (s < 12) ord(IC<s + 1>{}, IC<f>{});
          else tord(IC<0>{}, IC<f>{});
        } else if constexpr (f == 7) wait_lgkm0();
        else nop<0>();
      }
      if constexpr (n < 103) {
        if constexpr (t >= 1 && t <= 4) A[(n + 1) & 1][t - 1] = aperm(W[((n + 1) >> 3) & 3], (n + 1) & 7, t - 1);
      } else if constexpr (t & 1) tperm(IC<t / 2>{});
      SCHED;
    });
  });
  const rsrc_t rp = __builtin_amdgcn_make_buffer_rsrc((void*)(part + (size_t)(((br * NBATCH + b) * 8 + hg) * 4 + w) * 24 * 64), 0,
                                                      24 * 1024, RSRC_W3);
  u32x4 so[2];
  auto stp = [&](auto GI, auto KI) {
    constexpr int G = GI, pc = KI / 6, jj = KI % 6, t = TG[G][pc >> 2], r = pc & 3, i = jj > 0 ? jj - 1 : 0;
    if constexpr (jj < 4) {
      if constexpr (tile_v(2 * jj, t)) so[pc & 1][jj] = gpk(IC<jj>{}, IC<t>{}, IC<r>{});
      else acc_rd2<tile_a(2 * jj, t) + r, tile_a(2 * jj + 1, t) + r>(ta[jj & 1][0], ta[jj & 1][1]);
    }
    if constexpr (jj > 0 && jj < 5 && !tile_v(2 * i, t)) so[pc & 1][i] = pk_bf16(ta[i & 1][0], ta[i & 1][1]);
    if constexpr (jj == 5) {
      __builtin_amdgcn_raw_buffer_store_b128(so[pc & 1], rp, vo + r * 1024, t * 4096, AUX_PST);
      SCHED;
      nop<1>();
    }
  };
  sfor<3>([&](auto GI) {
    constexpr int G = GI;
    sfor<48>([&](auto UI) {
      constexpr int u = UI;
      tmf(GI, UI);
      SCHED;
      if constexpr (G == 0 && u + 3 < 48) {
        if constexpr (u + 3 == 16) wait_vm<8>();
        if constexpr (u + 3 == 32) wait_vm<0>();
        tperm(IC<u + 3>{});
      }
      if constexpr (G == 0 && u == 45) nop<0>();
      if constexpr (G < 2 && u % 6 == 4 && u / 6 < 6) tord(IC<(G < 2 ? G + 1 : 0)>{}, IC<u / 6>{});
      if constexpr (G < 2 && u == 47) wait_lgkm0();
      if constexpr (G > 0 && u >= 2) {
        constexpr int lo = (u - 2) * 48 / 46, hi = (u - 1) * 48 / 46;
        sfor<hi - lo>([&](auto KI) { stp(IC<(G > 0 ? G - 1 : 0)>{}, IC<lo + KI>{}); });
      }
      SCHED;
    });
  });
  STAMP(5);
  nop<7>();
  sfor<48>([&](auto KI) {
    stp(IC<2>{}, KI);
    SCHED;
  });
  STAMP(6);
}

__global__ __launch_bounds__(256) void k2_reduce(const u32x4* __restrict__ part, unsigned short* __restrict__ out) {
  const int i = blockIdx.x * 256 + threadIdx.x;
  const int bb = i / 6144, e = i % 6144;
  const rsrc_t rp = __builtin_amdgcn_make_buffer_rsrc((void*)(part + (size_t)bb * 8 * 6144), 0, 8 * 6144 * 16, RSRC_W3);
  u32x4 v[8];
#pragma unroll
  for (int h = 0; h < 8; ++h) v[h] = __builtin_amdgcn_raw_buffer_load_b128(rp, e * 16, h * 6144 * 16, AUX_PLD);
  float s[8];
#pragma unroll
  for (int j = 0; j < 8; ++j) s[j] = 0.f;
#pragma unroll
  for (int h = 0; h < 8; ++h)
#pragma unroll
    for (int j = 0; j < 4; ++j) {
      s[2 * j] += __builtin_bit_cast(float, v[h][j] << 16);
      s[2 * j + 1] += __builtin_bit_cast(float, v[h][j] & 0xffff0000u);
    }
  u32x4 o;
#pragma unroll
  for (int j = 0; j < 4; ++j) o[j] = pk_bf16(s[2 * j], s[2 * j + 1]);
  const int lane = e & 63, pc = e >> 6, r = pc & 3, t = (pc >> 2) % 6, w = pc / 24;
  const int q = lane & 15, g = lane >> 4;
  const int br = bb >> 4, b = bb & 15;
  *(u32x4*)(out + ((size_t)b * SQ + 16 * t + q) * 1024 + br * 512 + 128 * w + 32 * g + 8 * r) = o;
  asm volatile(".Lk2_end:");
}

extern "C" void kernel_launch(void* const* d_in, const int* in_sizes, int n_in, void* d_out, int out_size, void* d_ws,
                              size_t ws_size, hipStream_t stream) {
  Ptrs p;
  for (int br = 0; br < 2; ++br) {
    p.x[br] = (const unsigned short*)d_in[5 * br + 0];
    p.k[br] = (const unsigned short*)d_in[5 * br + 1];
    p.v[br] = (const unsigned short*)d_in[5 * br + 2];
    p.wq[br] = (const unsigned short*)d_in[5 * br + 3];
    p.wo[br] = (const unsigned short*)d_in[5 * br + 4];
  }
  u32x4* part = (u32x4*)d_ws;
  (void)hipFuncSetAttribute((const void*)k1_fused, hipFuncAttributeMaxDynamicSharedMemorySize, LDS_BYTES);
  k1_fused<<<256, 256, LDS_BYTES, stream>>>(p, part
  );
  k2_reduce<<<2 * 16 * 96 * 64 / 256, 256, 0, stream>>>(part, (unsigned short*)d_out);
}
```

```cpp
#include <hip/hip_runtime.h>
#include <hip/hip_bf16.h>
#include <utility>

typedef unsigned u32x2 __attribute__((ext_vector_type(2)));
typedef unsigned u32x4 __attribute__((ext_vector_type(4)));
typedef float f32x4 __attribute__((ext_vector_type(4)));
#define LDSP __attribute__((address_space(3)))
#define DI __device__ __forceinline__
typedef __amdgpu_buffer_rsrc_t rsrc_t;

constexpr int SQ = 96, DM = 512, NH = 32, DH = 128, NBATCH = 16;
constexpr int RSRC_W3 = 0x00020000;
#define POL_KV " nt"
#define POL_W ""
#define POL_PF ""
#define POL_X ""
#define AUX_PST 16
#define AUX_PLD 0

template <class F, int... I>
DI void sfor_impl(F&& f, std::integer_sequence<int, I...>) {
  (f(std::integral_constant<int, I>{}), ...);
}
template <int N, class F>
DI void sfor(F&& f) {
  sfor_impl(f, std::make_integer_sequence<int, N>{});
}
template <int N>
using IC = std::integral_constant<int, N>;

typedef float f32x2 __attribute__((ext_vector_type(2)));
typedef __bf16 bf16x2 __attribute__((ext_vector_type(2)));
DI unsigned pk_bf16(float lo, float hi) {
  return __builtin_bit_cast(unsigned, __builtin_convertvector(f32x2{lo, hi}, bf16x2));
}
DI unsigned perm_lo(unsigned e, unsigned o) { return __builtin_amdgcn_perm(o, e, 0x05040100u); }
DI unsigned perm_hi(unsigned e, unsigned o) { return __builtin_amdgcn_perm(o, e, 0x07060302u); }
#define SCHED __builtin_amdgcn_sched_barrier(0)

#define MF "v_mfma_f32_16x16x32_bf16 "
template <int D, int Bq, bool Z>
DI void mf_a_va(const u32x4& A) {
  if constexpr (Z) asm volatile(MF "a[%c0:%c1], %2, a[%c3:%c4], 0" ::"i"(D), "i"(D + 3), "v"(A), "i"(Bq), "i"(Bq + 3));
  else asm volatile(MF "a[%c0:%c1], %2, a[%c3:%c4], a[%c0:%c1]" ::"i"(D), "i"(D + 3), "v"(A), "i"(Bq), "i"(Bq + 3));
}
template <int Bq, bool Z>
DI void mf_v_va(f32x4& C, const u32x4& A) {
  if constexpr (Z) asm volatile(MF "%0, %1, a[%c2:%c3], 0" : "=&v"(C) : "v"(A), "i"(Bq), "i"(Bq + 3));
  else asm volatile(MF "%0, %1, a[%c2:%c3], %0" : "+v"(C) : "v"(A), "i"(Bq), "i"(Bq + 3));
}
template <int Aq, bool Z>
DI void mf_v_av(f32x4& C, const u32x4& Bv) {
  if constexpr (Z) asm volatile(MF "%0, a[%c2:%c3], %1, 0" : "=&v"(C) : "v"(Bv), "i"(Aq), "i"(Aq + 3));
  else asm volatile(MF "%0, a[%c2:%c3], %1, %0" : "+v"(C) : "v"(Bv), "i"(Aq), "i"(Aq + 3));
}
template <bool Z>
DI void mf_v_vv(f32x4& C, const u32x4& A, const u32x4& Bv) {
  if constexpr (Z) asm volatile(MF "%0, %1, %2, 0" : "=&v"(C) : "v"(A), "v"(Bv));
  else asm volatile(MF "%0, %1, %2, %0" : "+v"(C) : "v"(A), "v"(Bv));
}
template <int X, int Y>
DI unsigned pk_acc() {
  unsigned r, t0, t1;
  asm volatile("v_accvgpr_read_b32 %1, a[%c3]\n\tv_accvgpr_read_b32 %2, a[%c4]\n\tv_cvt_pk_bf16_f32 %0, %1, %2"
               : "=v"(r), "=&v"(t0), "=&v"(t1)
               : "i"(X), "i"(Y));
  return r;
}
template <int IMM>
DI void ld_w(u32x4& d, rsrc_t r, int voff, int soff) {
  asm volatile("buffer_load_dwordx4 %0, %1, %2, %3 offen offset:%c4" POL_W : "=v"(d) : "v"(voff), "s"(r), "s"(soff), "i"(IMM));
}
template <int D, int IMM>
DI void ld_k(rsrc_t r, int voff, int soff) {
  asm volatile("buffer_load_dwordx4 a[%c0:%c1], %2, %3, %4 offen offset:%c5" POL_KV ::"i"(D), "i"(D + 3), "v"(voff), "s"(r),
               "s"(soff), "i"(IMM));
}
template <int D>
DI void ld_pf(rsrc_t r, int voff, int soff) {
  asm volatile("buffer_load_dwordx4 a[%c0:%c1], %2, %3, %4 offen" POL_PF ::"i"(D), "i"(D + 3), "v"(voff), "s"(r), "s"(soff));
}
DI void ld_pfv(u32x4& d, rsrc_t r, int voff, int soff) {
  asm volatile("buffer_load_dwordx4 %0, %1, %2, %3 offen" POL_PF : "+v"(d) : "v"(voff), "s"(r), "s"(soff));
}
DI void dma_x(int m0v, rsrc_t r, int voff, int soff) {
  asm volatile("s_mov_b32 m0, %0\n\ts_nop 0\n\tbuffer_load_dwordx4 %1, %2, %3 offen" POL_X " lds" ::"s"(m0v), "v"(voff), "s"(r),
               "s"(soff)
               : "memory");
}
DI void dma_v(int m0v, rsrc_t r, int voff, int soff) {
  asm volatile("s_mov_b32 m0, %0\n\ts_nop 0\n\tbuffer_load_dwordx4 %1, %2, %3 offen" POL_KV " lds" ::"s"(m0v), "v"(voff), "s"(r),
               "s"(soff)
               : "memory");
}
template <int D, int IMM>
DI void lds_rd_a(int addr) {
  asm volatile("ds_read_b128 a[%c0:%c1], %2 offset:%c3" ::"i"(D), "i"(D + 3), "v"(addr), "i"(IMM) : "memory");
}
template <int D, int IMM>
DI void lds_rdtr_a(int addr) {
  asm volatile("ds_read_b64_tr_b16 a[%c0:%c1], %2 offset:%c3" ::"i"(D), "i"(D + 1), "v"(addr), "i"(IMM) : "memory");
}
template <int D, int IMM>
DI void lds_wr_a(int addr) {
  asm volatile("ds_write_b128 %2, a[%c0:%c1] offset:%c3" ::"i"(D), "i"(D + 3), "v"(addr), "i"(IMM) : "memory");
}
template <int IMM>
DI void lds_wr(int addr, const u32x4& d) {
  asm volatile("ds_write_b128 %0, %1 offset:%c2" ::"v"(addr), "v"(d), "i"(IMM) : "memory");
}
template <int N>
DI void wait_vm() {
  static_assert(N >= 0 && N < 64, "vmcnt");
  asm volatile("s_waitcnt vmcnt(%c0)" ::"i"(N) : "memory");
}
DI void wait_lgkm0() { asm volatile("s_waitcnt lgkmcnt(0)" ::: "memory"); }
DI void barrier() { asm volatile("s_barrier" ::: "memory"); }
template <int N>
DI void nop() {
  asm volatile("s_nop %c0" ::"i"(N));
}
DI float max3(float a, float b, float c) { return __builtin_elementwise_maximum(__builtin_elementwise_maximum(a, b), c); }
DI void xmax16(float& a) {
  float t;
  asm volatile("v_mov_b32 %1, %0\n\ts_nop 1\n\tv_permlane16_swap_b32 %0, %1\n\tv_max_f32 %0, %0, %1" : "+v"(a), "=&v"(t));
}
DI void xmax32s(float& a, float s) {
  float t;
  asm volatile("v_mov_b32 %1, %0\n\ts_nop 1\n\tv_permlane32_swap_b32 %0, %1\n\tv_max_f32 %0, %0, %1\n\tv_mul_f32 %0, %2, %0"
               : "+v"(a), "=&v"(t)
               : "s"(s));
}
template <int X, int Y>
DI void acc_rd2(float& a, float& b) {
  asm volatile("v_accvgpr_read_b32 %0, a[%c2]\n\tv_accvgpr_read_b32 %1, a[%c3]" : "=v"(a), "=v"(b) : "i"(X), "i"(Y));
}

#define STAMP(i)

#define HALFX(n) (n)
struct Ptrs {
  const unsigned short* x[2];
  const unsigned short* k[2];
  const unsigned short* v[2];
  const unsigned short* wq[2];
  const unsigned short* wo[2];
};

constexpr int LDS_BYTES = 163840;
constexpr int XQ_BYTES = 24576, V1_BASE = 98304;

constexpr int A_B = 112, A_K = 160, A_V = 0;
constexpr bool tile_v(int f, int t) { return t < 2 || (t == 2 && f < 4); }
constexpr int tile_vi(int f, int t) { return t * 8 + f; }
constexpr int tile_a(int f, int t) { return 4 * (t >= 3 ? (t - 3) * 8 + f : 24 + (f - 4)); }

#define PF_WO 8
enum { OX = 1000, OK_ = 2000, OP = 3000, OV = 4000, OW = 5000 };
struct ASched {
  int op[16][24];
  bool bar[16];
};
#define SCHV 5
#define PFPV 6
#define STAG 16
#define X6(q) OX + 6 * q, OX + 6 * q + 1, OX + 6 * q + 2, OX + 6 * q + 3, OX + 6 * q + 4, OX + 6 * q + 5
#define K4(t) OK_ + 4 * t, OK_ + 4 * t + 1, OK_ + 4 * t + 2, OK_ + 4 * t + 3
#define V4(j) OV + j, OV + j + 1, OV + j + 2, OV + j + 3
#define PRO_OP 2
constexpr int A_BURST[6][12] = {{X6(1), OP + 4}, {X6(2), OP + 5}, {OP + 6, K4(0), OK_ + 4, OK_ + 5},
                                {X6(3), OP + 7}, {OK_ + 6, OK_ + 7, K4(2), OK_ + 12}, {OK_ + 13, OK_ + 14, OK_ + 15, K4(4)}};
constexpr int A_TAIL[3][8] = {{K4(5), OV + 0, OV + 1, OV + 2}, {OV + 3, V4(4), OV + 8}, {OV + 9, OV + 10, OV + 16, OV + 17, OV + 18}};
constexpr int A_KST[6] = {8, 11, 12, 13, 14, 15};
constexpr int B_V[] = {19, 20, 21, 22, 23, 11, 12, 13, 14, 15};
constexpr int B_NV = 10, B_PFT = 4;
constexpr ASched a_make() {
  ASched a{};
  for (int s = 0; s <= 12; s += 2)
    for (int i = 0; i < 16; ++i) a.op[s][(i >> 1) * 3 + (i & 1)] = OW + 8 * (s + 2) + i;
  for (int s = 1; s <= 11; s += 2) {
    a.bar[s] = true;
    for (int i = 0; i < (int)(sizeof(A_BURST[0]) / sizeof(int)); ++i) a.op[s][i] = A_BURST[s >> 1][i];
  }
  for (int s = 13; s < 16; ++s) {
    int n = 0;
    while (n < 8 && A_TAIL[s - 13][n]) ++n;
    for (int i = 0; i < n; ++i) a.op[s][3 * (i * 8 / n)] = A_TAIL[s - 13][i];
  }
  return a;
}
constexpr ASched A_S = a_make();
constexpr int A_NP = 3 + 6 + PRO_OP + 16;
constexpr int a_seq(int op) {
  if (op >= OW && op < OW + 16) return 9 + PRO_OP + op - OW;
  int c = A_NP;
  for (int s = 0; s < 16; ++s)
    for (int k = 0; k < 24; ++k)
      if (A_S.op[s][k]) {
        if (A_S.op[s][k] == op) return c;
        ++c;
      }
  return -1;
}
constexpr int a_issued(int s, int k) {
  int c = A_NP;
  for (int i = 0; i < 16; ++i)
    for (int j = 0; j < 24; ++j)
      if (A_S.op[i][j] && (i < s || (i == s && j < k))) ++c;
  return c;
}
constexpr int a_kt(int s) {
  for (int k = 0; k < 6; ++k)
    if (A_KST[k] == s) return k;
  return -1;
}
constexpr int imax(int a, int b) { return a > b ? a : b; }
constexpr int a_need(int s) {
  int n = 0;
  for (int i = 0; i < 8; ++i) n = imax(n, a_seq(OW + 8 * (s + 1) + i));
  if (s == 2 || s == 6 || s == 10)
    for (int i = 0; i < 6; ++i) n = imax(n, a_seq(OX + 6 * ((s + 2) / 4) + i));
  if (a_kt(s + 1) >= 0)
    for (int i = 0; i < 4; ++i) n = imax(n, a_seq(OK_ + 4 * a_kt(s + 1) + i));
  if (s == 14)
    for (int i = 2; i < 8; ++i) n = imax(n, a_seq(OP + i));
  return n;
}
constexpr int a_wait(int s) { return a_issued(s, 21) - a_need(s) - 1; }
constexpr int a_wait_w(int ws, int s, int k) {
  int n = 0;
  for (int i = 0; i < 8; ++i) n = imax(n, a_seq(OW + 8 * ws + i));
  return a_issued(s, k) - n - 1;
}
constexpr bool a_check() {
  for (int i = 0; i < 24; ++i)
    if (a_seq(OK_ + i) < 0) return false;
  for (int i = 6; i < 24; ++i)
    if (a_seq(OX + i) < 0) return false;
  for (int i = 0; i < 128; ++i)
    if (a_seq(OW + i) < 0 || (i >= 32 && a_seq(OW + i) < a_issued(i / 8 - 3, 0))) return false;
  int nv = B_NV;
  for (int i = 0; i < 24; ++i)
    if (a_seq(OV + i) >= 0) {
      ++nv;
      if (i >= 11 && i < 16 && a_seq(OV + i) < a_issued(A_KST[5] + 1, 0)) return false;
      if (i >= 16 && a_seq(OV + i) < a_issued(7, 0)) return false;
    }
  for (int s = 0; s < 15; ++s)
    if (a_wait(s) < 0 || a_wait(s) > 63) return false;
  return nv == 24 && A_S.bar[3] && A_S.bar[7] && A_S.bar[11];
}
static_assert(a_check(), "phase A schedule");

#define CT 6
constexpr int C_NS = 16 - CT, C_S0 = C_NS - CT + 2, C_B = 192;
constexpr int c_n(int s) { return s < C_S0 && 4 * s < 8 * (CT - 4) ? 12 : 8; }
constexpr int c_cum(int s) {
  int c = 24;
  for (int i = 0; i < s; ++i) c += c_n(i);
  return c;
}
static_assert(CT >= 4 && CT <= 6 && c_cum(C_NS) == 120, "phase C schedule");
enum { TL = 1000, TP = 2000, TB = 3000, TS = 4000, TWV = 5000, TWL = 6000 };
struct CPlan {
  int bt[6 * CT], bs[6 * CT];
  int ct[6];
  int op[48 * CT][3];
  int nst;
};
constexpr CPlan c_make() {
  CPlan c{};
  int nx[6] = {0, 0, 0, 0, 0, 0}, done[6] = {0, 0, 0, 0, 0, 0}, nc = 0;
  for (int p = 0; p < 6 * CT; ++p)
    for (int t = 0; t < 6; ++t)
      if (nx[t] < CT && (nx[t] < CT - 2 || (nx[t] == CT - 2 && p >= 3) || p >= 6)) {
        c.bt[p] = t;
        c.bs[p] = nx[t]++;
        if (nx[t] == CT) {
          c.ct[nc] = t;
          done[nc++] = 8 * p + 7;
        }
        break;
      }
  int used[48 * CT] = {};
  auto put = [&](int u, int op, int w) {
    int k = 0;
    while (c.op[u][k]) ++k;
    c.op[u][k] = op;
    used[u] += w;
  };
  for (int i = 0; i < 8; ++i) put(i, TL + i, 2);
  put(8, TWV + 8, 0);
  put(32, TWV + 0, 0);
  for (int k = 0; k < 32; ++k) {
    put(8 + k / 2, TP + k, 1);
    put(32 + k / 2, TP + 32 + k, 1);
  }
  for (int r = 0; r + 1 < CT; ++r) {
    for (int j = 0; j < 6; ++j) put(r ? 48 * r + 6 * j : 24 + j, TB + 6 * (r + 1) + j, 1);
    put(48 * r + 46, TWL, 0);
  }
  int u = 0, m = 0;
  for (; m < 6 * 52; ++m) {
    const int w = m % 13 == 12 ? 2 : 1;
    if (u < done[m / 52] + 2) u = done[m / 52] + 2;
    while (u < 48 * CT && used[u] + w > 2) ++u;
    if (u >= 48 * CT) break;
    put(u, TS + m, w);
  }
  c.nst = m;
  return c;
}
constexpr CPlan C_P = c_make();

DI unsigned aperm(const u32x4 (&W)[8], int f, int p) {
  return (f & 1) ? perm_hi(W[2 * p][f >> 1], W[2 * p + 1][f >> 1]) : perm_lo(W[2 * p][f >> 1], W[2 * p + 1][f >> 1]);
}

__global__ __launch_bounds__(256, 1) void k1_fused(Ptrs p, u32x4* part
) {
  extern __shared__ __attribute__((aligned(16))) char lds[];
  asm volatile("" ::: "a0", "a255");
  const int bid = blockIdx.x;
  const int xcd = bid & 7, idx = bid >> 3;
  const int br = xcd >> 2, hg = (xcd & 3) * 2 + (idx & 1), b = idx >> 1;
  const int tid = threadIdx.x, lane = tid & 63;
  const int w = __builtin_amdgcn_readfirstlane(tid >> 6);
  const int q = lane & 15, g = lane >> 4;
  const int cchunk = q;
  const int lb = (int)(unsigned)(size_t)(LDSP char*)lds;
  STAMP(0);

  const int vk = lane * 16;
  u32x4 pfd = u32x4{0, 0, 0, 0};
  {
    unsigned long long pc;
    int len;
    asm volatile("s_getpc_b64 %0\n.Lk1_pc:\n\ts_mov_b32 %1, .Lk2_end-.Lk1_pc" : "=s"(pc), "=s"(len));
    const rsrc_t rc = __builtin_amdgcn_make_buffer_rsrc((void*)pc, 0, len, RSRC_W3);
    nop<4>();
    ld_pfv(pfd, rc, vk, (idx * 4 + w) * 1024);
  }
  const unsigned short *px = p.x[br], *pk = p.k[br], *pv = p.v[br], *pq = p.wq[br], *po = p.wo[br];
  asm volatile("" : "+s"(px), "+s"(pk), "+s"(pv), "+s"(pq), "+s"(po), "+s"(part));
  const int head = hg * 4 + w;
  const size_t kvoff = ((size_t)b * NH + head) * SQ * DH;
  const rsrc_t rx = __builtin_amdgcn_make_buffer_rsrc((void*)(px + (size_t)b * SQ * DM), 0, HALFX(SQ * DM * 2), RSRC_W3);
  const rsrc_t rk = __builtin_amdgcn_make_buffer_rsrc((void*)(pk + kvoff), 0, HALFX(SQ * DH * 2), RSRC_W3);
  const rsrc_t rv = __builtin_amdgcn_make_buffer_rsrc((void*)(pv + kvoff), 0, HALFX(SQ * DH * 2), RSRC_W3);
  const rsrc_t rq = __builtin_amdgcn_make_buffer_rsrc((void*)pq, 0, DM * NH * DH * 2, RSRC_W3);
  const rsrc_t ro = __builtin_amdgcn_make_buffer_rsrc((void*)(po + (size_t)hg * 512 * DM), 0, 512 * DM * 2, RSRC_W3);

  f32x4 accv[20];
  u32x4 W[4][8];
  u32x4 A[2];

  auto gmf = [&](auto FI, auto TI, auto BI, auto ZI, const u32x4& Af) {
    constexpr int f = FI, t = TI, bq = A_B + 24 * BI + 4 * t;
    constexpr bool z = ZI;
    if constexpr (tile_v(f, t)) mf_v_va<bq, z>(accv[tile_vi(f, t)], Af);
    else mf_a_va<tile_a(f, t), bq, z>(Af);
  };
  auto gpk = [&](auto PI, auto TI, auto RI) -> unsigned {
    constexpr int pp = PI, t = TI, r = RI;
    if constexpr (tile_v(2 * pp, t)) return pk_bf16(accv[tile_vi(2 * pp, t)][r], accv[tile_vi(2 * pp + 1, t)][r]);
    else return pk_acc<tile_a(2 * pp, t) + r, tile_a(2 * pp + 1, t) + r>();
  };

  const int vx = g * 1024 + ((q ^ g ^ (4 * w)) << 4);
  const int vwq = (8 * g) * 8192 + (hg * 512 + w * 128 + 8 * cchunk) * 2;
  auto xdma = [&](auto II) {
    constexpr int qt = II / 6, i = II % 6;
    dma_x(lb + qt * XQ_BYTES + (4 * i + w) * 1024, rx, vx, (4 * i + w) * 4096 + qt * 256);
  };
  const int vpf = ((lane >> 5) * 32 + 2 * b + (w >> 1)) * 8192 + hg * 1024 + (w & 1) * 512 + (lane & 31) * 16;
  nop<4>();
  STAMP(8);
  ld_pfv(pfd, rq, vpf, 0);
  ld_pfv(pfd, rq, vpf, 64 * 8192);
  sfor<6>([&](auto I) { xdma(I); });
  sfor<PRO_OP>([&](auto I) { ld_pfv(pfd, rq, vpf, (2 + I) * 64 * 8192); });
  STAMP(9);
  wait_vm<7 + PRO_OP>();
  STAMP(10);
  sfor<16>([&](auto I) { ld_w<0>(W[I >> 3][I & 7], rq, vwq, (32 * (I >> 3) + (I & 7)) * 8192); });
  int xa[2][4];
#pragma unroll
  for (int h = 0; h < 2; ++h)
#pragma unroll
    for (int j = 0; j < 4; ++j) xa[h][j] = lb + h * 2 * XQ_BYTES + q * 256 + (((4 * j + g) ^ q) << 4);
  const int kst = lb + V1_BASE + w * 16384 + 16384 - 16 * 272;
  const int kwa = kst + g * 272 + q * 16, kra = kst + q * 272 + g * 64;
  const int vv0 = g * 256 + ((q ^ (((g >> 1) << 2) | (g & 1))) << 4);
  const int vv1 = vv0 ^ 128;
  auto vdma = [&](auto JI) {
    constexpr int j = JI;
    dma_v(j < 16 ? lb + V1_BASE + w * 16384 + j * 1024 : lb + w * 8192 + (j - 16) * 1024, rv, (j & 1) ? vv1 : vv0, j * 1024);
  };
  STAMP(11);
  SCHED;
  if (b & 1) __builtin_amdgcn_s_sleep(STAG);
  wait_vm<A_NP - 9>();
  barrier();
  STAMP(1);

  constexpr int TG[3][2] = {{4, 5}, {2, 3}, {0, 1}};
  u32x4 AT[3][8];
  auto tperm = [&](auto KI) {
    constexpr int k = KI, m = k >> 1, d = 2 * (k & 1);
    AT[m >> 3][m & 7][d] = aperm(W[(13 + (m >> 3)) & 3], m & 7, d);
    AT[m >> 3][m & 7][d + 1] = aperm(W[(13 + (m >> 3)) & 3], m & 7, d + 1);
  };
  auto tmf = [&](auto GI, auto UI) {
    constexpr int G = GI, u = UI, sp = u >> 4, f = (u >> 1) & 7, tt = u & 1, t = TG[G][tt];
    constexpr int bq = A_B + 24 * ((G & 1) ^ 1) + 4 * (2 * sp + tt);
    if constexpr (tile_v(f, t)) mf_v_va<bq, false>(accv[tile_vi(f, t)], AT[sp][f]);
    else mf_a_va<tile_a(f, t), bq, false>(AT[sp][f]);
  };
  u32x4 Qf[6][4];
  float ta[2][2];
  auto qp = [&](auto II, auto JI) {
    constexpr int qi = II, t = TG[qi >> 1][qi & 1], j = JI, pp = (j & 15) >> 2, s = j & 3, i = j > 0 ? j - 1 : 0;
    if constexpr (j < 16) {
      if constexpr (tile_v(2 * pp, t)) Qf[qi][s][pp] = gpk(IC<pp>{}, IC<t>{}, IC<s>{});
      else acc_rd2<tile_a(2 * pp, t) + s, tile_a(2 * pp + 1, t) + s>(ta[j & 1][0], ta[j & 1][1]);
    }
    if constexpr (j > 0 && !tile_v(2 * (i >> 2), t)) Qf[qi][i & 3][i >> 2] = pk_bf16(ta[i & 1][0], ta[i & 1][1]);
  };

  auto xrdx = [&](auto SI, auto TI, auto DI_) {
    constexpr int s = SI, t = TI;
    lds_rd_a<DI_, ((s >> 2) & 1) * XQ_BYTES + t * 4096>(xa[s >> 3][s & 3]);
  };
  auto xrd = [&](auto SI, auto TI) { xrdx(SI, TI, IC<A_B + 24 * (SI & 1) + 4 * TI>{}); };
  auto txrd = [&](auto GI, auto JI) {
    constexpr int G = GI, jj = JI;
    xrdx(IC<13 + jj / 2>{}, IC<TG[G][jj & 1]>{}, IC<A_B + 24 * ((G & 1) ^ 1) + 4 * jj>{});
  };
  auto aop = [&](auto OI) {
    constexpr int op = OI, j = op % 1000;
    if constexpr (op >= OW) ld_w<0>(W[(j >> 3) & 3][j & 7], rq, vwq, (32 * (j >> 3) + (j & 7)) * 8192);
    else if constexpr (op >= OV) vdma(IC<j>{});
    else if constexpr (op >= OP) ld_pfv(pfd, rq, vpf, j * 64 * 8192);
    else if constexpr (op >= OK_) ld_k<A_K + 4 * j, (j & 3) * 1024>(rk, vk, (j >> 2) * 4096);
    else if constexpr (op >= OX) xdma(IC<j>{});
  };
  sfor<6>([&](auto T) { xrd(IC<0>{}, T); });
  wait_vm<8>();
  SCHED;
#pragma unroll
  for (int pp = 0; pp < 4; ++pp) A[0][pp] = aperm(W[0], 0, pp);
  SCHED;
  wait_lgkm0();
  nop<0>();
  SCHED;
  sfor<104>([&](auto NI) {
    constexpr int n = NI, s = n >> 3, f = n & 7;
    constexpr int kt = a_kt(s), ktc = kt < 0 ? 0 : kt;
    if constexpr (f == 0 && A_S.bar[s]) {
      barrier();
      SCHED;
    }
    sfor<6>([&](auto TI) {
      constexpr int t = TI;
      gmf(IC<f>{}, TI, IC<(s & 1)>{}, std::bool_constant<s == 0>{}, A[n & 1]);
      SCHED;
      if constexpr (t == 0 && f == 7) wait_vm<a_wait(s)>();
      aop(IC<((t & 1) ? 0 : A_S.op[s][3 * f + t / 2])>{});
      if constexpr (t == 5) {
        if constexpr (f < 6) {
          if constexpr (s < 12) xrd(IC<s + 1>{}, IC<f>{});
          else txrd(IC<0>{}, IC<f>{});
        } else if constexpr (f == 7) wait_lgkm0();
        else if constexpr (kt >= 0 && f == 6) lds_rd_a<A_K + 16 * ktc + 12, 48>(kra);
        else nop<0>();
      }
      if constexpr (n < 103) {
        if constexpr (t >= 1 && t <= 4) A[(n + 1) & 1][t - 1] = aperm(W[((n + 1) >> 3) & 3], (n + 1) & 7, t - 1);
      } else if constexpr (t & 1) tperm(IC<t / 2>{});
      if constexpr (t == 3 && kt >= 0) {
        if constexpr (f < 4) lds_wr_a<A_K + 16 * ktc + 4 * f, f * 4 * 272>(kwa);
        else if constexpr (f < 7) lds_rd_a<A_K + 16 * ktc + 4 * (f - 4), (f - 4) * 16>(kra);
      }
      SCHED;
    });
  });
  sfor<3>([&](auto GI) {
    constexpr int G = GI, s = 13 + G;
    constexpr int kt = a_kt(s), ktc = kt < 0 ? 0 : kt;
    sfor<48>([&](auto UI) {
      constexpr int u = UI;
      tmf(GI, UI);
      SCHED;
      if constexpr (u == 42 && s < 15) wait_vm<a_wait(s)>();
      if constexpr (G == 0 && u == 13) wait_vm<a_wait_w(14, 13, 7)>();
      if constexpr (G == 0 && u == 29) wait_vm<a_wait_w(15, 13, 15)>();
      aop(IC<((u & 1) ? 0 : A_S.op[s][u / 2])>{});
      if constexpr (G == 0 && u + 3 < 48) tperm(IC<u + 3>{});
      if constexpr (G == 0 && u == 45) nop<0>();
      if constexpr (G < 2 && u % 6 == 5 && u / 6 < 6) txrd(IC<(G < 2 ? G + 1 : 0)>{}, IC<u / 6>{});
      if constexpr (kt >= 0 && u % 6 == 3) {
        if constexpr (u / 6 < 4) lds_wr_a<A_K + 16 * ktc + 4 * (u / 6), (u / 6) * 4 * 272>(kwa);
        else if constexpr (u / 6 < 7) lds_rd_a<A_K + 16 * ktc + 4 * (u / 6 - 4), (u / 6 - 4) * 16>(kra);
      }
      if constexpr (kt >= 0 && u == 41) lds_rd_a<A_K + 16 * ktc + 12, 48>(kra);
      if constexpr (u == 47) wait_lgkm0();
      if constexpr (G > 0 && u >= 2) {
        constexpr int lo = (u - 2) * 34 / 45 < 34 ? (u - 2) * 34 / 45 : 34, hi = (u - 1) * 34 / 45 < 34 ? (u - 1) * 34 / 45 : 34;
        sfor<hi - lo>([&](auto KI) { qp(IC<2 * (G > 0 ? G - 1 : 0) + (lo + KI) / 17>{}, IC<(lo + KI) % 17>{}); });
      }
      SCHED;
    });
  });
  asm volatile("" ::"v"(pfd));
  STAMP(2);

  f32x4 sacc[2][6];
  u32x4 Pf[6][3];
  f32x4 oacc[2][8];
  f32x4 lacc[2];
  float mA = 0.f, mB = 0.f, li = 0.f;
  float ev[24];
  const float c = 0.08838834764831845f * 1.4426950408889634f;

  auto sm = [&](auto TI, auto JI) {
    constexpr int t = TI, j = JI;
    const f32x4(&S)[6] = sacc[t & 1];
    auto v = [&](int i) -> float { return S[i >> 2][i & 3]; };
    if constexpr (j == 0) {
      mA = max3(v(0), v(1), v(2));
      mB = max3(v(3), v(4), v(5));
    } else if constexpr (j < 5) {
      mA = max3(mA, v(4 * j + 2), v(4 * j + 3));
      mB = max3(mB, v(4 * j + 4), v(4 * j + 5));
    } else if constexpr (j == 5) {
      mA = __builtin_elementwise_maximum(max3(mA, v(22), v(23)), mB);
      xmax16(mA);
    } else if constexpr (j == 6) xmax32s(mA, -c);
    else if constexpr (j < 7 + 24) {
      constexpr int i = j - 7;
      ev[i] = __builtin_amdgcn_exp2f(__builtin_fmaf(v(i), c, mA));
    } else {
      constexpr int i = j - 31;
      Pf[t][i >> 2][i & 3] = pk_bf16(ev[2 * i], ev[2 * i + 1]);
    }
  };
  constexpr int SM_N = 43;
  const int vo = lane * 16;
  const int oa1 = lb + V1_BASE + w * 16384 + vo, oa2 = lb + w * 8192 + vo;
  u32x4 ot;
  auto oe = [&](auto TI, auto JI) {
    constexpr int t = TI, j = JI;
    if constexpr (j == 0) {
      asm volatile("" : "+v"(lacc[t & 1]));
      li = __builtin_amdgcn_rcpf(lacc[t & 1][0]);
    }
    else {
      constexpr int i = j - 1, sp = i / 5, e = i % 5;
      if constexpr (e < 4) {
        const f32x4& o = oacc[t & 1][2 * sp + (e >> 1)];
        ot[e] = pk_bf16(o[2 * (e & 1)] * li, __builtin_fmaf(o[2 * (e & 1) + 1], li, 0.f));
      } else {
        constexpr int fi = sp * 6 + TG[t >> 1][t & 1];
        if constexpr (fi < 16) lds_wr<fi * 1024>(oa1, ot);
        else lds_wr<(fi - 16) * 1024>(oa2, ot);
      }
    }
  };
  constexpr int OE_N = 21;
  const int vtr = (4 * g + ((lane >> 2) & 3)) * 256 +
                  (((lane & 3) ^ (((g & 1) << 3) | (((lane >> 3) & 1) << 2) | ((lane >> 2) & 1))) << 4) + 8 * (lane & 1);
  const int vt1 = lb + V1_BASE + w * 16384 + vtr, vt2 = lb + w * 8192 + vtr;
  auto vrd = [&](auto JI) {
    constexpr int j = JI, f = j / 6, t3 = (j % 6) >> 1, hh = j & 1, sp = f >> 1, h = f & 1;
    constexpr int x = (sp << 6) ^ (h << 3), d = A_V + 4 * (t3 * 8 + f) + 2 * hh;
    if constexpr (t3 < 2) lds_rdtr_a<d, (32 * t3 + 16 * hh) * 256>(vt1 ^ x);
    else lds_rdtr_a<d, 16 * hh * 256>(vt2 ^ x);
  };
  const int vwoA = (8 * g + 4 * (g & 1)) * 1024 + (w * 128 + 8 * cchunk) * 2;
  const int vwoB = (8 * g - 4 * (g & 1) + 4) * 1024 + (w * 128 + 8 * cchunk) * 2;
  u32x4 WC[16][8];
  int wso[16];
  auto wsof = [&](auto SI) {
    constexpr int S = SI;
    wso[S] = (((w + (S >> 2)) & 3) * 128 + 32 * (S & 3)) * 1024;
    asm volatile("" : "+s"(wso[S]));
  };
  auto wold = [&](auto SI, auto II) {
    constexpr int S = SI, i = II;
    if constexpr (S < 3 && i == 0) wsof(SI);
    ld_w<(i & 3) * 1024>(WC[S][i], ro, i < 4 ? vwoA : vwoB, wso[S]);
  };

  sfor<6>([&](auto TI) {
    constexpr int t = TI;
    constexpr int NQ = t == 0 ? 34 : 0, NS = t > 0 ? SM_N : 0, NV = t == 5 ? 48 : 0;
    constexpr int NF = NQ + NS + NV, M0 = t == 0 ? 2 : 0;
    if constexpr (t == 5) wait_vm<(PFPV < 0 ? PF_WO : 0)>();
    sfor<24>([&](auto MI) {
      constexpr int m = MI, s = m / 6, kt = m % 6;
      mf_v_av<A_K + 4 * (kt * 4 + s), s == 0>(sacc[t & 1][kt], Qf[t][s]);
      SCHED;
      if constexpr (m % 8 == 2 && t < 4 && 3 * t + m / 8 < B_NV) vdma(IC<B_V[(3 * t + m / 8) < B_NV ? 3 * t + m / 8 : 0]>{});
      if constexpr (t == (PFPV < 0 ? B_PFT : PFPV - 1) && m % 3 == 0 && m / 3 < PF_WO) ld_pf<A_B + 4 * (m / 3)>(ro, vk, (32 * b + 8 * w + m / 3) * 1024);
      constexpr int m0 = m < M0 ? 0 : m - M0, m1 = m + 1 < M0 ? 0 : m + 1 - M0;
      constexpr int lo = m0 * NF / 22 < NF ? m0 * NF / 22 : NF, hi = m1 * NF / 22 < NF ? m1 * NF / 22 : NF;
      sfor<hi - lo>([&](auto KI) {
        constexpr int k = lo + KI;
        if constexpr (k < NS) sm(IC<t - 1>{}, IC<k>{});
        else if constexpr (k < NS + NQ) qp(IC<4 + (k - NS) / 17>{}, IC<(k - NS) % 17>{});
        else vrd(IC<k - NS - NQ>{});
      });
      SCHED;
    });
  });
  wait_lgkm0();
  STAMP(3);
  u32x4 ones = u32x4{0x3F803F80u, 0x3F803F80u, 0x3F803F80u, 0x3F803F80u};
  asm volatile("" : "+v"(ones));
  nop<1>();
  sfor<6>([&](auto TI) {
    constexpr int t = TI;
    constexpr int NS = t == 0 ? SM_N : 0, NO = t > 0 ? OE_N : 0, NW = t >= 3 ? 8 : 0;
    constexpr int NF = NS + NO + NW;
    sfor<27>([&](auto MI) {
      constexpr int m = MI, t3 = m / 9, f = m % 9;
      if constexpr (f > 0) mf_v_av<A_V + 4 * (t3 * 8 + f - 1), t3 == 0>(oacc[t & 1][f - 1], Pf[t][t3]);
      else mf_v_vv<t3 == 0>(lacc[t & 1], ones, Pf[t][t3]);
      SCHED;
      if constexpr (t == PFPV - 7 && m % 3 == 0 && m / 3 < PF_WO) ld_pf<A_B + 4 * (m / 3)>(ro, vk, (32 * b + 8 * w + m / 3) * 1024);
      constexpr int lo = m * NF / 27, hi = (m + 1) * NF / 27;
      sfor<hi - lo>([&](auto KI) {
        constexpr int k = lo + KI;
        if constexpr (k < NS) sm(IC<5>{}, IC<k>{});
        else if constexpr (k < NS + NO) oe(IC<t - 1>{}, IC<k - NS>{});
        else wold(IC<t - 3>{}, IC<k - NS - NO>{});
      });
      SCHED;
    });
  });
  sfor<OE_N>([&](auto J) { oe(IC<5>{}, J); });
  SCHED;
  STAMP(4);

  int ob[4][2];
#pragma unroll
  for (int h = 0; h < 4; ++h) {
    const int wp = (w + h) & 3;
    ob[h][0] = lb + V1_BASE + wp * 16384 + vo;
    ob[h][1] = lb + wp * 8192 + vo;
  }
  auto ordx = [&](auto SI, auto TI, auto DI_) {
    constexpr int S = SI, t = TI, d = DI_, fi = (S & 3) * 6 + t;
    if constexpr (fi < 16) lds_rd_a<d, fi * 1024>(ob[S >> 2][0]);
    else lds_rd_a<d, (fi - 16) * 1024>(ob[S >> 2][1]);
  };
  auto ord = [&](auto SI, auto TI) { ordx(SI, TI, IC<C_B + 24 * (SI & 1) + 4 * TI>{}); };
  auto tord = [&](auto PI) {
    constexpr int p = PI;
    ordx(IC<C_NS + C_P.bs[p]>{}, IC<C_P.bt[p]>{}, IC<C_B + 24 * ((C_NS + p / 6) & 1) + 4 * (p % 6)>{});
  };
  u32x4 CA[CT][8];
  auto cperm = [&](auto SI, auto KI) {
    constexpr int S = SI, k = KI;
    CA[S - C_NS][k >> 2][k & 3] = aperm(WC[S], k >> 2, k & 3);
  };
  wait_vm<16>();
  sfor<6>([&](auto T) { ord(IC<0>{}, T); });
  wsof(IC<3>{});
  SCHED;
#pragma unroll
  for (int pp = 0; pp < 4; ++pp) A[0][pp] = aperm(WC[0], 0, pp);
  SCHED;
  wait_lgkm0();
  nop<0>();
  SCHED;
  sfor<8 * C_NS>([&](auto NI) {
    constexpr int n = NI, s = n >> 3, f = n & 7;
    constexpr bool cv = s >= C_S0;
    if constexpr (n == 24) {
      barrier();
      SCHED;
    }
    sfor<6>([&](auto TI) {
      constexpr int t = TI;
      mf_a_va<4 * (8 * t + f), C_B + 24 * (s & 1) + 4 * t, s == 0>(A[n & 1]);
      SCHED;
      constexpr int v = 2 * f + (t >> 2), li = c_cum(s) + v, ln = c_cum(s) + 2 * f + 2;
      constexpr bool ld = (t == 3 || t == 5) && v < c_n(s);
      if constexpr (ld) wold(IC<li / 8>{}, IC<li % 8>{});
      if constexpr (t == 0) {
        if constexpr (f < 6) {
          if constexpr (s + 1 < C_NS) ord(IC<s + 1>{}, IC<f>{});
          else tord(IC<f>{});
        }
        if constexpr (f < 7 && ln % 8 == 0 && ln <= c_cum(s + 1)) wsof(IC<ln / 8>{});
        if constexpr (f == 1 && cv) wait_vm<2>();
        if constexpr (f == 7) {
          if constexpr (s + 1 < C_NS) wait_vm<c_cum(s + 1) - 8 * (s + 2)>();
          wait_lgkm0();
        }
      }
      if constexpr (n + 1 < 8 * C_NS && (t == 1 || t == 2)) {
        A[(n + 1) & 1][2 * t - 2] = aperm(WC[(n + 1) >> 3], (n + 1) & 7, 2 * t - 2);
        A[(n + 1) & 1][2 * t - 1] = aperm(WC[(n + 1) >> 3], (n + 1) & 7, 2 * t - 1);
      }
      constexpr int cg = f == 0 ? -1 : f < 4 ? (t == 4 ? f - 1 : -1) : t >= 3 ? 3 * (f - 3) + t - 3 : (f == 6 && t == 0 ? 15 : -1);
      if constexpr (cv && cg >= 0) {
        cperm(IC<(cv ? s + CT - 2 : C_NS)>{}, IC<2 * (cg < 0 ? 0 : cg)>{});
        cperm(IC<(cv ? s + CT - 2 : C_NS)>{}, IC<2 * (cg < 0 ? 0 : cg) + 1>{});
      }
      SCHED;
    });
  });
  const rsrc_t rp = __builtin_amdgcn_make_buffer_rsrc((void*)(part + (size_t)(((br * NBATCH + b) * 8 + hg) * 4 + w) * 24 * 64), 0,
                                                      24 * 1024, RSRC_W3);
  u32x4 so[2];
  float tb[8];
  auto stp = [&](auto MI) {
    constexpr int m = MI, t = C_P.ct[m / 52], r = (m % 52) / 13, e = m % 13;
    constexpr int rd[13] = {0, 1, 2, 3, -1, 4, 5, -1, 6, 7, -1, -1, -1}, pk[13] = {-1, -1, -1, -1, 0, -1, -1, 1, -1, -1, 2, 3, -1};
    if constexpr (rd[e] >= 0) asm volatile("v_accvgpr_read_b32 %0, a[%c1]" : "=v"(tb[rd[e]]) : "i"(4 * (8 * t + rd[e]) + r));
    else if constexpr (pk[e] >= 0) so[r & 1][pk[e]] = pk_bf16(tb[2 * pk[e]], tb[2 * pk[e] + 1]);
    else __builtin_amdgcn_raw_buffer_store_b128(so[r & 1], rp, vo + r * 1024, t * 4096, AUX_PST);
    if constexpr (e == 2 && m >= 13) asm volatile("" ::"v"(so[(r & 1) ^ 1]));
    SCHED;
  };
  sfor<48 * CT>([&](auto UI) {
    constexpr int u = UI, p = u >> 3, f = u & 7;
    mf_a_va<4 * (8 * C_P.bt[p] + f), C_B + 24 * ((C_NS + p / 6) & 1) + 4 * (p % 6), false>(CA[C_P.bs[p]][f]);
    SCHED;
    sfor<3>([&](auto KI) {
      constexpr int op = C_P.op[u][KI], j = op % 1000;
      if constexpr (op >= TWL) wait_lgkm0();
      else if constexpr (op >= TWV) wait_vm<j>();
      else if constexpr (op >= TS) stp(IC<j>{});
      else if constexpr (op >= TB) tord(IC<j>{});
      else if constexpr (op >= TP) cperm(IC<14 + j / 32>{}, IC<j % 32>{});
      else if constexpr (op >= TL) wold(IC<15>{}, IC<j>{});
      SCHED;
    });
  });
  STAMP(5);
  nop<7>();
  sfor<6 * 52 - C_P.nst>([&](auto KI) { stp(IC<C_P.nst + KI>{}); });
  STAMP(6);
}

__global__ __launch_bounds__(256) void k2_reduce(const u32x4* __restrict__ part, unsigned short* __restrict__ out) {
  const int i = blockIdx.x * 256 + threadIdx.x;
  const int bb = i / 6144, e = i % 6144;
  const rsrc_t rp = __builtin_amdgcn_make_buffer_rsrc((void*)(part + (size_t)bb * 8 * 6144), 0, 8 * 6144 * 16, RSRC_W3);
  u32x4 v[8];
#pragma unroll
  for (int h = 0; h < 8; ++h) v[h] = __builtin_amdgcn_raw_buffer_load_b128(rp, e * 16, h * 6144 * 16, AUX_PLD);
  float s[8];
#pragma unroll
  for (int j = 0; j < 8; ++j) s[j] = 0.f;
#pragma unroll
  for (int h = 0; h < 8; ++h)
#pragma unroll
    for (int j = 0; j < 4; ++j) {
      s[2 * j] += __builtin_bit_cast(float, v[h][j] << 16);
      s[2 * j + 1] += __builtin_bit_cast(float, v[h][j] & 0xffff0000u);
    }
  u32x4 o;
#pragma unroll
  for (int j = 0; j < 4; ++j) o[j] = pk_bf16(s[2 * j], s[2 * j + 1]);
  const int lane = e & 63, pc = e >> 6, r = pc & 3, t = (pc >> 2) % 6, w = pc / 24;
  const int q = lane & 15, g = lane >> 4;
  const int br = bb >> 4, b = bb & 15;
  *(u32x4*)(out + ((size_t)b * SQ + 16 * t + q) * 1024 + br * 512 + 128 * w + 32 * g + 8 * r) = o;
  asm volatile(".Lk2_end:");
}

extern "C" void kernel_launch(void* const* d_in, const int* in_sizes, int n_in, void* d_out, int out_size, void* d_ws,
                              size_t ws_size, hipStream_t stream) {
  Ptrs p;
  for (int br = 0; br < 2; ++br) {
    p.x[br] = (const unsigned short*)d_in[5 * br + 0];
    p.k[br] = (const unsigned short*)d_in[5 * br + 1];
    p.v[br] = (const unsigned short*)d_in[5 * br + 2];
    p.wq[br] = (const unsigned short*)d_in[5 * br + 3];
    p.wo[br] = (const unsigned short*)d_in[5 * br + 4];
  }
  u32x4* part = (u32x4*)d_ws;
  (void)hipFuncSetAttribute((const void*)k1_fused, hipFuncAttributeMaxDynamicSharedMemorySize, LDS_BYTES);
  k1_fused<<<256, 256, LDS_BYTES, stream>>>(p, part
  );
  k2_reduce<<<2 * 16 * 96 * 64 / 256, 256, 0, stream>>>(part, (unsigned short*)d_out);
}
```

```cpp
#include <hip/hip_runtime.h>
#include <hip/hip_bf16.h>
#include <utility>

typedef unsigned u32x2 __attribute__((ext_vector_type(2)));
typedef unsigned u32x4 __attribute__((ext_vector_type(4)));
typedef float f32x4 __attribute__((ext_vector_type(4)));
#define LDSP __attribute__((address_space(3)))
#define DI __device__ __forceinline__
typedef __amdgpu_buffer_rsrc_t rsrc_t;

constexpr int SQ = 96, DM = 512, NH = 32, DH = 128, NBATCH = 16;
constexpr int RSRC_W3 = 0x00020000;
#define POL_KV " nt"
#define POL_W ""
#define POL_PF ""
#define POL_X ""
#define AUX_PST 16
#define AUX_PLD 0

template <class F, int... I>
DI void sfor_impl(F&& f, std::integer_sequence<int, I...>) {
  (f(std::integral_constant<int, I>{}), ...);
}
template <int N, class F>
DI void sfor(F&& f) {
  sfor_impl(f, std::make_integer_sequence<int, N>{});
}
template <int N>
using IC = std::integral_constant<int, N>;

typedef float f32x2 __attribute__((ext_vector_type(2)));
typedef __bf16 bf16x2 __attribute__((ext_vector_type(2)));
DI unsigned pk_bf16(float lo, float hi) {
  return __builtin_bit_cast(unsigned, __builtin_convertvector(f32x2{lo, hi}, bf16x2));
}
DI unsigned perm_lo(unsigned e, unsigned o) { return __builtin_amdgcn_perm(o, e, 0x05040100u); }
DI unsigned perm_hi(unsigned e, unsigned o) { return __builtin_amdgcn_perm(o, e, 0x07060302u); }
#define SCHED __builtin_amdgcn_sched_barrier(0)

#define MF "v_mfma_f32_16x16x32_bf16 "
template <int D, int Bq, bool Z>
DI void mf_a_va(const u32x4& A) {
  if constexpr (Z) asm volatile(MF "a[%c0:%c1], %2, a[%c3:%c4], 0" ::"i"(D), "i"(D + 3), "v"(A), "i"(Bq), "i"(Bq + 3));
  else asm volatile(MF "a[%c0:%c1], %2, a[%c3:%c4], a[%c0:%c1]" ::"i"(D), "i"(D + 3), "v"(A), "i"(Bq), "i"(Bq + 3));
}
template <int Bq, bool Z>
DI void mf_v_va(f32x4& C, const u32x4& A) {
  if constexpr (Z) asm volatile(MF "%0, %1, a[%c2:%c3], 0" : "=&v"(C) : "v"(A), "i"(Bq), "i"(Bq + 3));
  else asm volatile(MF "%0, %1, a[%c2:%c3], %0" : "+v"(C) : "v"(A), "i"(Bq), "i"(Bq + 3));
}
template <int Aq, bool Z>
DI void mf_v_av(f32x4& C, const u32x4& Bv) {
  if constexpr (Z) asm volatile(MF "%0, a[%c2:%c3], %1, 0" : "=&v"(C) : "v"(Bv), "i"(Aq), "i"(Aq + 3));
  else asm volatile(MF "%0, a[%c2:%c3], %1, %0" : "+v"(C) : "v"(Bv), "i"(Aq), "i"(Aq + 3));
}
template <bool Z>
DI void mf_v_vv(f32x4& C, const u32x4& A, const u32x4& Bv) {
  if constexpr (Z) asm volatile(MF "%0, %1, %2, 0" : "=&v"(C) : "v"(A), "v"(Bv));
  else asm volatile(MF "%0, %1, %2, %0" : "+v"(C) : "v"(A), "v"(Bv));
}
template <int X, int Y>
DI unsigned pk_acc() {
  unsigned r, t0, t1;
  asm volatile("v_accvgpr_read_b32 %1, a[%c3]\n\tv_accvgpr_read_b32 %2, a[%c4]\n\tv_cvt_pk_bf16_f32 %0, %1, %2"
               : "=v"(r), "=&v"(t0), "=&v"(t1)
               : "i"(X), "i"(Y));
  return r;
}
template <int IMM>
DI void ld_w(u32x4& d, rsrc_t r, int voff, int soff) {
  asm volatile("buffer_load_dwordx4 %0, %1, %2, %3 offen offset:%c4" POL_W : "=v"(d) : "v"(voff), "s"(r), "s"(soff), "i"(IMM));
}
template <int D, int IMM>
DI void ld_k(rsrc_t r, int voff, int soff) {
  asm volatile("buffer_load_dwordx4 a[%c0:%c1], %2, %3, %4 offen offset:%c5" POL_KV ::"i"(D), "i"(D + 3), "v"(voff), "s"(r),
               "s"(soff), "i"(IMM));
}
template <int D>
DI void ld_pf(rsrc_t r, int voff, int soff) {
  asm volatile("buffer_load_dwordx4 a[%c0:%c1], %2, %3, %4 offen" POL_PF ::"i"(D), "i"(D + 3), "v"(voff), "s"(r), "s"(soff));
}
DI void ld_pfv(u32x4& d, rsrc_t r, int voff, int soff) {
  asm volatile("buffer_load_dwordx4 %0, %1, %2, %3 offen" POL_PF : "+v"(d) : "v"(voff), "s"(r), "s"(soff));
}
template <int C>
DI void set_m0(int base) {
  asm volatile("s_add_i32 m0, %0, %c1" ::"s"(base), "i"(C) : "scc", "memory");
}
template <int IMM>
DI void dma_x(rsrc_t r, int voff, int soff) {
  asm volatile("buffer_load_dwordx4 %0, %1, %2 offen offset:%c3" POL_X " lds" ::"v"(voff), "s"(r), "s"(soff), "i"(IMM) : "memory");
}
template <int IMM>
DI void dma_v(rsrc_t r, int voff) {
  asm volatile("buffer_load_dwordx4 %0, %1, m0 offen offset:%c2" POL_KV " lds" ::"v"(voff), "s"(r), "i"(IMM) : "memory");
}
template <int D, int IMM>
DI void lds_rd_a(int addr) {
  asm volatile("ds_read_b128 a[%c0:%c1], %2 offset:%c3" ::"i"(D), "i"(D + 3), "v"(addr), "i"(IMM) : "memory");
}
template <int D, int IMM>
DI void lds_rdtr_a(int addr) {
  asm volatile("ds_read_b64_tr_b16 a[%c0:%c1], %2 offset:%c3" ::"i"(D), "i"(D + 1), "v"(addr), "i"(IMM) : "memory");
}
template <int D, int IMM>
DI void lds_wr_a(int addr) {
  asm volatile("ds_write_b128 %2, a[%c0:%c1] offset:%c3" ::"i"(D), "i"(D + 3), "v"(addr), "i"(IMM) : "memory");
}
template <int IMM>
DI void lds_wr(int addr, const u32x4& d) {
  asm volatile("ds_write_b128 %0, %1 offset:%c2" ::"v"(addr), "v"(d), "i"(IMM) : "memory");
}
template <int N>
DI void wait_vm() {
  static_assert(N >= 0 && N < 64, "vmcnt");
  asm volatile("s_waitcnt vmcnt(%c0)" ::"i"(N) : "memory");
}
DI void wait_lgkm0() { asm volatile("s_waitcnt lgkmcnt(0)" ::: "memory"); }
template <int N>
DI void wait_lgkm() {
  static_assert(N >= 0 && N < 16, "lgkmcnt");
  asm volatile("s_waitcnt lgkmcnt(%c0)" ::"i"(N) : "memory");
}
DI void barrier() { asm volatile("s_barrier" ::: "memory"); }
template <int N>
DI void nop() {
  asm volatile("s_nop %c0" ::"i"(N));
}
DI float max3(float a, float b, float c) { return __builtin_elementwise_maximum(__builtin_elementwise_maximum(a, b), c); }
DI void xmax16(float& a) {
  float t;
  asm volatile("v_mov_b32 %1, %0\n\ts_nop 1\n\tv_permlane16_swap_b32 %0, %1\n\tv_max_f32 %0, %0, %1" : "+v"(a), "=&v"(t));
}
DI void xmax32s(float& a, float s) {
  float t;
  asm volatile("v_mov_b32 %1, %0\n\ts_nop 1\n\tv_permlane32_swap_b32 %0, %1\n\tv_max_f32 %0, %0, %1\n\tv_mul_f32 %0, %2, %0"
               : "+v"(a), "=&v"(t)
               : "s"(s));
}
template <int X, int Y>
DI void acc_rd2(float& a, float& b) {
  asm volatile("v_accvgpr_read_b32 %0, a[%c2]\n\tv_accvgpr_read_b32 %1, a[%c3]" : "=v"(a), "=v"(b) : "i"(X), "i"(Y));
}

#define STAMP(i)

#define BSTAMP(i)
#define HALFX(n) (n)
struct Ptrs {
  const unsigned short* x[2];
  const unsigned short* k[2];
  const unsigned short* v[2];
  const unsigned short* wq[2];
  const unsigned short* wo[2];
};

constexpr int LDS_BYTES = 163840;
constexpr int XQ_BYTES = 24576, V1_BASE = 98304;

constexpr int A_B = 112, A_K = 160, A_V = 0;
constexpr bool tile_v(int f, int t) { return t < 2 || (t == 2 && f < 4); }
constexpr int tile_vi(int f, int t) { return t * 8 + f; }
constexpr int tile_a(int f, int t) { return 4 * (t >= 3 ? (t - 3) * 8 + f : 24 + (f - 4)); }

#define PF_WO 8
enum { OX = 1000, OK_ = 2000, OP = 3000, OV = 4000, OW = 5000 };
struct ASched {
  int op[16][24];
  bool bar[16];
};
#define SCHV 5
#define PFPV 6
#define STAG 16
#define X6(q) OX + 6 * q, OX + 6 * q + 1, OX + 6 * q + 2, OX + 6 * q + 3, OX + 6 * q + 4, OX + 6 * q + 5
#define K4(t) OK_ + 4 * t, OK_ + 4 * t + 1, OK_ + 4 * t + 2, OK_ + 4 * t + 3
#define V4(j) OV + j, OV + j + 1, OV + j + 2, OV + j + 3
#define PRO_OP 2
constexpr int A_BURST[6][12] = {{X6(1), OP + 4}, {X6(2), OP + 5}, {OP + 6, K4(0), OK_ + 4, OK_ + 5},
                                {X6(3), OP + 7}, {OK_ + 6, OK_ + 7, K4(2), OK_ + 12}, {OK_ + 13, OK_ + 14, OK_ + 15, K4(4)}};
constexpr int A_TAIL[3][8] = {{K4(5), OV + 0, OV + 1, OV + 2}, {OV + 3, V4(4), OV + 8}, {OV + 9, OV + 10, OV + 16, OV + 17, OV + 18}};
constexpr int A_KST[6] = {8, 11, 12, 13, 14, 15};
constexpr int B_V[] = {19, 20, 21, 22, 23, 11, 12, 13, 14, 15};
constexpr int B_NV = 10, B_PFT = 4;
constexpr ASched a_make() {
  ASched a{};
  for (int s = 0; s <= 12; s += 2)
    for (int i = 0; i < 16; ++i) a.op[s][i] = OW + 8 * (s + 2) + i;
  for (int s = 1; s <= 11; s += 2) {
    a.bar[s] = true;
    for (int i = 0; i < (int)(sizeof(A_BURST[0]) / sizeof(int)); ++i) a.op[s][i] = A_BURST[s >> 1][i];
  }
  for (int s = 13; s < 16; ++s) {
    int n = 0;
    while (n < 8 && A_TAIL[s - 13][n]) ++n;
    for (int i = 0; i < n; ++i) a.op[s][3 * (i * 8 / n)] = A_TAIL[s - 13][i];
  }
  return a;
}
constexpr ASched A_S = a_make();
constexpr int A_NP = 3 + 6 + PRO_OP + 16;
constexpr int a_seq(int op) {
  if (op >= OW && op < OW + 16) return 9 + PRO_OP + op - OW;
  int c = A_NP;
  for (int s = 0; s < 16; ++s)
    for (int k = 0; k < 24; ++k)
      if (A_S.op[s][k]) {
        if (A_S.op[s][k] == op) return c;
        ++c;
      }
  return -1;
}
constexpr int a_issued(int s, int k) {
  int c = A_NP;
  for (int i = 0; i < 16; ++i)
    for (int j = 0; j < 24; ++j)
      if (A_S.op[i][j] && (i < s || (i == s && j < k))) ++c;
  return c;
}
constexpr int a_kt(int s) {
  for (int k = 0; k < 6; ++k)
    if (A_KST[k] == s) return k;
  return -1;
}
constexpr int imax(int a, int b) { return a > b ? a : b; }
constexpr int a_need(int s) {
  int n = 0;
  for (int i = 0; i < 8; ++i) n = imax(n, a_seq(OW + 8 * (s + 1) + i));
  if (s == 2 || s == 6 || s == 10)
    for (int i = 0; i < 6; ++i) n = imax(n, a_seq(OX + 6 * ((s + 2) / 4) + i));
  if (a_kt(s + 1) >= 0)
    for (int i = 0; i < 4; ++i) n = imax(n, a_seq(OK_ + 4 * a_kt(s + 1) + i));
  if (s == 14)
    for (int i = 2; i < 8; ++i) n = imax(n, a_seq(OP + i));
  return n;
}
constexpr int a_wait(int s) { return a_issued(s, s < 13 ? 14 : 21) - a_need(s) - 1; }
constexpr int v_jb(int j) {
  int jb = -99;
  for (int i = 0; i < 16 * 24 + B_NV; ++i) {
    const int op = i < 16 * 24 ? A_S.op[i / 24][i % 24] : OV + B_V[i - 16 * 24];
    if (op >= OX && op < OK_) jb = -99;
    if (op < OV || op >= OW) continue;
    const int v = op - OV;
    if (!(v >= jb && v - jb < 4 && (v < 16) == (jb < 16))) jb = v;
    if (v == j) return jb;
  }
  return -1;
}
constexpr int a_wait_w(int ws, int s, int k) {
  int n = 0;
  for (int i = 0; i < 8; ++i) n = imax(n, a_seq(OW + 8 * ws + i));
  return a_issued(s, k) - n - 1;
}
constexpr bool a_check() {
  for (int i = 0; i < 24; ++i)
    if (a_seq(OK_ + i) < 0) return false;
  for (int i = 6; i < 24; ++i)
    if (a_seq(OX + i) < 0) return false;
  for (int i = 0; i < 128; ++i)
    if (a_seq(OW + i) < 0 || (i >= 32 && a_seq(OW + i) < a_issued(i / 8 - 3, 0))) return false;
  int nv = B_NV;
  for (int i = 0; i < 24; ++i)
    if (a_seq(OV + i) >= 0) {
      ++nv;
      if (i >= 11 && i < 16 && a_seq(OV + i) < a_issued(A_KST[5] + 1, 0)) return false;
      if (i >= 16 && a_seq(OV + i) < a_issued(7, 0)) return false;
    }
  for (int s = 0; s < 15; ++s)
    if (a_wait(s) < 0 || a_wait(s) > 63) return false;
  return nv == 24 && A_S.bar[3] && A_S.bar[7] && A_S.bar[11];
}
static_assert(a_check(), "phase A schedule");

#define CT 6
constexpr int C_NS = 16 - CT, C_S0 = C_NS - CT + 2, C_B = 192;
constexpr int c_n(int s) { return s < C_S0 && 4 * s < 8 * (CT - 4) ? 12 : 8; }
constexpr int c_cum(int s) {
  int c = 24;
  for (int i = 0; i < s; ++i) c += c_n(i);
  return c;
}
static_assert(CT >= 4 && CT <= 6 && c_cum(C_NS) == 120, "phase C schedule");
enum { TL = 1000, TP = 2000, TB = 3000, TS = 4000, TWV = 5000, TWL = 6000 };
struct CPlan {
  int bt[6 * CT], bs[6 * CT];
  int ct[6];
  int op[48 * CT][3];
  int nst;
};
constexpr CPlan c_make() {
  CPlan c{};
  int nx[6] = {0, 0, 0, 0, 0, 0}, done[6] = {0, 0, 0, 0, 0, 0}, nc = 0;
  for (int p = 0; p < 6 * CT; ++p)
    for (int t = 0; t < 6; ++t)
      if (nx[t] < CT && (nx[t] < CT - 2 || (nx[t] == CT - 2 && p >= 3) || p >= 6)) {
        c.bt[p] = t;
        c.bs[p] = nx[t]++;
        if (nx[t] == CT) {
          c.ct[nc] = t;
          done[nc++] = 8 * p + 7;
        }
        break;
      }
  int used[48 * CT] = {};
  auto put = [&](int u, int op, int w) {
    int k = 0;
    while (c.op[u][k]) ++k;
    c.op[u][k] = op;
    used[u] += w;
  };
  for (int i = 0; i < 8; ++i) put(i, TL + i, 2);
  put(8, TWV + 8, 0);
  put(32, TWV + 0, 0);
  for (int k = 0; k < 32; ++k) {
    put(8 + k / 2, TP + k, 1);
    put(32 + k / 2, TP + 32 + k, 1);
  }
  for (int r = 0; r + 1 < CT; ++r) {
    for (int j = 0; j < 6; ++j) put(r ? 48 * r + 6 * j : 24 + j, TB + 6 * (r + 1) + j, 1);
    put(48 * r + 46, TWL, 0);
  }
  int u = 0, m = 0;
  for (; m < 6 * 52; ++m) {
    const int w = m % 13 == 12 ? 2 : 1;
    if (u < done[m / 52] + 2) u = done[m / 52] + 2;
    while (u < 48 * CT && used[u] + w > 2) ++u;
    if (u >= 48 * CT) break;
    put(u, TS + m, w);
  }
  c.nst = m;
  return c;
}
constexpr CPlan C_P = c_make();

constexpr int pv_t3(int t, int m) { return t == 0 ? (m < 9 ? 0 : m < 18 ? 2 : 1) : (m < 9 ? m / 3 : ((m - 9) % 6) / 2); }
constexpr int pv_f(int t, int m) { return t == 0 ? m % 9 : (m < 9 ? m % 3 : 3 + 2 * ((m - 9) / 6) + (m - 9) % 2); }
constexpr bool pv_z(int t, int m) { return t == 0 ? m < 9 : pv_t3(t, m) == 0; }
constexpr int pv_first(int t, int f) {
  for (int m = 0; m < 27; ++m)
    if (pv_f(t, m) == f) return 27 * t + m;
  return -1;
}
constexpr int pv_last(int t, int f) {
  for (int m = 26; m >= 0; --m)
    if (pv_f(t, m) == f) return 27 * t + m;
  return -1;
}
#define PV_R 177
constexpr int SM_N = 43, OE_N = 53, PV_N = 162;
constexpr int E_RD[13] = {0, 1, 2, 3, -1, 4, 5, -1, 6, 7, -1, -1, -1}, E_PK[13] = {-1, -1, -1, -1, 0, -1, -1, 1, -1, -1, 2, 3, -1};
enum { PS = 1000, PVR = 2000, PO = 3000, PW = 4000, PR = 5000, PP = 6000, PX = 7000, PB = 7500, PWV = 8000, PWL = 9000 };
constexpr int PV_X = 3;
constexpr int c_rdt(int s, int f) { return s < PV_X ? (f < 4 ? f + 2 : f - 5) : (f < 6 ? f : -1); }
struct PPlan {
  int op[PV_N + 48 * PV_X][12];
  bool ok;
};
constexpr PPlan p_make() {
  PPlan c{};
  int it[512] = {}, cost[512] = {}, rdy[512] = {}, dl[512] = {}, n = 0;
  auto add = [&](int op, int w, int r, int d) {
    it[n] = op;
    cost[n] = w;
    rdy[n] = r;
    dl[n++] = d;
  };
  auto piece = [&](int t, int sp, int nt) {
    for (int e = 0; e < 13; ++e) {
      const int f = 1 + 2 * sp + (E_RD[e] >> 2);
      if (e == 12 && nt >= 0) add(PO + 64 * nt, 2, pv_last(nt, 0) + 3, nt < 4 ? pv_first(nt + 2, 0) : 999);
      add(PO + 64 * t + 1 + 13 * sp + e, 1, E_RD[e] >= 0 ? pv_last(t, f) + 3 : 0,
          t < 4 ? (E_RD[e] >= 0 ? pv_first(t + 2, f) : PV_N) : (sp ? PV_N + 48 * (sp - 1) + 6 * (t + 1) : PV_N));
    }
  };
  for (int j = 0; j < SM_N; ++j) {
    if (j == SM_N - 1) add(PO, 2, pv_last(0, 0) + 3, pv_first(2, 0));
    add(PS + j, j < 5 ? 2 : j < 7 ? 6 : j < 31 ? 3 : 1, 0, 27 * 5 - 1);
  }
  for (int t = 0; t < 4; ++t)
    for (int sp = 0; sp < 4; ++sp) piece(t, sp, sp == 3 ? t + 1 : -1);
  piece(4, 0, -1);
  piece(4, 1, 5);
  piece(5, 0, -1);
  for (int k = 0; k < 6; ++k) add(PR + (k + 2) % 6, 1, 0, PV_N - 3);
  add(PWV + 99, 0, 0, PV_N - 1);
  for (int k = 0; k < 4; ++k) add(PP + k, 1, 0, PV_N - 1);
  add(PX, 1, 0, PV_N);
  piece(5, 1, -1);
  for (int sp = 2; sp < 4; ++sp) {
    piece(4, sp, -1);
    piece(5, sp, -1);
  }
  for (int k = 2; k < 8; ++k) add(PB + k, 1, 0, PV_N + 48 * PV_X);
  int placed = 0, idx = 0, nw = 0, nds = 0;
  c.ok = true;
  for (int g = 0; g < PV_N + 48 * PV_X; ++g) {
    int k = 0, used = 0;
    auto put = [&](int op, int w) {
      c.op[g][k++] = op;
      placed += w;
      used += w;
    };
    if (g == 1) {
      put(PWL + 0, 0);
      put(PWV + PF_WO, 0);
    }
    for (int i = 0; i < 16; ++i)
      if (g == 2 + i * 12 / 16) put(PVR + i, 1);
    if (g == 17) put(PWL + 0, 0);
    for (int i = 0; i < 24; ++i)
      if (g == 81 + i * 80 / 24) {
        put(PW + i, i % 8 ? 2 : 3);
        ++nw;
      }
    const int cs = (g - PV_N) / 48, cf = (g - PV_N) % 48 / 6, ctt = (g - PV_N) % 6;
    const int cap = g < PV_N ? 99 : (ctt == 4 || ((ctt == 3 || ctt == 5) && 2 * cf + (ctt >> 2) >= c_n(cs)) ? 2 : 0);
    while (idx < n && rdy[idx] <= g && used + cost[idx] <= cap && (g >= PV_N || placed * 64 < PV_R * (g + 1))) {
      const int op = it[idx];
      if (g >= dl[idx]) c.ok = false;
      if (op >= PR && op < PP) nds = 0;
      else if (op >= PO && op < PW && (op - PO) % 64 > 0 && ((op - PO) % 64 - 1) % 13 == 12) ++nds;
      if (op == PWV + 99 && nw < 8) c.ok = false;
      put(op == PWV + 99 ? PWV + nw - 8 : op, cost[idx]);
      ++idx;
    }
    if (g == PV_N - 1) put(PWL + nds, 0);
    if (k > 11) c.ok = false;
  }
  c.ok = c.ok && idx == n && nw == 24;
  return c;
}
constexpr PPlan P_P = p_make();
static_assert(P_P.ok, "PV schedule");

DI unsigned aperm(const u32x4 (&W)[8], int f, int p) {
  return (f & 1) ? perm_hi(W[2 * p][f >> 1], W[2 * p + 1][f >> 1]) : perm_lo(W[2 * p][f >> 1], W[2 * p + 1][f >> 1]);
}

__global__ __launch_bounds__(256, 1) void k1_fused(Ptrs p, u32x4* part
) {
  extern __shared__ __attribute__((aligned(16))) char lds[];
  asm volatile("" ::: "a0", "a255");
  const int bid = blockIdx.x;
  const int xcd = bid & 7, idx = bid >> 3;
  const int br = xcd >> 2, hg = (xcd & 3) * 2 + (idx & 1), b = idx >> 1;
  const int tid = threadIdx.x, lane = tid & 63;
  const int w = __builtin_amdgcn_readfirstlane(tid >> 6);
  const int q = lane & 15, g = lane >> 4;
  const int cchunk = q;
  const int lb = (int)(unsigned)(size_t)(LDSP char*)lds;
  STAMP(0);

  const int vk = lane * 16;
  u32x4 pfd = u32x4{0, 0, 0, 0};
  {
    unsigned long long pc;
    int len;
    asm volatile("s_getpc_b64 %0\n.Lk1_pc:\n\ts_mov_b32 %1, .Lk2_end-.Lk1_pc" : "=s"(pc), "=s"(len));
    const rsrc_t rc = __builtin_amdgcn_make_buffer_rsrc((void*)pc, 0, len, RSRC_W3);
    nop<4>();
    ld_pfv(pfd, rc, vk, (idx * 4 + w) * 1024);
  }
  const unsigned short *px = p.x[br], *pk = p.k[br], *pv = p.v[br], *pq = p.wq[br], *po = p.wo[br];
  asm volatile("" : "+s"(px), "+s"(pk), "+s"(pv), "+s"(pq), "+s"(po), "+s"(part));
  const int head = hg * 4 + w;
  const size_t kvoff = ((size_t)b * NH + head) * SQ * DH;
  const rsrc_t rx = __builtin_amdgcn_make_buffer_rsrc((void*)(px + (size_t)b * SQ * DM), 0, HALFX(SQ * DM * 2), RSRC_W3);
  const rsrc_t rk = __builtin_amdgcn_make_buffer_rsrc((void*)(pk + kvoff), 0, HALFX(SQ * DH * 2), RSRC_W3);
  const rsrc_t rq = __builtin_amdgcn_make_buffer_rsrc((void*)pq, 0, DM * NH * DH * 2, RSRC_W3);
  const rsrc_t ro = __builtin_amdgcn_make_buffer_rsrc((void*)(po + (size_t)hg * 512 * DM), 0, 512 * DM * 2, RSRC_W3);

  f32x4 accv[20];
  u32x4 W[4][8];
  u32x4 A[2];

  auto gmf = [&](auto FI, auto TI, auto BI, auto ZI, const u32x4& Af) {
    constexpr int f = FI, t = TI, bq = A_B + 24 * BI + 4 * t;
    constexpr bool z = ZI;
    if constexpr (tile_v(f, t)) mf_v_va<bq, z>(accv[tile_vi(f, t)], Af);
    else mf_a_va<tile_a(f, t), bq, z>(Af);
  };
  auto gpk = [&](auto PI, auto TI, auto RI) -> unsigned {
    constexpr int pp = PI, t = TI, r = RI;
    if constexpr (tile_v(2 * pp, t)) return pk_bf16(accv[tile_vi(2 * pp, t)][r], accv[tile_vi(2 * pp + 1, t)][r]);
    else return pk_acc<tile_a(2 * pp, t) + r, tile_a(2 * pp + 1, t) + r>();
  };

  const int vx = g * 1024 + ((q ^ g ^ (4 * w)) << 4);
  const int vwq = (8 * g) * 8192 + (hg * 512 + w * 128 + 8 * cchunk) * 2;
  int sxb = lb + w * 1024, sxo[6];
#pragma unroll
  for (int i = 0; i < 6; ++i) sxo[i] = (4 * i + w) * 4096;
  asm volatile("" : "+s"(sxb), "+s"(sxo[0]), "+s"(sxo[1]), "+s"(sxo[2]), "+s"(sxo[3]), "+s"(sxo[4]), "+s"(sxo[5]));
  auto xm0 = [&](auto II) { set_m0<(II / 6) * (XQ_BYTES - 256) + (II % 6) * 4096>(sxb); };
  auto xdma = [&](auto II) { dma_x<(II / 6) * 256>(rx, vx, sxo[II % 6]); };
  const int vpf = ((lane >> 5) * 32 + 2 * b + (w >> 1)) * 8192 + hg * 1024 + (w & 1) * 512 + (lane & 31) * 16;
  nop<4>();
  STAMP(8);
  ld_pfv(pfd, rq, vpf, 0);
  ld_pfv(pfd, rq, vpf, 64 * 8192);
  sfor<6>([&](auto I) {
    xm0(I);
    nop<0>();
    xdma(I);
  });
  sfor<PRO_OP>([&](auto I) { ld_pfv(pfd, rq, vpf, (2 + I) * 64 * 8192); });
  STAMP(9);
  wait_vm<7 + PRO_OP>();
  STAMP(10);
  sfor<16>([&](auto I) { ld_w<0>(W[I >> 3][I & 7], rq, vwq, (32 * (I >> 3) + (I & 7)) * 8192); });
  int xa[2][4];
#pragma unroll
  for (int h = 0; h < 2; ++h)
#pragma unroll
    for (int j = 0; j < 4; ++j) xa[h][j] = lb + h * 2 * XQ_BYTES + q * 256 + (((4 * j + g) ^ q) << 4);
  const int kst = lb + V1_BASE + w * 16384 + 16384 - 16 * 272;
  const int kwa = kst + g * 272 + q * 16, kra = kst + q * 272 + g * 64;
  const int vv0 = g * 256 + ((q ^ (((g >> 1) << 2) | (g & 1))) << 4);
  const int vv1 = vv0 ^ 128;
  int svb[2] = {lb + V1_BASE + w * 16384, lb + w * 8192};
  const rsrc_t rv0 = __builtin_amdgcn_make_buffer_rsrc((void*)((const char*)(pv + kvoff) - svb[0]), 0, HALFX(svb[0] + 16384), RSRC_W3);
  const rsrc_t rv1 = __builtin_amdgcn_make_buffer_rsrc((void*)((const char*)(pv + kvoff) + 16384 - svb[1]), 0, HALFX(svb[1] + 8192), RSRC_W3);
  asm volatile("" : "+s"(svb[0]), "+s"(svb[1]));
  auto vm0 = [&](auto JI) {
    constexpr int j = JI;
    if constexpr (v_jb(j) == j) set_m0<(j & 15) * 1024>(svb[j >> 4]);
  };
  auto vdma = [&](auto JI) {
    constexpr int j = JI;
    dma_v<(j - v_jb(j)) * 1024>(j < 16 ? rv0 : rv1, (j & 1) ? vv1 : vv0);
  };
  int sob[4][2];
#pragma unroll
  for (int h = 1; h < 4; ++h) {
    sob[h][0] = lb + V1_BASE + ((w + h) & 3) * 16384;
    sob[h][1] = lb + ((w + h) & 3) * 8192;
    asm volatile("" : "+s"(sob[h][0]), "+s"(sob[h][1]));
  }
  STAMP(11);
  SCHED;
  if (b & 1) __builtin_amdgcn_s_sleep(STAG);
  wait_vm<A_NP - 9>();
  barrier();
  STAMP(1);

  constexpr int TG[3][2] = {{4, 5}, {2, 3}, {0, 1}};
  u32x4 AT[3][8];
  auto tperm = [&](auto KI) {
    constexpr int k = KI, m = k >> 1, d = 2 * (k & 1);
    AT[m >> 3][m & 7][d] = aperm(W[(13 + (m >> 3)) & 3], m & 7, d);
    AT[m >> 3][m & 7][d + 1] = aperm(W[(13 + (m >> 3)) & 3], m & 7, d + 1);
  };
  auto tmf = [&](auto GI, auto UI) {
    constexpr int G = GI, u = UI, sp = u >> 4, f = (u >> 1) & 7, tt = u & 1, t = TG[G][tt];
    constexpr int bq = A_B + 24 * ((G & 1) ^ 1) + 4 * (2 * sp + tt);
    if constexpr (tile_v(f, t)) mf_v_va<bq, false>(accv[tile_vi(f, t)], AT[sp][f]);
    else mf_a_va<tile_a(f, t), bq, false>(AT[sp][f]);
  };
  u32x4 Qf[6][4];
  float ta[2][2];
  auto qp = [&](auto II, auto JI) {
    constexpr int qi = II, t = TG[qi >> 1][qi & 1], j = JI, pp = (j & 15) >> 2, s = j & 3, i = j > 0 ? j - 1 : 0;
    if constexpr (j < 16) {
      if constexpr (tile_v(2 * pp, t)) Qf[qi][s][pp] = gpk(IC<pp>{}, IC<t>{}, IC<s>{});
      else acc_rd2<tile_a(2 * pp, t) + s, tile_a(2 * pp + 1, t) + s>(ta[j & 1][0], ta[j & 1][1]);
    }
    if constexpr (j > 0 && !tile_v(2 * (i >> 2), t)) Qf[qi][i & 3][i >> 2] = pk_bf16(ta[i & 1][0], ta[i & 1][1]);
  };

  auto xrdx = [&](auto SI, auto TI, auto DI_) {
    constexpr int s = SI, t = TI;
    lds_rd_a<DI_, ((s >> 2) & 1) * XQ_BYTES + t * 4096>(xa[s >> 3][s & 3]);
  };
  auto xrd = [&](auto SI, auto TI) { xrdx(SI, TI, IC<A_B + 24 * (SI & 1) + 4 * TI>{}); };
  auto txrd = [&](auto GI, auto JI) {
    constexpr int G = GI, jj = JI;
    xrdx(IC<13 + jj / 2>{}, IC<TG[G][jj & 1]>{}, IC<A_B + 24 * ((G & 1) ^ 1) + 4 * jj>{});
  };
  int so[2];
  auto aprep = [&](auto OI, auto PI) {
    constexpr int op = OI, j = op % 1000, pi = PI;
    if constexpr (op >= OV && op < OW) vm0(IC<j>{});
    else if constexpr (op >= OK_) {
      so[pi] = op >= OW ? (32 * (j >> 3) + (j & 7)) * 8192 : op >= OP ? j * 64 * 8192 : (j >> 2) * 4096;
      asm volatile("" : "+s"(so[pi]));
    } else if constexpr (op >= OX) xm0(IC<j>{});
  };
  auto aop = [&](auto OI, auto PI) {
    constexpr int op = OI, j = op % 1000, pi = PI;
    if constexpr (op >= OW) ld_w<0>(W[(j >> 3) & 3][j & 7], rq, vwq, so[pi]);
    else if constexpr (op >= OV) vdma(IC<j>{});
    else if constexpr (op >= OP) ld_pfv(pfd, rq, vpf, so[pi]);
    else if constexpr (op >= OK_) ld_k<A_K + 4 * j, (j & 3) * 1024>(rk, vk, so[pi]);
    else if constexpr (op >= OX) xdma(IC<j>{});
  };
  sfor<6>([&](auto T) { xrd(IC<0>{}, T); });
  wait_vm<8>();
  SCHED;
#pragma unroll
  for (int pp = 0; pp < 4; ++pp) A[0][pp] = aperm(W[0], 0, pp);
  SCHED;
  wait_lgkm0();
  nop<0>();
  SCHED;
  sfor<104>([&](auto NI) {
    constexpr int n = NI, s = n >> 3, f = n & 7;
    constexpr int kt = a_kt(s), ktc = kt < 0 ? 0 : kt;
    if constexpr (f == 0 && A_S.bar[s]) {
      barrier();
      SCHED;
    }
    sfor<6>([&](auto TI) {
      constexpr int t = TI;
      gmf(IC<f>{}, TI, IC<(s & 1)>{}, std::bool_constant<s == 0>{}, A[n & 1]);
      SCHED;
      constexpr int opb = A_S.op[s][2 * f + 1];
      constexpr bool dmb = (opb >= OX && opb < OK_) || (opb >= OV && opb < OW);
      if constexpr (t == 0 && f == 7) wait_vm<a_wait(s)>();
      if constexpr (t == 0) aprep(IC<A_S.op[s][2 * f]>{}, IC<0>{});
      if constexpr (t == (dmb ? 4 : 0)) aprep(IC<opb>{}, IC<1>{});
      if constexpr (t == 4 && f < 6) {
        if constexpr (s < 12) xrd(IC<s + 1>{}, IC<f>{});
        else txrd(IC<0>{}, IC<f>{});
      }
      if constexpr (t == 3 || t == 5) aop(IC<A_S.op[s][2 * f + t / 4]>{}, IC<t / 4>{});
      if constexpr (t == 1 || t == 2) {
        if constexpr (n < 103) {
          A[(n + 1) & 1][2 * t - 2] = aperm(W[((n + 1) >> 3) & 3], (n + 1) & 7, 2 * t - 2);
          A[(n + 1) & 1][2 * t - 1] = aperm(W[((n + 1) >> 3) & 3], (n + 1) & 7, 2 * t - 1);
        } else tperm(IC<t - 1>{});
      }
      if constexpr (n == 103 && t == 4) tperm(IC<2>{});
      if constexpr (t == 4 && kt >= 0) {
        if constexpr (f < 4) lds_wr_a<A_K + 16 * ktc + 4 * f, f * 4 * 272>(kwa);
        else lds_rd_a<A_K + 16 * ktc + 4 * (f - 4), (f - 4) * 16>(kra);
      }
      if constexpr (t == 5 && f == 7) wait_lgkm<(kt >= 0 ? 2 : 0)>();
      SCHED;
    });
  });
  sfor<3>([&](auto GI) {
    constexpr int G = GI, s = 13 + G;
    constexpr int kt = a_kt(s), ktc = kt < 0 ? 0 : kt;
    sfor<48>([&](auto UI) {
      constexpr int u = UI;
      tmf(GI, UI);
      SCHED;
      if constexpr (u == 42 && s < 15) wait_vm<a_wait(s)>();
      if constexpr (G == 0 && u == 13) wait_vm<a_wait_w(14, 13, 6)>();
      if constexpr (G == 0 && u == 29) wait_vm<a_wait_w(15, 13, 14)>();
      if constexpr (u & 1) aop(IC<A_S.op[s][u / 2]>{}, IC<0>{});
      else aprep(IC<A_S.op[s][u / 2]>{}, IC<0>{});
      if constexpr (G == 0 && u + 3 < 48) tperm(IC<u + 3>{});
      if constexpr (G == 0 && u == 45) nop<0>();
      if constexpr (G < 2 && u % 6 == 5 && u / 6 < 6) txrd(IC<(G < 2 ? G + 1 : 0)>{}, IC<u / 6>{});
      if constexpr (kt >= 0 && u % 6 == 3) {
        if constexpr (u / 6 < 4) lds_wr_a<A_K + 16 * ktc + 4 * (u / 6), (u / 6) * 4 * 272>(kwa);
        else if constexpr (u / 6 < 7) lds_rd_a<A_K + 16 * ktc + 4 * (u / 6 - 4), (u / 6 - 4) * 16>(kra);
      }
      if constexpr (kt >= 0 && u == 41) lds_rd_a<A_K + 16 * ktc + 12, 48>(kra);
      if constexpr (u == 47) wait_lgkm0();
      if constexpr (G > 0 && u >= 2) {
        constexpr int lo = (u - 2) * 34 / 45 < 34 ? (u - 2) * 34 / 45 : 34, hi = (u - 1) * 34 / 45 < 34 ? (u - 1) * 34 / 45 : 34;
        sfor<hi - lo>([&](auto KI) { qp(IC<2 * (G > 0 ? G - 1 : 0) + (lo + KI) / 17>{}, IC<(lo + KI) % 17>{}); });
      }
      SCHED;
    });
  });
  asm volatile("" ::"v"(pfd));
  STAMP(2);

  f32x4 sacc[2][6];
  u32x4 Pf[6][3];
  f32x4 oacc[2][8];
  f32x4 lacc[2];
  float mA = 0.f, mB = 0.f, li[2] = {0.f, 0.f};
  float ev[24];
  const float c = 0.08838834764831845f * 1.4426950408889634f;

  auto sm = [&](auto TI, auto JI) {
    constexpr int t = TI, j = JI;
    const f32x4(&S)[6] = sacc[t & 1];
    auto v = [&](int i) -> float { return S[i >> 2][i & 3]; };
    if constexpr (j == 0) {
      mA = max3(v(0), v(1), v(2));
      mB = max3(v(3), v(4), v(5));
    } else if constexpr (j < 5) {
      mA = max3(mA, v(4 * j + 2), v(4 * j + 3));
      mB = max3(mB, v(4 * j + 4), v(4 * j + 5));
    } else if constexpr (j == 5) {
      mA = __builtin_elementwise_maximum(max3(mA, v(22), v(23)), mB);
      xmax16(mA);
    } else if constexpr (j == 6) xmax32s(mA, -c);
    else if constexpr (j < 7 + 24) {
      constexpr int i = j - 7;
      ev[i] = __builtin_amdgcn_exp2f(__builtin_fmaf(v(i), c, mA));
    } else {
      constexpr int i = j - 31;
      Pf[t][i >> 2][i & 3] = pk_bf16(ev[2 * i], ev[2 * i + 1]);
    }
  };
  const int vo = lane * 16;
  const int oa1 = lb + V1_BASE + w * 16384 + vo, oa2 = lb + w * 8192 + vo;
  u32x4 ot;
  float om[8];
  auto oe = [&](auto TI, auto KI) {
    constexpr int t = TI, k = KI;
    if constexpr (k == 0) {
      asm volatile("" : "+v"(lacc[t & 1]));
      li[t & 1] = __builtin_amdgcn_rcpf(lacc[t & 1][0]);
    } else {
      constexpr int sp = (k - 1) / 13, e = (k - 1) % 13, r = E_RD[e], pk = E_PK[e];
      if constexpr (r >= 0) asm volatile("v_mul_f32 %0, %1, %2" : "=v"(om[r]) : "v"(oacc[t & 1][2 * sp + (r >> 2)][r & 3]), "v"(li[t & 1]));
      else if constexpr (pk >= 0) ot[pk] = pk_bf16(om[2 * pk], om[2 * pk + 1]);
      else {
        constexpr int fi = sp * 6 + TG[t >> 1][t & 1];
        if constexpr (fi < 16) lds_wr<fi * 1024>(oa1, ot);
        else lds_wr<(fi - 16) * 1024>(oa2, ot);
      }
    }
  };
  const int vtr = (4 * g + ((lane >> 2) & 3)) * 256 +
                  (((lane & 3) ^ (((g & 1) << 3) | (((lane >> 3) & 1) << 2) | ((lane >> 2) & 1))) << 4) + 8 * (lane & 1);
  const int vt1 = lb + V1_BASE + w * 16384 + vtr, vt2 = lb + w * 8192 + vtr;
  auto vrd = [&](auto T3, auto JI) {
    constexpr int t3 = T3, j = JI, f = j >> 1, hh = j & 1, sp = f >> 1, h = f & 1;
    constexpr int x = (sp << 6) ^ (h << 3), d = A_V + 4 * (t3 * 8 + f) + 2 * hh;
    if constexpr (t3 < 2) lds_rdtr_a<d, (32 * t3 + 16 * hh) * 256>(vt1 ^ x);
    else lds_rdtr_a<d, 16 * hh * 256>(vt2 ^ x);
  };
  const int vwoA = (8 * g + 4 * (g & 1)) * 1024 + (w * 128 + 8 * cchunk) * 2;
  const int vwoB = (8 * g - 4 * (g & 1) + 4) * 1024 + (w * 128 + 8 * cchunk) * 2;
  u32x4 WC[16][8];
  int wso[16];
  auto wsof = [&](auto SI) {
    constexpr int S = SI;
    wso[S] = (((w + (S >> 2)) & 3) * 128 + 32 * (S & 3)) * 1024;
    asm volatile("" : "+s"(wso[S]));
  };
  auto wold = [&](auto SI, auto II) {
    constexpr int S = SI, i = II;
    if constexpr (S < 3 && i == 0) wsof(SI);
    ld_w<(i & 3) * 1024>(WC[S][i], ro, i < 4 ? vwoA : vwoB, wso[S]);
  };

  int ob[4][2] = {{oa1, oa2}};
  auto ordx = [&](auto SI, auto TI, auto DI_) {
    constexpr int S = SI, t = TI, d = DI_, fi = (S & 3) * 6 + t;
    if constexpr (fi < 16) lds_rd_a<d, fi * 1024>(ob[S >> 2][0]);
    else lds_rd_a<d, (fi - 16) * 1024>(ob[S >> 2][1]);
  };
  auto ord = [&](auto SI, auto TI) { ordx(SI, TI, IC<C_B + 24 * (SI & 1) + 4 * TI>{}); };

  sfor<6>([&](auto TI) {
    constexpr int t = TI;
    constexpr int NQ = t == 0 ? 34 : 0, NS = t > 0 ? SM_N : 0;
    constexpr int NF = NQ + NS, M0 = t == 0 ? 2 : 0, MN = 24 - M0;
    BSTAMP(t);
    if constexpr (t == 4) wait_vm<B_NV>();
    if constexpr (t == 5) {
      wait_lgkm0();
      wait_vm<B_NV - 5>();
    }
    if constexpr (t == 5) BSTAMP(6);
    sfor<24>([&](auto MI) {
      constexpr int m = MI, s = m / 6, kt = m % 6;
      mf_v_av<A_K + 4 * (kt * 4 + s), s == 0>(sacc[t & 1][kt], Qf[t][s]);
      SCHED;
      if constexpr (m % 8 == 1 && t < 4 && 3 * t + m / 8 < B_NV) vm0(IC<B_V[(3 * t + m / 8) < B_NV ? 3 * t + m / 8 : 0]>{});
      if constexpr (m % 8 == 2 && t < 4 && 3 * t + m / 8 < B_NV) vdma(IC<B_V[(3 * t + m / 8) < B_NV ? 3 * t + m / 8 : 0]>{});
      if constexpr (t == 5 && m % 3 == 0 && m / 3 < PF_WO) ld_pf<A_B + 4 * (m / 3)>(ro, vk, (32 * b + 8 * w + m / 3) * 1024);
      if constexpr (t >= 4 && m % 3 != 0) vrd(IC<(t == 4 ? 0 : 2)>{}, IC<m - m / 3 - 1>{});
      constexpr int m0 = m < M0 ? 0 : m - M0, m1 = m + 1 < M0 ? 0 : m + 1 - M0;
      constexpr int lo = m0 * NF / MN, hi = m1 * NF / MN;
      sfor<hi - lo>([&](auto KI) {
        constexpr int k = lo + KI;
        if constexpr (k < NS) sm(IC<t - 1>{}, IC<k>{});
        else qp(IC<4 + (k - NS) / 17>{}, IC<(k - NS) % 17>{});
      });
      SCHED;
    });
  });
  STAMP(3);
  u32x4 ones = u32x4{0x3F803F80u, 0x3F803F80u, 0x3F803F80u, 0x3F803F80u};
  asm volatile("" : "+v"(ones));
  nop<1>();
  auto pop = [&](auto UI) {
    sfor<12>([&](auto KI) {
      constexpr int op = P_P.op[UI][KI], j = op % 1000;
      if constexpr (op >= PWL) wait_lgkm<j>();
      else if constexpr (op >= PWV) wait_vm<j>();
      else if constexpr (op >= PB) ob[(j - 500) >> 1][j & 1] = sob[(j - 500) >> 1][j & 1] + vo;
      else if constexpr (op >= PX) wsof(IC<3>{});
      else if constexpr (op >= PP) A[0][j] = aperm(WC[0], 0, j);
      else if constexpr (op >= PR) ord(IC<0>{}, IC<j>{});
      else if constexpr (op >= PW) wold(IC<j / 8>{}, IC<j % 8>{});
      else if constexpr (op >= PO) oe(IC<j / 64>{}, IC<j % 64>{});
      else if constexpr (op >= PVR) vrd(IC<1>{}, IC<j>{});
      else if constexpr (op >= PS) sm(IC<5>{}, IC<j>{});
      if constexpr (op > 0) SCHED;
    });
  };
  sfor<PV_N>([&](auto UI) {
    constexpr int u = UI, t = u / 27, m = u % 27, t3 = pv_t3(t, m), f = pv_f(t, m);
    if constexpr (m == 0) BSTAMP(7 + t);
    if constexpr (f > 0) mf_v_av<A_V + 4 * (t3 * 8 + f - 1), pv_z(t, m)>(oacc[t & 1][f - 1], Pf[t][t3]);
    else mf_v_vv<pv_z(t, m)>(lacc[t & 1], ones, Pf[t][t3]);
    SCHED;
    pop(UI);
  });
  BSTAMP(13);
  STAMP(4);

  auto tord = [&](auto PI) {
    constexpr int p = PI;
    ordx(IC<C_NS + C_P.bs[p]>{}, IC<C_P.bt[p]>{}, IC<C_B + 24 * ((C_NS + p / 6) & 1) + 4 * (p % 6)>{});
  };
  u32x4 CA[CT][8];
  auto cperm = [&](auto SI, auto KI) {
    constexpr int S = SI, k = KI;
    CA[S - C_NS][k >> 2][k & 3] = aperm(WC[S], k >> 2, k & 3);
  };
  sfor<8 * C_NS>([&](auto NI) {
    constexpr int n = NI, s = n >> 3, f = n & 7;
    constexpr bool cv = s >= C_S0;
    if constexpr (n == 8) BSTAMP(14);
    if constexpr (n == 16) BSTAMP(15);
    if constexpr (n == 24) {
      barrier();
      SCHED;
    }
    sfor<6>([&](auto TI) {
      constexpr int t = TI;
      mf_a_va<4 * (8 * t + f), C_B + 24 * (s & 1) + 4 * t, s == 0>(A[n & 1]);
      SCHED;
      constexpr int v = 2 * f + (t >> 2), li = c_cum(s) + v, ln = c_cum(s) + 2 * f + 2;
      constexpr bool ld = (t == 3 || t == 5) && v < c_n(s);
      if constexpr (ld) wold(IC<li / 8>{}, IC<li % 8>{});
      if constexpr (t == 0) {
        if constexpr (c_rdt(s, f) >= 0 && f < 7) {
          if constexpr (s + 1 < C_NS) ord(IC<s + 1>{}, IC<c_rdt(s, f)>{});
          else tord(IC<f>{});
        }
        if constexpr (f < 7 && ln % 8 == 0 && ln <= c_cum(s + 1)) wsof(IC<ln / 8>{});
        if constexpr (f == 1 && cv) wait_vm<2>();
        if constexpr (f == 7) {
          if constexpr (s + 1 < C_NS) wait_vm<c_cum(s + 1) - 8 * (s + 2)>();
          wait_lgkm0();
        }
      }
      if constexpr (n + 1 < 8 * C_NS && (t == 1 || t == 2)) {
        A[(n + 1) & 1][2 * t - 2] = aperm(WC[(n + 1) >> 3], (n + 1) & 7, 2 * t - 2);
        A[(n + 1) & 1][2 * t - 1] = aperm(WC[(n + 1) >> 3], (n + 1) & 7, 2 * t - 1);
      }
      if constexpr (s < PV_X) pop(IC<PV_N + 6 * n + t>{});
      constexpr int cg = f == 0 ? -1 : f < 4 ? (t == 4 ? f - 1 : -1) : t >= 3 ? 3 * (f - 3) + t - 3 : (f == 6 && t == 0 ? 15 : -1);
      if constexpr (cv && cg >= 0) {
        cperm(IC<(cv ? s + CT - 2 : C_NS)>{}, IC<2 * (cg < 0 ? 0 : cg)>{});
        cperm(IC<(cv ? s + CT - 2 : C_NS)>{}, IC<2 * (cg < 0 ? 0 : cg) + 1>{});
      }
      SCHED;
    });
  });
  const rsrc_t rp = __builtin_amdgcn_make_buffer_rsrc((void*)(part + (size_t)(((br * NBATCH + b) * 8 + hg) * 4 + w) * 24 * 64), 0,
                                                      24 * 1024, RSRC_W3);
  u32x4 sd[2];
  float tb[8];
  auto stp = [&](auto MI) {
    constexpr int m = MI, t = C_P.ct[m / 52], r = (m % 52) / 13, e = m % 13;
    constexpr int rd[13] = {0, 1, 2, 3, -1, 4, 5, -1, 6, 7, -1, -1, -1}, pk[13] = {-1, -1, -1, -1, 0, -1, -1, 1, -1, -1, 2, 3, -1};
    if constexpr (rd[e] >= 0) asm volatile("v_accvgpr_read_b32 %0, a[%c1]" : "=v"(tb[rd[e]]) : "i"(4 * (8 * t + rd[e]) + r));
    else if constexpr (pk[e] >= 0) sd[r & 1][pk[e]] = pk_bf16(tb[2 * pk[e]], tb[2 * pk[e] + 1]);
    else __builtin_amdgcn_raw_buffer_store_b128(sd[r & 1], rp, vo + r * 1024, t * 4096, AUX_PST);
    if constexpr (e == 2 && m >= 13) asm volatile("" ::"v"(sd[(r & 1) ^ 1]));
    SCHED;
  };
  sfor<48 * CT>([&](auto UI) {
    constexpr int u = UI, p = u >> 3, f = u & 7;
    mf_a_va<4 * (8 * C_P.bt[p] + f), C_B + 24 * ((C_NS + p / 6) & 1) + 4 * (p % 6), false>(CA[C_P.bs[p]][f]);
    SCHED;
    sfor<3>([&](auto KI) {
      constexpr int op = C_P.op[u][KI], j = op % 1000;
      if constexpr (op >= TWL) wait_lgkm0();
      else if constexpr (op >= TWV) wait_vm<j>();
      else if constexpr (op >= TS) stp(IC<j>{});
      else if constexpr (op >= TB) tord(IC<j>{});
      else if constexpr (op >= TP) cperm(IC<14 + j / 32>{}, IC<j % 32>{});
      else if constexpr (op >= TL) wold(IC<15>{}, IC<j>{});
      SCHED;
    });
  });
  STAMP(5);
  nop<7>();
  sfor<6 * 52 - C_P.nst>([&](auto KI) { stp(IC<C_P.nst + KI>{}); });
  STAMP(6);
}

__global__ __launch_bounds__(256) void k2_reduce(const u32x4* __restrict__ part, unsigned short* __restrict__ out) {
  const int i = blockIdx.x * 256 + threadIdx.x;
  const int bb = i / 6144, e = i % 6144;
  const rsrc_t rp = __builtin_amdgcn_make_buffer_rsrc((void*)(part + (size_t)bb * 8 * 6144), 0, 8 * 6144 * 16, RSRC_W3);
  u32x4 v[8];
#pragma unroll
  for (int h = 0; h < 8; ++h) v[h] = __builtin_amdgcn_raw_buffer_load_b128(rp, e * 16, h * 6144 * 16, AUX_PLD);
  float s[8];
#pragma unroll
  for (int j = 0; j < 8; ++j) s[j] = 0.f;
#pragma unroll
  for (int h = 0; h < 8; ++h)
#pragma unroll
    for (int j = 0; j < 4; ++j) {
      s[2 * j] += __builtin_bit_cast(float, v[h][j] << 16);
      s[2 * j + 1] += __builtin_bit_cast(float, v[h][j] & 0xffff0000u);
    }
  u32x4 o;
#pragma unroll
  for (int j = 0; j < 4; ++j) o[j] = pk_bf16(s[2 * j], s[2 * j + 1]);
  const int lane = e & 63, pc = e >> 6, r = pc & 3, t = (pc >> 2) % 6, w = pc / 24;
  const int q = lane & 15, g = lane >> 4;
  const int br = bb >> 4, b = bb & 15;
  *(u32x4*)(out + ((size_t)b * SQ + 16 * t + q) * 1024 + br * 512 + 128 * w + 32 * g + 8 * r) = o;
  asm volatile(".Lk2_end:");
}

extern "C" void kernel_launch(void* const* d_in, const int* in_sizes, int n_in, void* d_out, int out_size, void* d_ws,
                              size_t ws_size, hipStream_t stream) {
  Ptrs p;
  for (int br = 0; br < 2; ++br) {
    p.x[br] = (const unsigned short*)d_in[5 * br + 0];
    p.k[br] = (const unsigned short*)d_in[5 * br + 1];
    p.v[br] = (const unsigned short*)d_in[5 * br + 2];
    p.wq[br] = (const unsigned short*)d_in[5 * br + 3];
    p.wo[br] = (const unsigned short*)d_in[5 * br + 4];
  }
  u32x4* part = (u32x4*)d_ws;
  (void)hipFuncSetAttribute((const void*)k1_fused, hipFuncAttributeMaxDynamicSharedMemorySize, LDS_BYTES);
  k1_fused<<<256, 256, LDS_BYTES, stream>>>(p, part
  );
  k2_reduce<<<2 * 16 * 96 * 64 / 256, 256, 0, stream>>>(part, (unsigned short*)d_out);
}
```

```cpp
#include <hip/hip_runtime.h>
#include <hip/hip_bf16.h>
#include <utility>

typedef unsigned u32x2 __attribute__((ext_vector_type(2)));
typedef unsigned u32x4 __attribute__((ext_vector_type(4)));
typedef float f32x4 __attribute__((ext_vector_type(4)));
#define LDSP __attribute__((address_space(3)))
#define DI __device__ __forceinline__
typedef __amdgpu_buffer_rsrc_t rsrc_t;

constexpr int SQ = 96, DM = 512, NH = 32, DH = 128, NBATCH = 16;
constexpr int RSRC_W3 = 0x00020000;
#define POL_KV " nt"
#define POL_W ""
#define POL_PF ""
#define POL_X ""
#define AUX_PST 16
#define AUX_PLD 0

template <class F, int... I>
DI void sfor_impl(F&& f, std::integer_sequence<int, I...>) {
  (f(std::integral_constant<int, I>{}), ...);
}
template <int N, class F>
DI void sfor(F&& f) {
  sfor_impl(f, std::make_integer_sequence<int, N>{});
}
template <int N>
using IC = std::integral_constant<int, N>;

typedef float f32x2 __attribute__((ext_vector_type(2)));
typedef __bf16 bf16x2 __attribute__((ext_vector_type(2)));
DI unsigned pk_bf16(float lo, float hi) {
  return __builtin_bit_cast(unsigned, __builtin_convertvector(f32x2{lo, hi}, bf16x2));
}
DI unsigned perm_lo(unsigned e, unsigned o) { return __builtin_amdgcn_perm(o, e, 0x05040100u); }
DI unsigned perm_hi(unsigned e, unsigned o) { return __builtin_amdgcn_perm(o, e, 0x07060302u); }
#define SCHED __builtin_amdgcn_sched_barrier(0)

#define MF "v_mfma_f32_16x16x32_bf16 "
template <int D, int Bq, bool Z>
DI void mf_a_va(const u32x4& A) {
  if constexpr (Z) asm volatile(MF "a[%c0:%c1], %2, a[%c3:%c4], 0" ::"i"(D), "i"(D + 3), "v"(A), "i"(Bq), "i"(Bq + 3));
  else asm volatile(MF "a[%c0:%c1], %2, a[%c3:%c4], a[%c0:%c1]" ::"i"(D), "i"(D + 3), "v"(A), "i"(Bq), "i"(Bq + 3));
}
template <int Bq, bool Z>
DI void mf_v_va(f32x4& C, const u32x4& A) {
  if constexpr (Z) asm volatile(MF "%0, %1, a[%c2:%c3], 0" : "=&v"(C) : "v"(A), "i"(Bq), "i"(Bq + 3));
  else asm volatile(MF "%0, %1, a[%c2:%c3], %0" : "+v"(C) : "v"(A), "i"(Bq), "i"(Bq + 3));
}
template <int Aq, bool Z>
DI void mf_v_av(f32x4& C, const u32x4& Bv) {
  if constexpr (Z) asm volatile(MF "%0, a[%c2:%c3], %1, 0" : "=&v"(C) : "v"(Bv), "i"(Aq), "i"(Aq + 3));
  else asm volatile(MF "%0, a[%c2:%c3], %1, %0" : "+v"(C) : "v"(Bv), "i"(Aq), "i"(Aq + 3));
}
template <bool Z>
DI void mf_v_vv(f32x4& C, const u32x4& A, const u32x4& Bv) {
  if constexpr (Z) asm volatile(MF "%0, %1, %2, 0" : "=&v"(C) : "v"(A), "v"(Bv));
  else asm volatile(MF "%0, %1, %2, %0" : "+v"(C) : "v"(A), "v"(Bv));
}
template <int X, int Y>
DI unsigned pk_acc() {
  unsigned r, t0, t1;
  asm volatile("v_accvgpr_read_b32 %1, a[%c3]\n\tv_accvgpr_read_b32 %2, a[%c4]\n\tv_cvt_pk_bf16_f32 %0, %1, %2"
               : "=v"(r), "=&v"(t0), "=&v"(t1)
               : "i"(X), "i"(Y));
  return r;
}
template <int IMM>
DI void ld_w(u32x4& d, rsrc_t r, int voff, int soff) {
  asm volatile("buffer_load_dwordx4 %0, %1, %2, %3 offen offset:%c4" POL_W : "=v"(d) : "v"(voff), "s"(r), "s"(soff), "i"(IMM));
}
template <int D, int IMM>
DI void ld_k(rsrc_t r, int voff, int soff) {
  asm volatile("buffer_load_dwordx4 a[%c0:%c1], %2, %3, %4 offen offset:%c5" POL_KV ::"i"(D), "i"(D + 3), "v"(voff), "s"(r),
               "s"(soff), "i"(IMM));
}
template <int D>
DI void ld_pf(rsrc_t r, int voff, int soff) {
  asm volatile("buffer_load_dwordx4 a[%c0:%c1], %2, %3, %4 offen" POL_PF ::"i"(D), "i"(D + 3), "v"(voff), "s"(r), "s"(soff));
}
DI void ld_pfv(u32x4& d, rsrc_t r, int voff, int soff) {
  asm volatile("buffer_load_dwordx4 %0, %1, %2, %3 offen" POL_PF : "+v"(d) : "v"(voff), "s"(r), "s"(soff));
}
template <int C>
DI void set_m0(int base) {
  asm volatile("s_add_i32 m0, %0, %c1" ::"s"(base), "i"(C) : "scc", "memory");
}
template <int IMM>
DI void dma_x(rsrc_t r, int voff, int soff) {
  asm volatile("buffer_load_dwordx4 %0, %1, %2 offen offset:%c3" POL_X " lds" ::"v"(voff), "s"(r), "s"(soff), "i"(IMM) : "memory");
}
template <int IMM>
DI void dma_v(rsrc_t r, int voff) {
  asm volatile("buffer_load_dwordx4 %0, %1, m0 offen offset:%c2" POL_KV " lds" ::"v"(voff), "s"(r), "i"(IMM) : "memory");
}
template <int D, int IMM>
DI void lds_rd_a(int addr) {
  asm volatile("ds_read_b128 a[%c0:%c1], %2 offset:%c3" ::"i"(D), "i"(D + 3), "v"(addr), "i"(IMM) : "memory");
}
template <int D, int IMM>
DI void lds_rdtr_a(int addr) {
  asm volatile("ds_read_b64_tr_b16 a[%c0:%c1], %2 offset:%c3" ::"i"(D), "i"(D + 1), "v"(addr), "i"(IMM) : "memory");
}
template <int D, int IMM>
DI void lds_wr_a(int addr) {
  asm volatile("ds_write_b128 %2, a[%c0:%c1] offset:%c3" ::"i"(D), "i"(D + 3), "v"(addr), "i"(IMM) : "memory");
}
template <int IMM>
DI void lds_wr(int addr, const u32x4& d) {
  asm volatile("ds_write_b128 %0, %1 offset:%c2" ::"v"(addr), "v"(d), "i"(IMM) : "memory");
}
template <int N>
DI void wait_vm() {
  static_assert(N >= 0 && N < 64, "vmcnt");
  asm volatile("s_waitcnt vmcnt(%c0)" ::"i"(N) : "memory");
}
DI void wait_lgkm0() { asm volatile("s_waitcnt lgkmcnt(0)" ::: "memory"); }
template <int N>
DI void wait_lgkm() {
  static_assert(N >= 0 && N < 16, "lgkmcnt");
  asm volatile("s_waitcnt lgkmcnt(%c0)" ::"i"(N) : "memory");
}
DI void barrier() { asm volatile("s_barrier" ::: "memory"); }
template <int N>
DI void nop() {
  asm volatile("s_nop %c0" ::"i"(N));
}
DI float max3(float a, float b, float c) { return __builtin_elementwise_maximum(__builtin_elementwise_maximum(a, b), c); }
DI void xmax16(float& a) {
  float t;
  asm volatile("v_mov_b32 %1, %0\n\ts_nop 1\n\tv_permlane16_swap_b32 %0, %1\n\tv_max_f32 %0, %0, %1" : "+v"(a), "=&v"(t));
}
DI void xmax32s(float& a, float s) {
  float t;
  asm volatile("v_mov_b32 %1, %0\n\ts_nop 1\n\tv_permlane32_swap_b32 %0, %1\n\tv_max_f32 %0, %0, %1\n\tv_mul_f32 %0, %2, %0"
               : "+v"(a), "=&v"(t)
               : "s"(s));
}
template <int X, int Y>
DI void acc_rd2(float& a, float& b) {
  asm volatile("v_accvgpr_read_b32 %0, a[%c2]\n\tv_accvgpr_read_b32 %1, a[%c3]" : "=v"(a), "=v"(b) : "i"(X), "i"(Y));
}

#define STAMP(i)

#define BSTAMP(i)
#define HALFX(n) (n)
struct Ptrs {
  const unsigned short* x[2];
  const unsigned short* k[2];
  const unsigned short* v[2];
  const unsigned short* wq[2];
  const unsigned short* wo[2];
};

constexpr int LDS_BYTES = 163840;
constexpr int XQ_BYTES = 24576, V1_BASE = 98304;

constexpr int A_B = 112, A_K = 160, A_V = 0;
constexpr bool tile_v(int f, int t) { return t < 2 || (t == 2 && f < 4); }
constexpr int tile_vi(int f, int t) { return t * 8 + f; }
constexpr int tile_a(int f, int t) { return 4 * (t >= 3 ? (t - 3) * 8 + f : 24 + (f - 4)); }

#define PF_WO 8
enum { OX = 1000, OK_ = 2000, OP = 3000, OV = 4000, OW = 5000 };
struct ASched {
  int op[16][24];
  bool bar[16];
};
#define SCHV 5
#define PFPV 6
#define STAG 16
#define X6(q) OX + 6 * q, OX + 6 * q + 1, OX + 6 * q + 2, OX + 6 * q + 3, OX + 6 * q + 4, OX + 6 * q + 5
#define K4(t) OK_ + 4 * t, OK_ + 4 * t + 1, OK_ + 4 * t + 2, OK_ + 4 * t + 3
#define V4(j) OV + j, OV + j + 1, OV + j + 2, OV + j + 3
#define PRO_OP 2
constexpr int A_BURST[6][12] = {{X6(1), OP + 4}, {X6(2), OP + 5}, {OP + 6, K4(0), OK_ + 4, OK_ + 5},
                                {X6(3), OP + 7}, {OK_ + 6, OK_ + 7, K4(2), OK_ + 12}, {OK_ + 13, OK_ + 14, OK_ + 15, K4(4)}};
constexpr int A_TAIL[3][8] = {{K4(5), OV + 0, OV + 1, OV + 2}, {OV + 3, V4(4), OV + 8}, {OV + 9, OV + 10, OV + 16, OV + 17, OV + 18}};
constexpr int A_KST[6] = {8, 11, 12, 13, 14, 15};
constexpr int B_V[] = {19, 20, 21, 22, 23, 11, 12, 13, 14, 15};
constexpr int B_NV = 10, B_PFT = 4;
constexpr ASched a_make() {
  ASched a{};
  for (int s = 0; s <= 12; s += 2)
    for (int i = 0; i < 16; ++i) a.op[s][i] = OW + 8 * (s + 2) + i;
  for (int s = 1; s <= 11; s += 2) {
    a.bar[s] = true;
    for (int i = 0; i < (int)(sizeof(A_BURST[0]) / sizeof(int)); ++i) a.op[s][i] = A_BURST[s >> 1][i];
  }
  for (int s = 13; s < 16; ++s) {
    int n = 0;
    while (n < 8 && A_TAIL[s - 13][n]) ++n;
    for (int i = 0; i < n; ++i) a.op[s][3 * (i * 8 / n)] = A_TAIL[s - 13][i];
  }
  return a;
}
constexpr ASched A_S = a_make();
constexpr int A_NP = 3 + 6 + PRO_OP + 16;
constexpr int a_seq(int op) {
  if (op >= OW && op < OW + 16) return 9 + PRO_OP + op - OW;
  int c = A_NP;
  for (int s = 0; s < 16; ++s)
    for (int k = 0; k < 24; ++k)
      if (A_S.op[s][k]) {
        if (A_S.op[s][k] == op) return c;
        ++c;
      }
  return -1;
}
constexpr int a_issued(int s, int k) {
  int c = A_NP;
  for (int i = 0; i < 16; ++i)
    for (int j = 0; j < 24; ++j)
      if (A_S.op[i][j] && (i < s || (i == s && j < k))) ++c;
  return c;
}
constexpr int a_kt(int s) {
  for (int k = 0; k < 6; ++k)
    if (A_KST[k] == s) return k;
  return -1;
}
constexpr int imax(int a, int b) { return a > b ? a : b; }
constexpr int a_need(int s) {
  int n = 0;
  for (int i = 0; i < 8; ++i) n = imax(n, a_seq(OW + 8 * (s + 1) + i));
  if (s == 2 || s == 6 || s == 10)
    for (int i = 0; i < 6; ++i) n = imax(n, a_seq(OX + 6 * ((s + 2) / 4) + i));
  if (a_kt(s + 1) >= 0)
    for (int i = 0; i < 4; ++i) n = imax(n, a_seq(OK_ + 4 * a_kt(s + 1) + i));
  if (s == 14)
    for (int i = 2; i < 8; ++i) n = imax(n, a_seq(OP + i));
  return n;
}
constexpr int a_wait(int s) { return a_issued(s, s < 13 ? 14 : 21) - a_need(s) - 1; }
constexpr int v_jb(int j) {
  int jb = -99;
  for (int i = 0; i < 16 * 24 + B_NV; ++i) {
    const int op = i < 16 * 24 ? A_S.op[i / 24][i % 24] : OV + B_V[i - 16 * 24];
    if (op >= OX && op < OK_) jb = -99;
    if (op < OV || op >= OW) continue;
    const int v = op - OV;
    if (!(v >= jb && v - jb < 4 && (v < 16) == (jb < 16))) jb = v;
    if (v == j) return jb;
  }
  return -1;
}
constexpr int a_wait_w(int ws, int s, int k) {
  int n = 0;
  for (int i = 0; i < 8; ++i) n = imax(n, a_seq(OW + 8 * ws + i));
  return a_issued(s, k) - n - 1;
}
constexpr bool a_check() {
  for (int i = 0; i < 24; ++i)
    if (a_seq(OK_ + i) < 0) return false;
  for (int i = 6; i < 24; ++i)
    if (a_seq(OX + i) < 0) return false;
  for (int i = 0; i < 128; ++i)
    if (a_seq(OW + i) < 0 || (i >= 32 && a_seq(OW + i) < a_issued(i / 8 - 3, 0))) return false;
  int nv = B_NV;
  for (int i = 0; i < 24; ++i)
    if (a_seq(OV + i) >= 0) {
      ++nv;
      if (i >= 11 && i < 16 && a_seq(OV + i) < a_issued(A_KST[5] + 1, 0)) return false;
      if (i >= 16 && a_seq(OV + i) < a_issued(7, 0)) return false;
    }
  for (int s = 0; s < 15; ++s)
    if (a_wait(s) < 0 || a_wait(s) > 63) return false;
  return nv == 24 && A_S.bar[3] && A_S.bar[7] && A_S.bar[11];
}
static_assert(a_check(), "phase A schedule");

#define CT 6
constexpr int C_NS = 16 - CT, C_S0 = C_NS - CT + 2, C_B = 192;
constexpr int c_n(int s) { return s < C_S0 && 4 * s < 8 * (CT - 4) ? 12 : 8; }
constexpr int c_cum(int s) {
  int c = 24;
  for (int i = 0; i < s; ++i) c += c_n(i);
  return c;
}
static_assert(CT >= 4 && CT <= 6 && c_cum(C_NS) == 120, "phase C schedule");
enum { TL = 1000, TP = 2000, TB = 3000, TS = 4000, TWV = 5000, TWL = 6000 };
struct CPlan {
  int bt[6 * CT], bs[6 * CT];
  int ct[6];
  int op[48 * CT][3];
  int nst;
};
constexpr CPlan c_make() {
  CPlan c{};
  int nx[6] = {0, 0, 0, 0, 0, 0}, done[6] = {0, 0, 0, 0, 0, 0}, nc = 0;
  for (int p = 0; p < 6 * CT; ++p)
    for (int t = 0; t < 6; ++t)
      if (nx[t] < CT && (nx[t] < CT - 2 || (nx[t] == CT - 2 && p >= 3) || p >= 6)) {
        c.bt[p] = t;
        c.bs[p] = nx[t]++;
        if (nx[t] == CT) {
          c.ct[nc] = t;
          done[nc++] = 8 * p + 7;
        }
        break;
      }
  int used[48 * CT] = {};
  auto put = [&](int u, int op, int w) {
    int k = 0;
    while (c.op[u][k]) ++k;
    c.op[u][k] = op;
    used[u] += w;
  };
  for (int i = 0; i < 8; ++i) put(i, TL + i, 2);
  put(8, TWV + 8, 0);
  put(32, TWV + 0, 0);
  for (int k = 0; k < 32; ++k) {
    put(8 + k / 2, TP + k, 1);
    put(32 + k / 2, TP + 32 + k, 1);
  }
  for (int r = 0; r + 1 < CT; ++r) {
    for (int j = 0; j < 6; ++j) put(r ? 48 * r + 6 * j : 24 + j, TB + 6 * (r + 1) + j, 1);
    put(48 * r + 46, TWL, 0);
  }
  int u = 0, m = 0;
  for (; m < 6 * 52; ++m) {
    const int w = m % 13 == 12 ? 2 : 1;
    if (u < done[m / 52] + 2) u = done[m / 52] + 2;
    while (u < 48 * CT && used[u] + w > 2) ++u;
    if (u >= 48 * CT) break;
    put(u, TS + m, w);
  }
  c.nst = m;
  return c;
}
constexpr CPlan C_P = c_make();

constexpr int pv_t3(int t, int m) { return t == 0 ? (m < 9 ? 0 : m < 18 ? 2 : 1) : (m < 9 ? m / 3 : ((m - 9) % 6) / 2); }
constexpr int pv_f(int t, int m) { return t == 0 ? m % 9 : (m < 9 ? m % 3 : 3 + 2 * ((m - 9) / 6) + (m - 9) % 2); }
constexpr bool pv_z(int t, int m) { return t == 0 ? m < 9 : pv_t3(t, m) == 0; }
constexpr int pv_first(int t, int f) {
  for (int m = 0; m < 27; ++m)
    if (pv_f(t, m) == f) return 27 * t + m;
  return -1;
}
constexpr int pv_last(int t, int f) {
  for (int m = 26; m >= 0; --m)
    if (pv_f(t, m) == f) return 27 * t + m;
  return -1;
}
#define PV_R 177
constexpr int SM_N = 43, OE_N = 53, PV_N = 162;
constexpr int E_RD[13] = {0, 1, 2, 3, -1, 4, 5, -1, 6, 7, -1, -1, -1}, E_PK[13] = {-1, -1, -1, -1, 0, -1, -1, 1, -1, -1, 2, 3, -1};
enum { PS = 1000, PVR = 2000, PO = 3000, PW = 4000, PR = 5000, PP = 6000, PX = 7000, PB = 7500, PWV = 8000, PWL = 9000 };
constexpr int PV_X = 3;
constexpr int c_rdt(int s, int f) { return s < PV_X ? (f < 4 ? f + 2 : f - 5) : (f < 6 ? f : -1); }
struct PPlan {
  int op[PV_N + 48 * PV_X][12];
  bool ok;
};
constexpr PPlan p_make() {
  PPlan c{};
  int it[512] = {}, cost[512] = {}, rdy[512] = {}, dl[512] = {}, n = 0;
  auto add = [&](int op, int w, int r, int d) {
    it[n] = op;
    cost[n] = w;
    rdy[n] = r;
    dl[n++] = d;
  };
  auto piece = [&](int t, int sp, int nt) {
    for (int e = 0; e < 13; ++e) {
      const int f = 1 + 2 * sp + (E_RD[e] >> 2);
      if (e == 12 && nt >= 0) add(PO + 64 * nt, 2, pv_last(nt, 0) + 3, nt < 4 ? pv_first(nt + 2, 0) : 999);
      add(PO + 64 * t + 1 + 13 * sp + e, 1, E_RD[e] >= 0 ? pv_last(t, f) + 3 : 0,
          t < 4 ? (E_RD[e] >= 0 ? pv_first(t + 2, f) : PV_N) : (sp ? PV_N + 48 * (sp - 1) + 6 * (t + 1) : PV_N));
    }
  };
  for (int j = 0; j < SM_N; ++j) {
    if (j == SM_N - 1) add(PO, 2, pv_last(0, 0) + 3, pv_first(2, 0));
    add(PS + j, j < 5 ? 2 : j < 7 ? 6 : j < 31 ? 3 : 1, 0, 27 * 5 - 1);
  }
  for (int t = 0; t < 4; ++t)
    for (int sp = 0; sp < 4; ++sp) piece(t, sp, sp == 3 ? t + 1 : -1);
  piece(4, 0, -1);
  piece(4, 1, 5);
  piece(5, 0, -1);
  for (int k = 0; k < 6; ++k) add(PR + (k + 2) % 6, 1, 0, PV_N - 3);
  add(PWV + 99, 0, 0, PV_N - 1);
  for (int k = 0; k < 4; ++k) add(PP + k, 1, 0, PV_N - 1);
  add(PX, 1, 0, PV_N);
  piece(5, 1, -1);
  for (int sp = 2; sp < 4; ++sp) {
    piece(4, sp, -1);
    piece(5, sp, -1);
  }
  for (int k = 2; k < 8; ++k) add(PB + k, 1, 0, PV_N + 48 * PV_X);
  int placed = 0, idx = 0, nw = 0, nds = 0;
  c.ok = true;
  for (int g = 0; g < PV_N + 48 * PV_X; ++g) {
    int k = 0, used = 0;
    auto put = [&](int op, int w) {
      c.op[g][k++] = op;
      placed += w;
      used += w;
    };
    if (g == 1) {
      put(PWL + 0, 0);
      put(PWV + PF_WO, 0);
    }
    for (int i = 0; i < 16; ++i)
      if (g == 2 + i * 12 / 16) put(PVR + i, 1);
    if (g == 17) put(PWL + 0, 0);
    for (int i = 0; i < 24; ++i)
      if (g == 81 + i * 80 / 24) {
        put(PW + i, i % 8 ? 2 : 3);
        ++nw;
      }
    const int cs = (g - PV_N) / 48, cf = (g - PV_N) % 48 / 6, ctt = (g - PV_N) % 6;
    const int cap = g < PV_N ? 99 : (ctt == 4 || ((ctt == 3 || ctt == 5) && 2 * cf + (ctt >> 2) >= c_n(cs)) ? 2 : 0);
    while (idx < n && rdy[idx] <= g && used + cost[idx] <= cap && (g >= PV_N || placed * 64 < PV_R * (g + 1))) {
      const int op = it[idx];
      if (g >= dl[idx]) c.ok = false;
      if (op >= PR && op < PP) nds = 0;
      else if (op >= PO && op < PW && (op - PO) % 64 > 0 && ((op - PO) % 64 - 1) % 13 == 12) ++nds;
      if (op == PWV + 99 && nw < 8) c.ok = false;
      put(op == PWV + 99 ? PWV + nw - 8 : op, cost[idx]);
      ++idx;
    }
    if (g == PV_N - 1) put(PWL + nds, 0);
    if (k > 11) c.ok = false;
  }
  c.ok = c.ok && idx == n && nw == 24;
  return c;
}
constexpr PPlan P_P = p_make();
static_assert(P_P.ok, "PV schedule");

DI unsigned aperm(const u32x4 (&W)[8], int f, int p) {
  return (f & 1) ? perm_hi(W[2 * p][f >> 1], W[2 * p + 1][f >> 1]) : perm_lo(W[2 * p][f >> 1], W[2 * p + 1][f >> 1]);
}

__global__ __launch_bounds__(256, 1) void k1_fused(Ptrs p, u32x4* part
) {
  extern __shared__ __attribute__((aligned(16))) char lds[];
  asm volatile("" ::: "a0", "a255");
  const int bid = blockIdx.x;
  const int xcd = bid & 7, idx = bid >> 3;
  const int br = xcd >> 2, hg = (xcd & 3) * 2 + (idx & 1), b = idx >> 1;
  const int tid = threadIdx.x, lane = tid & 63;
  const int w = __builtin_amdgcn_readfirstlane(tid >> 6);
  const int q = lane & 15, g = lane >> 4;
  const int cchunk = q;
  const int lb = (int)(unsigned)(size_t)(LDSP char*)lds;
  STAMP(0);

  const int vk = lane * 16;
  u32x4 pfd = u32x4{0, 0, 0, 0};
  {
    unsigned long long pc;
    int len;
    asm volatile("s_getpc_b64 %0\n.Lk1_pc:\n\ts_mov_b32 %1, .Lk2_end-.Lk1_pc" : "=s"(pc), "=s"(len));
    const rsrc_t rc = __builtin_amdgcn_make_buffer_rsrc((void*)pc, 0, len, RSRC_W3);
    nop<4>();
    ld_pfv(pfd, rc, vk, (idx * 4 + w) * 1024);
  }
  const unsigned short *px = p.x[br], *pk = p.k[br], *pv = p.v[br], *pq = p.wq[br], *po = p.wo[br];
  asm volatile("" : "+s"(px), "+s"(pk), "+s"(pv), "+s"(pq), "+s"(po), "+s"(part));
  const int head = hg * 4 + w;
  const size_t kvoff = ((size_t)b * NH + head) * SQ * DH;
  const rsrc_t rx = __builtin_amdgcn_make_buffer_rsrc((void*)(px + (size_t)b * SQ * DM), 0, HALFX(SQ * DM * 2), RSRC_W3);
  const rsrc_t rk = __builtin_amdgcn_make_buffer_rsrc((void*)(pk + kvoff), 0, HALFX(SQ * DH * 2), RSRC_W3);
  const rsrc_t rq = __builtin_amdgcn_make_buffer_rsrc((void*)pq, 0, DM * NH * DH * 2, RSRC_W3);
  const rsrc_t ro = __builtin_amdgcn_make_buffer_rsrc((void*)(po + (size_t)hg * 512 * DM), 0, 512 * DM * 2, RSRC_W3);

  f32x4 accv[20];
  u32x4 W[4][8];
  u32x4 A[2];

  auto gmf = [&](auto FI, auto TI, auto BI, auto ZI, const u32x4& Af) {
    constexpr int f = FI, t = TI, bq = A_B + 24 * BI + 4 * t;
    constexpr bool z = ZI;
    if constexpr (tile_v(f, t)) mf_v_va<bq, z>(accv[tile_vi(f, t)], Af);
    else mf_a_va<tile_a(f, t), bq, z>(Af);
  };
  auto gpk = [&](auto PI, auto TI, auto RI) -> unsigned {
    constexpr int pp = PI, t = TI, r = RI;
    if constexpr (tile_v(2 * pp, t)) return pk_bf16(accv[tile_vi(2 * pp, t)][r], accv[tile_vi(2 * pp + 1, t)][r]);
    else return pk_acc<tile_a(2 * pp, t) + r, tile_a(2 * pp + 1, t) + r>();
  };

  const int vx = g * 1024 + ((q ^ g ^ (4 * w)) << 4);
  const int vwq = (8 * g) * 8192 + (hg * 512 + w * 128 + 8 * cchunk) * 2;
  int sxb = lb + w * 1024, sxo[6];
#pragma unroll
  for (int i = 0; i < 6; ++i) sxo[i] = (4 * i + w) * 4096;
  asm volatile("" : "+s"(sxb), "+s"(sxo[0]), "+s"(sxo[1]), "+s"(sxo[2]), "+s"(sxo[3]), "+s"(sxo[4]), "+s"(sxo[5]));
  auto xm0 = [&](auto II) { set_m0<(II / 6) * (XQ_BYTES - 256) + (II % 6) * 4096>(sxb); };
  auto xdma = [&](auto II) { dma_x<(II / 6) * 256>(rx, vx, sxo[II % 6]); };
  const int vpf = ((lane >> 5) * 32 + 2 * b + (w >> 1)) * 8192 + hg * 1024 + (w & 1) * 512 + (lane & 31) * 16;
  nop<4>();
  STAMP(8);
  ld_pfv(pfd, rq, vpf, 0);
  ld_pfv(pfd, rq, vpf, 64 * 8192);
  sfor<6>([&](auto I) {
    xm0(I);
    nop<0>();
    xdma(I);
  });
  sfor<PRO_OP>([&](auto I) { ld_pfv(pfd, rq, vpf, (2 + I) * 64 * 8192); });
  STAMP(9);
  wait_vm<7 + PRO_OP>();
  STAMP(10);
  sfor<16>([&](auto I) { ld_w<0>(W[I >> 3][I & 7], rq, vwq, (32 * (I >> 3) + (I & 7)) * 8192); });
  int xa[2][4];
#pragma unroll
  for (int h = 0; h < 2; ++h)
#pragma unroll
    for (int j = 0; j < 4; ++j) xa[h][j] = lb + h * 2 * XQ_BYTES + q * 256 + (((4 * j + g) ^ q) << 4);
  const int kst = lb + V1_BASE + w * 16384 + 16384 - 16 * 272;
  const int kwa = kst + g * 272 + q * 16, kra = kst + q * 272 + g * 64;
  const int vv0 = g * 256 + ((q ^ (((g >> 1) << 2) | (g & 1))) << 4);
  const int vv1 = vv0 ^ 128;
  int svb[2] = {lb + V1_BASE + w * 16384, lb + w * 8192};
  const rsrc_t rv0 = __builtin_amdgcn_make_buffer_rsrc((void*)((const char*)(pv + kvoff) - svb[0]), 0, HALFX(svb[0] + 16384), RSRC_W3);
  const rsrc_t rv1 = __builtin_amdgcn_make_buffer_rsrc((void*)((const char*)(pv + kvoff) + 16384 - svb[1]), 0, HALFX(svb[1] + 8192), RSRC_W3);
  asm volatile("" : "+s"(svb[0]), "+s"(svb[1]));
  auto vm0 = [&](auto JI) {
    constexpr int j = JI;
    if constexpr (v_jb(j) == j) set_m0<(j & 15) * 1024>(svb[j >> 4]);
  };
  auto vdma = [&](auto JI) {
    constexpr int j = JI;
    dma_v<(j - v_jb(j)) * 1024>(j < 16 ? rv0 : rv1, (j & 1) ? vv1 : vv0);
  };
  int sob[4][2];
#pragma unroll
  for (int h = 1; h < 4; ++h) {
    sob[h][0] = lb + V1_BASE + ((w + h) & 3) * 16384;
    sob[h][1] = lb + ((w + h) & 3) * 8192;
    asm volatile("" : "+s"(sob[h][0]), "+s"(sob[h][1]));
  }
  STAMP(11);
  SCHED;
  if (b & 1) __builtin_amdgcn_s_sleep(STAG);
  wait_vm<A_NP - 9>();
  barrier();
  STAMP(1);

  constexpr int TG[3][2] = {{4, 5}, {2, 3}, {0, 1}};
  u32x4 AT[3][8];
  auto tperm = [&](auto KI) {
    constexpr int k = KI, m = k >> 1, d = 2 * (k & 1);
    AT[m >> 3][m & 7][d] = aperm(W[(13 + (m >> 3)) & 3], m & 7, d);
    AT[m >> 3][m & 7][d + 1] = aperm(W[(13 + (m >> 3)) & 3], m & 7, d + 1);
  };
  auto tmf = [&](auto GI, auto UI) {
    constexpr int G = GI, u = UI, sp = u >> 4, f = (u >> 1) & 7, tt = u & 1, t = TG[G][tt];
    constexpr int bq = A_B + 24 * ((G & 1) ^ 1) + 4 * (2 * sp + tt);
    if constexpr (tile_v(f, t)) mf_v_va<bq, false>(accv[tile_vi(f, t)], AT[sp][f]);
    else mf_a_va<tile_a(f, t), bq, false>(AT[sp][f]);
  };
  u32x4 Qf[6][4];
  float ta[2][2];
  auto qp = [&](auto II, auto JI) {
    constexpr int qi = II, t = TG[qi >> 1][qi & 1], j = JI, pp = (j & 15) >> 2, s = j & 3, i = j > 0 ? j - 1 : 0;
    if constexpr (j < 16) {
      if constexpr (tile_v(2 * pp, t)) Qf[qi][s][pp] = gpk(IC<pp>{}, IC<t>{}, IC<s>{});
      else acc_rd2<tile_a(2 * pp, t) + s, tile_a(2 * pp + 1, t) + s>(ta[j & 1][0], ta[j & 1][1]);
    }
    if constexpr (j > 0 && !tile_v(2 * (i >> 2), t)) Qf[qi][i & 3][i >> 2] = pk_bf16(ta[i & 1][0], ta[i & 1][1]);
  };

  auto xrdx = [&](auto SI, auto TI, auto DI_) {
    constexpr int s = SI, t = TI;
    lds_rd_a<DI_, ((s >> 2) & 1) * XQ_BYTES + t * 4096>(xa[s >> 3][s & 3]);
  };
  auto xrd = [&](auto SI, auto TI) { xrdx(SI, TI, IC<A_B + 24 * (SI & 1) + 4 * TI>{}); };
  auto txrd = [&](auto GI, auto JI) {
    constexpr int G = GI, jj = JI;
    xrdx(IC<13 + jj / 2>{}, IC<TG[G][jj & 1]>{}, IC<A_B + 24 * ((G & 1) ^ 1) + 4 * jj>{});
  };
  int so[2];
  auto aprep = [&](auto OI, auto PI) {
    constexpr int op = OI, j = op % 1000, pi = PI;
    if constexpr (op >= OV && op < OW) vm0(IC<j>{});
    else if constexpr (op >= OK_) {
      so[pi] = op >= OW ? (32 * (j >> 3) + (j & 7)) * 8192 : op >= OP ? j * 64 * 8192 : (j >> 2) * 4096;
      asm volatile("" : "+s"(so[pi]));
    } else if constexpr (op >= OX) xm0(IC<j>{});
  };
  auto aop = [&](auto OI, auto PI) {
    constexpr int op = OI, j = op % 1000, pi = PI;
    if constexpr (op >= OW) ld_w<0>(W[(j >> 3) & 3][j & 7], rq, vwq, so[pi]);
    else if constexpr (op >= OV) vdma(IC<j>{});
    else if constexpr (op >= OP) ld_pfv(pfd, rq, vpf, so[pi]);
    else if constexpr (op >= OK_) ld_k<A_K + 4 * j, (j & 3) * 1024>(rk, vk, so[pi]);
    else if constexpr (op >= OX) xdma(IC<j>{});
  };
  sfor<6>([&](auto T) { xrd(IC<0>{}, T); });
  wait_vm<8>();
  SCHED;
#pragma unroll
  for (int pp = 0; pp < 4; ++pp) A[0][pp] = aperm(W[0], 0, pp);
  SCHED;
  wait_lgkm0();
  nop<0>();
  SCHED;
  sfor<104>([&](auto NI) {
    constexpr int n = NI, s = n >> 3, f = n & 7;
    constexpr int kt = a_kt(s), ktc = kt < 0 ? 0 : kt;
    if constexpr (f == 0 && A_S.bar[s]) {
      barrier();
      SCHED;
    }
    sfor<6>([&](auto TI) {
      constexpr int t = TI;
      gmf(IC<f>{}, TI, IC<(s & 1)>{}, std::bool_constant<s == 0>{}, A[n & 1]);
      SCHED;
      constexpr int opb = A_S.op[s][2 * f + 1];
      constexpr bool dmb = (opb >= OX && opb < OK_) || (opb >= OV && opb < OW);
      if constexpr (t == 0 && f == 7) wait_vm<a_wait(s)>();
      if constexpr (t == 0) aprep(IC<A_S.op[s][2 * f]>{}, IC<0>{});
      if constexpr (t == (dmb ? 4 : 0)) aprep(IC<opb>{}, IC<1>{});
      if constexpr (t == 4 && f < 6) {
        if constexpr (s < 12) xrd(IC<s + 1>{}, IC<f>{});
        else txrd(IC<0>{}, IC<f>{});
      }
      if constexpr (t == 3 || t == 5) aop(IC<A_S.op[s][2 * f + t / 4]>{}, IC<t / 4>{});
      if constexpr (t == 1 || t == 2) {
        if constexpr (n < 103) {
          A[(n + 1) & 1][2 * t - 2] = aperm(W[((n + 1) >> 3) & 3], (n + 1) & 7, 2 * t - 2);
          A[(n + 1) & 1][2 * t - 1] = aperm(W[((n + 1) >> 3) & 3], (n + 1) & 7, 2 * t - 1);
        } else tperm(IC<t - 1>{});
      }
      if constexpr (n == 103 && t == 4) tperm(IC<2>{});
      if constexpr (t == 4 && kt >= 0) {
        if constexpr (f < 4) lds_wr_a<A_K + 16 * ktc + 4 * f, f * 4 * 272>(kwa);
        else lds_rd_a<A_K + 16 * ktc + 4 * (f - 4), (f - 4) * 16>(kra);
      }
      if constexpr (t == 5 && f == 7) wait_lgkm<(kt >= 0 ? 2 : 0)>();
      SCHED;
    });
  });
  sfor<3>([&](auto GI) {
    constexpr int G = GI, s = 13 + G;
    constexpr int kt = a_kt(s), ktc = kt < 0 ? 0 : kt;
    sfor<48>([&](auto UI) {
      constexpr int u = UI;
      tmf(GI, UI);
      SCHED;
      if constexpr (u == 42 && s < 15) wait_vm<a_wait(s)>();
      if constexpr (G == 0 && u == 13) wait_vm<a_wait_w(14, 13, 6)>();
      if constexpr (G == 0 && u == 29) wait_vm<a_wait_w(15, 13, 14)>();
      if constexpr (u & 1) aop(IC<A_S.op[s][u / 2]>{}, IC<0>{});
      else aprep(IC<A_S.op[s][u / 2]>{}, IC<0>{});
      if constexpr (G == 0 && u + 3 < 48) tperm(IC<u + 3>{});
      if constexpr (G == 0 && u == 45) nop<0>();
      if constexpr (G < 2 && u % 6 == 5 && u / 6 < 6) txrd(IC<(G < 2 ? G + 1 : 0)>{}, IC<u / 6>{});
      if constexpr (kt >= 0 && u % 6 == 3) {
        if constexpr (u / 6 < 4) lds_wr_a<A_K + 16 * ktc + 4 * (u / 6), (u / 6) * 4 * 272>(kwa);
        else if constexpr (u / 6 < 7) lds_rd_a<A_K + 16 * ktc + 4 * (u / 6 - 4), (u / 6 - 4) * 16>(kra);
      }
      if constexpr (kt >= 0 && u == 41) lds_rd_a<A_K + 16 * ktc + 12, 48>(kra);
      if constexpr (u == 47) wait_lgkm0();
      if constexpr (G > 0 && u >= 2) {
        constexpr int lo = (u - 2) * 34 / 45 < 34 ? (u - 2) * 34 / 45 : 34, hi = (u - 1) * 34 / 45 < 34 ? (u - 1) * 34 / 45 : 34;
        sfor<hi - lo>([&](auto KI) { qp(IC<2 * (G > 0 ? G - 1 : 0) + (lo + KI) / 17>{}, IC<(lo + KI) % 17>{}); });
      }
      SCHED;
    });
  });
  asm volatile("" ::"v"(pfd));
  STAMP(2);

  f32x4 sacc[2][6];
  u32x4 Pf[6][3];
  f32x4 oacc[2][8];
  f32x4 lacc[2];
  float mA = 0.f, mB = 0.f, li[2] = {0.f, 0.f};
  float ev[24];
  const float c = 0.08838834764831845f * 1.4426950408889634f;

  auto sm = [&](auto TI, auto JI) {
    constexpr int t = TI, j = JI;
    const f32x4(&S)[6] = sacc[t & 1];
    auto v = [&](int i) -> float { return S[i >> 2][i & 3]; };
    if constexpr (j == 0) {
      mA = max3(v(0), v(1), v(2));
      mB = max3(v(3), v(4), v(5));
    } else if constexpr (j < 5) {
      mA = max3(mA, v(4 * j + 2), v(4 * j + 3));
      mB = max3(mB, v(4 * j + 4), v(4 * j + 5));
    } else if constexpr (j == 5) {
      mA = __builtin_elementwise_maximum(max3(mA, v(22), v(23)), mB);
      xmax16(mA);
    } else if constexpr (j == 6) xmax32s(mA, -c);
    else if constexpr (j < 7 + 24) {
      constexpr int i = j - 7;
      ev[i] = __builtin_amdgcn_exp2f(__builtin_fmaf(v(i), c, mA));
    } else {
      constexpr int i = j - 31;
      Pf[t][i >> 2][i & 3] = pk_bf16(ev[2 * i], ev[2 * i + 1]);
    }
  };
  const int vo = lane * 16;
  const int oa1 = lb + V1_BASE + w * 16384 + vo, oa2 = lb + w * 8192 + vo;
  u32x4 ot;
  float om[8];
  auto oe = [&](auto TI, auto KI) {
    constexpr int t = TI, k = KI;
    if constexpr (k == 0) {
      asm volatile("" : "+v"(lacc[t & 1]));
      li[t & 1] = __builtin_amdgcn_rcpf(lacc[t & 1][0]);
    } else {
      constexpr int sp = (k - 1) / 13, e = (k - 1) % 13, r = E_RD[e], pk = E_PK[e];
      if constexpr (r >= 0) asm volatile("v_mul_f32 %0, %1, %2" : "=v"(om[r]) : "v"(oacc[t & 1][2 * sp + (r >> 2)][r & 3]), "v"(li[t & 1]));
      else if constexpr (pk >= 0) ot[pk] = pk_bf16(om[2 * pk], om[2 * pk + 1]);
      else {
        constexpr int fi = sp * 6 + TG[t >> 1][t & 1];
        if constexpr (fi < 16) lds_wr<fi * 1024>(oa1, ot);
        else lds_wr<(fi - 16) * 1024>(oa2, ot);
      }
    }
  };
  const int vtr = (4 * g + ((lane >> 2) & 3)) * 256 +
                  (((lane & 3) ^ (((g & 1) << 3) | (((lane >> 3) & 1) << 2) | ((lane >> 2) & 1))) << 4) + 8 * (lane & 1);
  const int vt1 = lb + V1_BASE + w * 16384 + vtr, vt2 = lb + w * 8192 + vtr;
  auto vrd = [&](auto T3, auto JI) {
    constexpr int t3 = T3, j = JI, f = j >> 1, hh = j & 1, sp = f >> 1, h = f & 1;
    constexpr int x = (sp << 6) ^ (h << 3), d = A_V + 4 * (t3 * 8 + f) + 2 * hh;
    if constexpr (t3 < 2) lds_rdtr_a<d, (32 * t3 + 16 * hh) * 256>(vt1 ^ x);
    else lds_rdtr_a<d, 16 * hh * 256>(vt2 ^ x);
  };
  const int vwoA = (8 * g + 4 * (g & 1)) * 1024 + (w * 128 + 8 * cchunk) * 2;
  const int vwoB = (8 * g - 4 * (g & 1) + 4) * 1024 + (w * 128 + 8 * cchunk) * 2;
  u32x4 WC[16][8];
  int wso[16];
  auto wsof = [&](auto SI) {
    constexpr int S = SI;
    wso[S] = (((w + (S >> 2)) & 3) * 128 + 32 * (S & 3)) * 1024;
    asm volatile("" : "+s"(wso[S]));
  };
  auto wold = [&](auto SI, auto II) {
    constexpr int S = SI, i = II;
    if constexpr (S < 3 && i == 0) wsof(SI);
    ld_w<(i & 3) * 1024>(WC[S][i], ro, i < 4 ? vwoA : vwoB, wso[S]);
  };

  int ob[4][2] = {{oa1, oa2}};
  auto ordx = [&](auto SI, auto TI, auto DI_) {
    constexpr int S = SI, t = TI, d = DI_, fi = (S & 3) * 6 + t;
    if constexpr (fi < 16) lds_rd_a<d, fi * 1024>(ob[S >> 2][0]);
    else lds_rd_a<d, (fi - 16) * 1024>(ob[S >> 2][1]);
  };
  auto ord = [&](auto SI, auto TI) { ordx(SI, TI, IC<C_B + 24 * (SI & 1) + 4 * TI>{}); };

  sfor<6>([&](auto TI) {
    constexpr int t = TI;
    constexpr int NQ = t == 0 ? 34 : 0, NS = t > 0 ? SM_N : 0;
    constexpr int NF = NQ + NS, M0 = t == 0 ? 2 : 0, MN = 24 - M0;
    BSTAMP(t);
    if constexpr (t == 4) wait_vm<B_NV>();
    if constexpr (t == 5) {
      wait_lgkm0();
      wait_vm<B_NV - 5>();
    }
    if constexpr (t == 5) BSTAMP(6);
    sfor<24>([&](auto MI) {
      constexpr int m = MI, s = m / 6, kt = m % 6;
      mf_v_av<A_K + 4 * (kt * 4 + s), s == 0>(sacc[t & 1][kt], Qf[t][s]);
      SCHED;
      if constexpr (m % 8 == 1 && t < 4 && 3 * t + m / 8 < B_NV) vm0(IC<B_V[(3 * t + m / 8) < B_NV ? 3 * t + m / 8 : 0]>{});
      if constexpr (m % 8 == 2 && t < 4 && 3 * t + m / 8 < B_NV) vdma(IC<B_V[(3 * t + m / 8) < B_NV ? 3 * t + m / 8 : 0]>{});
      if constexpr (t == 5 && m % 3 == 0 && m / 3 < PF_WO) ld_pf<A_B + 4 * (m / 3)>(ro, vk, (32 * b + 8 * w + m / 3) * 1024);
      if constexpr (t >= 4 && m % 3 != 0) vrd(IC<(t == 4 ? 0 : 2)>{}, IC<m - m / 3 - 1>{});
      constexpr int m0 = m < M0 ? 0 : m - M0, m1 = m + 1 < M0 ? 0 : m + 1 - M0;
      constexpr int lo = m0 * NF / MN, hi = m1 * NF / MN;
      sfor<hi - lo>([&](auto KI) {
        constexpr int k = lo + KI;
        if constexpr (k < NS) sm(IC<t - 1>{}, IC<k>{});
        else qp(IC<4 + (k - NS) / 17>{}, IC<(k - NS) % 17>{});
      });
      SCHED;
    });
  });
  STAMP(3);
  u32x4 ones = u32x4{0x3F803F80u, 0x3F803F80u, 0x3F803F80u, 0x3F803F80u};
  asm volatile("" : "+v"(ones));
  nop<1>();
  auto pop = [&](auto UI) {
    sfor<12>([&](auto KI) {
      constexpr int op = P_P.op[UI][KI], j = op % 1000;
      if constexpr (op >= PWL) wait_lgkm<j>();
      else if constexpr (op >= PWV) wait_vm<j>();
      else if constexpr (op >= PB) ob[(j - 500) >> 1][j & 1] = sob[(j - 500) >> 1][j & 1] + vo;
      else if constexpr (op >= PX) wsof(IC<3>{});
      else if constexpr (op >= PP) A[0][j] = aperm(WC[0], 0, j);
      else if constexpr (op >= PR) ord(IC<0>{}, IC<j>{});
      else if constexpr (op >= PW) wold(IC<j / 8>{}, IC<j % 8>{});
      else if constexpr (op >= PO) oe(IC<j / 64>{}, IC<j % 64>{});
      else if constexpr (op >= PVR) vrd(IC<1>{}, IC<j>{});
      else if constexpr (op >= PS) sm(IC<5>{}, IC<j>{});
      if constexpr (op > 0) SCHED;
    });
  };
  sfor<PV_N>([&](auto UI) {
    constexpr int u = UI, t = u / 27, m = u % 27, t3 = pv_t3(t, m), f = pv_f(t, m);
    if constexpr (m == 0) BSTAMP(7 + t);
    if constexpr (f > 0) mf_v_av<A_V + 4 * (t3 * 8 + f - 1), pv_z(t, m)>(oacc[t & 1][f - 1], Pf[t][t3]);
    else mf_v_vv<pv_z(t, m)>(lacc[t & 1], ones, Pf[t][t3]);
    SCHED;
    pop(UI);
  });
  BSTAMP(13);
  STAMP(4);

  auto tord = [&](auto PI) {
    constexpr int p = PI;
    ordx(IC<C_NS + C_P.bs[p]>{}, IC<C_P.bt[p]>{}, IC<C_B + 24 * ((C_NS + p / 6) & 1) + 4 * (p % 6)>{});
  };
  u32x4 CA[CT][8];
  auto cperm = [&](auto SI, auto KI) {
    constexpr int S = SI, k = KI;
    CA[S - C_NS][k >> 2][k & 3] = aperm(WC[S], k >> 2, k & 3);
  };
  sfor<8 * C_NS>([&](auto NI) {
    constexpr int n = NI, s = n >> 3, f = n & 7;
    constexpr bool cv = s >= C_S0;
    if constexpr (n == 8) BSTAMP(14);
    if constexpr (n == 16) BSTAMP(15);
    if constexpr (n == 24) {
      barrier();
      SCHED;
    }
    sfor<6>([&](auto TI) {
      constexpr int t = TI;
      mf_a_va<4 * (8 * t + f), C_B + 24 * (s & 1) + 4 * t, s == 0>(A[n & 1]);
      SCHED;
      constexpr int v = 2 * f + (t >> 2), li = c_cum(s) + v, ln = c_cum(s) + 2 * f + 2;
      constexpr bool ld = (t == 3 || t == 5) && v < c_n(s);
      if constexpr (ld) wold(IC<li / 8>{}, IC<li % 8>{});
      if constexpr (t == 0) {
        if constexpr (c_rdt(s, f) >= 0 && f < 7) {
          if constexpr (s + 1 < C_NS) ord(IC<s + 1>{}, IC<c_rdt(s, f)>{});
          else tord(IC<f>{});
        }
        if constexpr (f < 7 && ln % 8 == 0 && ln <= c_cum(s + 1)) wsof(IC<ln / 8>{});
        if constexpr (f == 1 && cv) wait_vm<2>();
        if constexpr (f == 7) {
          if constexpr (s + 1 < C_NS) wait_vm<c_cum(s + 1) - 8 * (s + 2)>();
          wait_lgkm0();
        }
      }
      if constexpr (n + 1 < 8 * C_NS && (t == 1 || t == 2)) {
        A[(n + 1) & 1][2 * t - 2] = aperm(WC[(n + 1) >> 3], (n + 1) & 7, 2 * t - 2);
        A[(n + 1) & 1][2 * t - 1] = aperm(WC[(n + 1) >> 3], (n + 1) & 7, 2 * t - 1);
      }
      if constexpr (s < PV_X) pop(IC<PV_N + 6 * n + t>{});
      constexpr int cg = f == 0 ? -1 : f < 4 ? (t == 4 ? f - 1 : -1) : t >= 3 ? 3 * (f - 3) + t - 3 : (f == 6 && t == 0 ? 15 : -1);
      if constexpr (cv && cg >= 0) {
        cperm(IC<(cv ? s + CT - 2 : C_NS)>{}, IC<2 * (cg < 0 ? 0 : cg)>{});
        cperm(IC<(cv ? s + CT - 2 : C_NS)>{}, IC<2 * (cg < 0 ? 0 : cg) + 1>{});
      }
      SCHED;
    });
  });
  const rsrc_t rp = __builtin_amdgcn_make_buffer_rsrc((void*)(part + (size_t)(((br * NBATCH + b) * 8 + hg) * 4 + w) * 24 * 64), 0,
                                                      24 * 1024, RSRC_W3);
  u32x4 sd[2];
  float tb[8];
  auto stp = [&](auto MI) {
    constexpr int m = MI, t = C_P.ct[m / 52], r = (m % 52) / 13, e = m % 13;
    constexpr int rd[13] = {0, 1, 2, 3, -1, 4, 5, -1, 6, 7, -1, -1, -1}, pk[13] = {-1, -1, -1, -1, 0, -1, -1, 1, -1, -1, 2, 3, -1};
    if constexpr (rd[e] >= 0) asm volatile("v_accvgpr_read_b32 %0, a[%c1]" : "=v"(tb[rd[e]]) : "i"(4 * (8 * t + rd[e]) + r));
    else if constexpr (pk[e] >= 0) sd[r & 1][pk[e]] = pk_bf16(tb[2 * pk[e]], tb[2 * pk[e] + 1]);
    else __builtin_amdgcn_raw_buffer_store_b128(sd[r & 1], rp, vo + r * 1024, t * 4096, AUX_PST);
    if constexpr (e == 2 && m >= 13) asm volatile("" ::"v"(sd[(r & 1) ^ 1]));
    SCHED;
  };
  sfor<48 * CT>([&](auto UI) {
    constexpr int u = UI, p = u >> 3, f = u & 7;
    mf_a_va<4 * (8 * C_P.bt[p] + f), C_B + 24 * ((C_NS + p / 6) & 1) + 4 * (p % 6), false>(CA[C_P.bs[p]][f]);
    SCHED;
    sfor<3>([&](auto KI) {
      constexpr int op = C_P.op[u][KI], j = op % 1000;
      if constexpr (op >= TWL) wait_lgkm0();
      else if constexpr (op >= TWV) wait_vm<j>();
      else if constexpr (op >= TS) stp(IC<j>{});
      else if constexpr (op >= TB) tord(IC<j>{});
      else if constexpr (op >= TP) cperm(IC<14 + j / 32>{}, IC<j % 32>{});
      else if constexpr (op >= TL) wold(IC<15>{}, IC<j>{});
      SCHED;
    });
  });
  STAMP(5);
  nop<7>();
  sfor<6 * 52 - C_P.nst>([&](auto KI) { stp(IC<C_P.nst + KI>{}); });
  STAMP(6);
}

__global__ __launch_bounds__(256) void k2_reduce(const u32x4* __restrict__ part, unsigned short* __restrict__ out) {
  const int bb = blockIdx.x / 24, j = blockIdx.x % 24;
  const int tid = threadIdx.x;
  const rsrc_t rp = __builtin_amdgcn_make_buffer_rsrc((void*)(part + (size_t)bb * 8 * 6144 + j * 256), 0, 8 * 6144 * 16, RSRC_W3);
  u32x4 v[8];
#pragma unroll
  for (int h = 0; h < 8; ++h) v[h] = __builtin_amdgcn_raw_buffer_load_b128(rp, tid * 16, h * 6144 * 16, AUX_PLD);
  SCHED;
  const int t = j % 6, w = j / 6, br = bb >> 4, b = bb & 15;
  unsigned short* po = out + ((size_t)b * SQ + 16 * t) * 1024 + br * 512 + 128 * w;
  int vo = (tid & 15) * 2048 + (tid & 48) * 4 + (tid >> 6) * 16;
  unsigned hi = 0xffff0000u;
  asm volatile("" : "+s"(po), "+v"(vo), "+s"(hi));
  const rsrc_t ro = __builtin_amdgcn_make_buffer_rsrc((void*)po, 0, 15 * 2048 + 256, RSRC_W3);
  f32x2 s[4];
#pragma unroll
  for (int h = 0; h < 8; ++h) {
#pragma unroll
    for (int i = 0; i < 4; ++i) {
      const f32x2 x = f32x2{__builtin_bit_cast(float, v[h][i] << 16), __builtin_bit_cast(float, v[h][i] & hi)};
      s[i] = h ? s[i] + x : x;
    }
    asm volatile("" : "+v"(s[0]), "+v"(s[1]), "+v"(s[2]), "+v"(s[3]));
  }
  u32x4 o;
#pragma unroll
  for (int i = 0; i < 4; ++i) o[i] = pk_bf16(s[i][0], s[i][1]);
  __builtin_amdgcn_raw_buffer_store_b128(o, ro, vo, 0, 0);
  asm volatile(".Lk2_end:");
}

extern "C" void kernel_launch(void* const* d_in, const int* in_sizes, int n_in, void* d_out, int out_size, void* d_ws,
                              size_t ws_size, hipStream_t stream) {
  Ptrs p;
  for (int br = 0; br < 2; ++br) {
    p.x[br] = (const unsigned short*)d_in[5 * br + 0];
    p.k[br] = (const unsigned short*)d_in[5 * br + 1];
    p.v[br] = (const unsigned short*)d_in[5 * br + 2];
    p.wq[br] = (const unsigned short*)d_in[5 * br + 3];
    p.wo[br] = (const unsigned short*)d_in[5 * br + 4];
  }
  u32x4* part = (u32x4*)d_ws;
  (void)hipFuncSetAttribute((const void*)k1_fused, hipFuncAttributeMaxDynamicSharedMemorySize, LDS_BYTES);
  k1_fused<<<256, 256, LDS_BYTES, stream>>>(p, part
  );
  k2_reduce<<<2 * 16 * 96 * 64 / 256, 256, 0, stream>>>(part, (unsigned short*)d_out);
}
```

```cpp
#include <hip/hip_runtime.h>
#include <hip/hip_bf16.h>
#include <utility>

typedef unsigned u32x2 __attribute__((ext_vector_type(2)));
typedef unsigned u32x4 __attribute__((ext_vector_type(4)));
typedef float f32x4 __attribute__((ext_vector_type(4)));
#define LDSP __attribute__((address_space(3)))
#define DI __device__ __forceinline__
typedef __amdgpu_buffer_rsrc_t rsrc_t;

constexpr int SQ = 96, DM = 512, NH = 32, DH = 128, NBATCH = 16;
constexpr int RSRC_W3 = 0x00020000;
#define POL_KV " nt"
#define POL_W ""
#define POL_PF ""
#define POL_X ""
#define AUX_PST 16
#define AUX_PLD 0

template <class F, int... I>
DI void sfor_impl(F&& f, std::integer_sequence<int, I...>) {
  (f(std::integral_constant<int, I>{}), ...);
}
template <int N, class F>
DI void sfor(F&& f) {
  sfor_impl(f, std::make_integer_sequence<int, N>{});
}
template <int N>
using IC = std::integral_constant<int, N>;

typedef float f32x2 __attribute__((ext_vector_type(2)));
typedef __bf16 bf16x2 __attribute__((ext_vector_type(2)));
DI unsigned pk_bf16(float lo, float hi) {
  return __builtin_bit_cast(unsigned, __builtin_convertvector(f32x2{lo, hi}, bf16x2));
}
DI unsigned perm_lo(unsigned e, unsigned o) { return __builtin_amdgcn_perm(o, e, 0x05040100u); }
DI unsigned perm_hi(unsigned e, unsigned o) { return __builtin_amdgcn_perm(o, e, 0x07060302u); }
#define SCHED __builtin_amdgcn_sched_barrier(0)

#define MF "v_mfma_f32_16x16x32_bf16 "
template <int D, int Bq, bool Z>
DI void mf_a_va(const u32x4& A) {
  if constexpr (Z) asm volatile(MF "a[%c0:%c1], %2, a[%c3:%c4], 0" ::"i"(D), "i"(D + 3), "v"(A), "i"(Bq), "i"(Bq + 3));
  else asm volatile(MF "a[%c0:%c1], %2, a[%c3:%c4], a[%c0:%c1]" ::"i"(D), "i"(D + 3), "v"(A), "i"(Bq), "i"(Bq + 3));
}
template <int Bq, bool Z>
DI void mf_v_va(f32x4& C, const u32x4& A) {
  if constexpr (Z) asm volatile(MF "%0, %1, a[%c2:%c3], 0" : "=&v"(C) : "v"(A), "i"(Bq), "i"(Bq + 3));
  else asm volatile(MF "%0, %1, a[%c2:%c3], %0" : "+v"(C) : "v"(A), "i"(Bq), "i"(Bq + 3));
}
template <int Aq, bool Z>
DI void mf_v_av(f32x4& C, const u32x4& Bv) {
  if constexpr (Z) asm volatile(MF "%0, a[%c2:%c3], %1, 0" : "=&v"(C) : "v"(Bv), "i"(Aq), "i"(Aq + 3));
  else asm volatile(MF "%0, a[%c2:%c3], %1, %0" : "+v"(C) : "v"(Bv), "i"(Aq), "i"(Aq + 3));
}
template <bool Z>
DI void mf_v_vv(f32x4& C, const u32x4& A, const u32x4& Bv) {
  if constexpr (Z) asm volatile(MF "%0, %1, %2, 0" : "=&v"(C) : "v"(A), "v"(Bv));
  else asm volatile(MF "%0, %1, %2, %0" : "+v"(C) : "v"(A), "v"(Bv));
}
template <int X, int Y>
DI unsigned pk_acc() {
  unsigned r, t0, t1;
  asm volatile("v_accvgpr_read_b32 %1, a[%c3]\n\tv_accvgpr_read_b32 %2, a[%c4]\n\tv_cvt_pk_bf16_f32 %0, %1, %2"
               : "=v"(r), "=&v"(t0), "=&v"(t1)
               : "i"(X), "i"(Y));
  return r;
}
template <int IMM>
DI void ld_w(u32x4& d, rsrc_t r, int voff, int soff) {
  asm volatile("buffer_load_dwordx4 %0, %1, %2, %3 offen offset:%c4" POL_W : "=v"(d) : "v"(voff), "s"(r), "s"(soff), "i"(IMM));
}
template <int D, int IMM>
DI void ld_k(rsrc_t r, int voff, int soff) {
  asm volatile("buffer_load_dwordx4 a[%c0:%c1], %2, %3, %4 offen offset:%c5" POL_KV ::"i"(D), "i"(D + 3), "v"(voff), "s"(r),
               "s"(soff), "i"(IMM));
}
template <int D>
DI void ld_pf(rsrc_t r, int voff, int soff) {
  asm volatile("buffer_load_dwordx4 a[%c0:%c1], %2, %3, %4 offen" POL_PF ::"i"(D), "i"(D + 3), "v"(voff), "s"(r), "s"(soff));
}
DI void ld_pfv(u32x4& d, rsrc_t r, int voff, int soff) {
  asm volatile("buffer_load_dwordx4 %0, %1, %2, %3 offen" POL_PF : "+v"(d) : "v"(voff), "s"(r), "s"(soff));
}
template <int C>
DI void set_m0(int base) {
  asm volatile("s_add_i32 m0, %0, %c1" ::"s"(base), "i"(C) : "scc", "memory");
}
template <int IMM>
DI void dma_x(rsrc_t r, int voff, int soff) {
  asm volatile("buffer_load_dwordx4 %0, %1, %2 offen offset:%c3" POL_X " lds" ::"v"(voff), "s"(r), "s"(soff), "i"(IMM) : "memory");
}
template <int IMM>
DI void dma_v(rsrc_t r, int voff) {
  asm volatile("buffer_load_dwordx4 %0, %1, m0 offen offset:%c2" POL_KV " lds" ::"v"(voff), "s"(r), "i"(IMM) : "memory");
}
template <int D, int IMM>
DI void lds_rd_a(int addr) {
  asm volatile("ds_read_b128 a[%c0:%c1], %2 offset:%c3" ::"i"(D), "i"(D + 3), "v"(addr), "i"(IMM) : "memory");
}
template <int D, int IMM>
DI void lds_rdtr_a(int addr) {
  asm volatile("ds_read_b64_tr_b16 a[%c0:%c1], %2 offset:%c3" ::"i"(D), "i"(D + 1), "v"(addr), "i"(IMM) : "memory");
}
template <int D, int IMM>
DI void lds_wr_a(int addr) {
  asm volatile("ds_write_b128 %2, a[%c0:%c1] offset:%c3" ::"i"(D), "i"(D + 3), "v"(addr), "i"(IMM) : "memory");
}
template <int IMM>
DI void lds_wr(int addr, const u32x4& d) {
  asm volatile("ds_write_b128 %0, %1 offset:%c2" ::"v"(addr), "v"(d), "i"(IMM) : "memory");
}
template <int N>
DI void wait_vm() {
  static_assert(N >= 0 && N < 64, "vmcnt");
  asm volatile("s_waitcnt vmcnt(%c0)" ::"i"(N) : "memory");
}
DI void wait_lgkm0() { asm volatile("s_waitcnt lgkmcnt(0)" ::: "memory"); }
template <int N>
DI void wait_lgkm() {
  static_assert(N >= 0 && N < 16, "lgkmcnt");
  asm volatile("s_waitcnt lgkmcnt(%c0)" ::"i"(N) : "memory");
}
DI void barrier() { asm volatile("s_barrier" ::: "memory"); }
template <int N>
DI void nop() {
  asm volatile("s_nop %c0" ::"i"(N));
}
DI float max3(float a, float b, float c) { return __builtin_elementwise_maximum(__builtin_elementwise_maximum(a, b), c); }
DI void xmax16(float& a) {
  float t;
  asm volatile("v_mov_b32 %1, %0\n\ts_nop 1\n\tv_permlane16_swap_b32 %0, %1\n\tv_max_f32 %0, %0, %1" : "+v"(a), "=&v"(t));
}
DI void xmax32s(float& a, float s) {
  float t;
  asm volatile("v_mov_b32 %1, %0\n\ts_nop 1\n\tv_permlane32_swap_b32 %0, %1\n\tv_max_f32 %0, %0, %1\n\tv_mul_f32 %0, %2, %0"
               : "+v"(a), "=&v"(t)
               : "s"(s));
}
template <int X, int Y>
DI void acc_rd2(float& a, float& b) {
  asm volatile("v_accvgpr_read_b32 %0, a[%c2]\n\tv_accvgpr_read_b32 %1, a[%c3]" : "=v"(a), "=v"(b) : "i"(X), "i"(Y));
}

#define STAMP(i)

#define BSTAMP(i)
#define HALFX(n) (n)
struct Ptrs {
  const unsigned short* x[2];
  const unsigned short* k[2];
  const unsigned short* v[2];
  const unsigned short* wq[2];
  const unsigned short* wo[2];
};

constexpr int LDS_BYTES = 163840;
constexpr int XQ_BYTES = 24576, V1_BASE = 98304;

constexpr int A_B = 112, A_K = 160, A_V = 0;
constexpr bool tile_v(int f, int t) { return t < 2 || (t == 2 && f < 4); }
constexpr int tile_vi(int f, int t) { return t * 8 + f; }
constexpr int tile_a(int f, int t) { return 4 * (t >= 3 ? (t - 3) * 8 + f : 24 + (f - 4)); }

#define PF_WO 8
enum { OX = 1000, OK_ = 2000, OP = 3000, OV = 4000, OW = 5000 };
struct ASched {
  int op[16][24];
  bool bar[16];
};
#define SCHV 5
#define PFPV 6
#define STAG 16
#define X6(q) OX + 6 * q, OX + 6 * q + 1, OX + 6 * q + 2, OX + 6 * q + 3, OX + 6 * q + 4, OX + 6 * q + 5
#define K4(t) OK_ + 4 * t, OK_ + 4 * t + 1, OK_ + 4 * t + 2, OK_ + 4 * t + 3
#define V4(j) OV + j, OV + j + 1, OV + j + 2, OV + j + 3
#define PRO_OP 2
constexpr int A_BURST[6][12] = {{X6(1), OP + 4}, {X6(2), OP + 5}, {OP + 6, K4(0), OK_ + 4, OK_ + 5},
                                {X6(3), OP + 7}, {OK_ + 6, OK_ + 7, K4(2), OK_ + 12}, {OK_ + 13, OK_ + 14, OK_ + 15, K4(4)}};
constexpr int A_TAIL[3][8] = {{K4(5), OV + 0, OV + 1, OV + 2}, {OV + 3, V4(4), OV + 8}, {OV + 9, OV + 10, OV + 16, OV + 17, OV + 18}};
constexpr int A_KST[6] = {8, 11, 12, 13, 14, 15};
constexpr int B_V[] = {19, 20, 21, 22, 23, 11, 12, 13, 14, 15};
constexpr int B_NV = 10, B_PFT = 4;
constexpr ASched a_make() {
  ASched a{};
  for (int s = 0; s <= 12; s += 2)
    for (int i = 0; i < 16; ++i) a.op[s][i] = OW + 8 * (s + 2) + i;
  for (int s = 1; s <= 11; s += 2) {
    a.bar[s] = true;
    for (int i = 0; i < (int)(sizeof(A_BURST[0]) / sizeof(int)); ++i) a.op[s][i] = A_BURST[s >> 1][i];
  }
  for (int s = 13; s < 16; ++s) {
    int n = 0;
    while (n < 8 && A_TAIL[s - 13][n]) ++n;
    for (int i = 0; i < n; ++i) a.op[s][3 * (i * 8 / n)] = A_TAIL[s - 13][i];
  }
  return a;
}
constexpr ASched A_S = a_make();
constexpr int A_NP = 3 + 6 + PRO_OP + 16;
constexpr int a_seq(int op) {
  if (op >= OW && op < OW + 16) return 9 + PRO_OP + op - OW;
  int c = A_NP;
  for (int s = 0; s < 16; ++s)
    for (int k = 0; k < 24; ++k)
      if (A_S.op[s][k]) {
        if (A_S.op[s][k] == op) return c;
        ++c;
      }
  return -1;
}
constexpr int a_issued(int s, int k) {
  int c = A_NP;
  for (int i = 0; i < 16; ++i)
    for (int j = 0; j < 24; ++j)
      if (A_S.op[i][j] && (i < s || (i == s && j < k))) ++c;
  return c;
}
constexpr int a_kt(int s) {
  for (int k = 0; k < 6; ++k)
    if (A_KST[k] == s) return k;
  return -1;
}
constexpr int imax(int a, int b) { return a > b ? a : b; }
constexpr int a_need(int s) {
  int n = 0;
  for (int i = 0; i < 8; ++i) n = imax(n, a_seq(OW + 8 * (s + 1) + i));
  if (s == 2 || s == 6 || s == 10)
    for (int i = 0; i < 6; ++i) n = imax(n, a_seq(OX + 6 * ((s + 2) / 4) + i));
  if (a_kt(s + 1) >= 0)
    for (int i = 0; i < 4; ++i) n = imax(n, a_seq(OK_ + 4 * a_kt(s + 1) + i));
  if (s == 14)
    for (int i = 2; i < 8; ++i) n = imax(n, a_seq(OP + i));
  return n;
}
constexpr int a_wait(int s) { return a_issued(s, s < 13 ? 14 : 21) - a_need(s) - 1; }
constexpr int v_jb(int j) {
  int jb = -99;
  for (int i = 0; i < 16 * 24 + B_NV; ++i) {
    const int op = i < 16 * 24 ? A_S.op[i / 24][i % 24] : OV + B_V[i - 16 * 24];
    if (op >= OX && op < OK_) jb = -99;
    if (op < OV || op >= OW) continue;
    const int v = op - OV;
    if (!(v >= jb && v - jb < 4 && (v < 16) == (jb < 16))) jb = v;
    if (v == j) return jb;
  }
  return -1;
}
constexpr int a_wait_w(int ws, int s, int k) {
  int n = 0;
  for (int i = 0; i < 8; ++i) n = imax(n, a_seq(OW + 8 * ws + i));
  return a_issued(s, k) - n - 1;
}
constexpr bool a_check() {
  for (int i = 0; i < 24; ++i)
    if (a_seq(OK_ + i) < 0) return false;
  for (int i = 6; i < 24; ++i)
    if (a_seq(OX + i) < 0) return false;
  for (int i = 0; i < 128; ++i)
    if (a_seq(OW + i) < 0 || (i >= 32 && a_seq(OW + i) < a_issued(i / 8 - 3, 0))) return false;
  int nv = B_NV;
  for (int i = 0; i < 24; ++i)
    if (a_seq(OV + i) >= 0) {
      ++nv;
      if (i >= 11 && i < 16 && a_seq(OV + i) < a_issued(A_KST[5] + 1, 0)) return false;
      if (i >= 16 && a_seq(OV + i) < a_issued(7, 0)) return false;
    }
  for (int s = 0; s < 15; ++s)
    if (a_wait(s) < 0 || a_wait(s) > 63) return false;
  return nv == 24 && A_S.bar[3] && A_S.bar[7] && A_S.bar[11];
}
static_assert(a_check(), "phase A schedule");

#define CT 6
constexpr int C_NS = 16 - CT, C_S0 = C_NS - CT + 2, C_B = 192;
constexpr int c_n(int s) { return s < C_S0 && 4 * s < 8 * (CT - 4) ? 12 : 8; }
constexpr int c_cum(int s) {
  int c = 24;
  for (int i = 0; i < s; ++i) c += c_n(i);
  return c;
}
static_assert(CT >= 4 && CT <= 6 && c_cum(C_NS) == 120, "phase C schedule");
enum { TL = 1000, TP = 2000, TB = 3000, TS = 4000, TWV = 5000, TWL = 6000 };
struct CPlan {
  int bt[6 * CT], bs[6 * CT];
  int ct[6];
  int op[48 * CT][3];
  int nst;
};
constexpr CPlan c_make() {
  CPlan c{};
  int nx[6] = {0, 0, 0, 0, 0, 0}, done[6] = {0, 0, 0, 0, 0, 0}, nc = 0;
  for (int p = 0; p < 6 * CT; ++p)
    for (int t = 0; t < 6; ++t)
      if (nx[t] < CT && (nx[t] < CT - 2 || (nx[t] == CT - 2 && p >= 3) || p >= 6)) {
        c.bt[p] = t;
        c.bs[p] = nx[t]++;
        if (nx[t] == CT) {
          c.ct[nc] = t;
          done[nc++] = 8 * p + 7;
        }
        break;
      }
  int used[48 * CT] = {};
  auto put = [&](int u, int op, int w) {
    int k = 0;
    while (c.op[u][k]) ++k;
    c.op[u][k] = op;
    used[u] += w;
  };
  for (int i = 0; i < 8; ++i) put(i, TL + i, 2);
  put(8, TWV + 8, 0);
  put(32, TWV + 0, 0);
  for (int k = 0; k < 32; ++k) {
    put(8 + k / 2, TP + k, 1);
    put(32 + k / 2, TP + 32 + k, 1);
  }
  for (int r = 0; r + 1 < CT; ++r) {
    for (int j = 0; j < 6; ++j) put(r ? 48 * r + 6 * j : 24 + j, TB + 6 * (r + 1) + j, 1);
    put(48 * r + 46, TWL, 0);
  }
  int u = 0, m = 0;
  for (; m < 6 * 52; ++m) {
    const int w = m % 13 == 12 ? 2 : 1;
    if (u < done[m / 52] + 2) u = done[m / 52] + 2;
    while (u < 48 * CT && used[u] + w > 2) ++u;
    if (u >= 48 * CT) break;
    put(u, TS + m, w);
  }
  c.nst = m;
  return c;
}
constexpr CPlan C_P = c_make();

constexpr int pv_t3(int t, int m) { return t == 0 ? (m < 9 ? 0 : m < 18 ? 2 : 1) : (m < 9 ? m / 3 : ((m - 9) % 6) / 2); }
constexpr int pv_f(int t, int m) { return t == 0 ? m % 9 : (m < 9 ? m % 3 : 3 + 2 * ((m - 9) / 6) + (m - 9) % 2); }
constexpr bool pv_z(int t, int m) { return t == 0 ? m < 9 : pv_t3(t, m) == 0; }
constexpr int pv_first(int t, int f) {
  for (int m = 0; m < 27; ++m)
    if (pv_f(t, m) == f) return 27 * t + m;
  return -1;
}
constexpr int pv_last(int t, int f) {
  for (int m = 26; m >= 0; --m)
    if (pv_f(t, m) == f) return 27 * t + m;
  return -1;
}
#define PV_R 177
constexpr int SM_N = 43, OE_N = 53, PV_N = 162;
constexpr int E_RD[13] = {0, 1, 2, 3, -1, 4, 5, -1, 6, 7, -1, -1, -1}, E_PK[13] = {-1, -1, -1, -1, 0, -1, -1, 1, -1, -1, 2, 3, -1};
enum { PS = 1000, PVR = 2000, PO = 3000, PW = 4000, PR = 5000, PP = 6000, PX = 7000, PB = 7500, PWV = 8000, PWL = 9000 };
constexpr int PV_X = 3;
constexpr int c_rdt(int s, int f) { return s < PV_X ? (f < 4 ? f + 2 : f - 5) : (f < 6 ? f : -1); }
struct PPlan {
  int op[PV_N + 48 * PV_X][12];
  bool ok;
};
constexpr PPlan p_make() {
  PPlan c{};
  int it[512] = {}, cost[512] = {}, rdy[512] = {}, dl[512] = {}, n = 0;
  auto add = [&](int op, int w, int r, int d) {
    it[n] = op;
    cost[n] = w;
    rdy[n] = r;
    dl[n++] = d;
  };
  auto piece = [&](int t, int sp, int nt) {
    for (int e = 0; e < 13; ++e) {
      const int f = 1 + 2 * sp + (E_RD[e] >> 2);
      if (e == 12 && nt >= 0) add(PO + 64 * nt, 2, pv_last(nt, 0) + 3, nt < 4 ? pv_first(nt + 2, 0) : 999);
      add(PO + 64 * t + 1 + 13 * sp + e, 1, E_RD[e] >= 0 ? pv_last(t, f) + 3 : 0,
          t < 4 ? (E_RD[e] >= 0 ? pv_first(t + 2, f) : PV_N) : (sp ? PV_N + 48 * (sp - 1) + 6 * (t + 1) : PV_N));
    }
  };
  for (int j = 0; j < SM_N; ++j) {
    if (j == SM_N - 1) add(PO, 2, pv_last(0, 0) + 3, pv_first(2, 0));
    add(PS + j, j < 5 ? 2 : j < 7 ? 6 : j < 31 ? 3 : 1, 0, 27 * 5 - 1);
  }
  for (int t = 0; t < 4; ++t)
    for (int sp = 0; sp < 4; ++sp) piece(t, sp, sp == 3 ? t + 1 : -1);
  piece(4, 0, -1);
  piece(4, 1, 5);
  piece(5, 0, -1);
  for (int k = 0; k < 6; ++k) add(PR + (k + 2) % 6, 1, 0, PV_N - 3);
  add(PWV + 99, 0, 0, PV_N - 1);
  for (int k = 0; k < 4; ++k) add(PP + k, 1, 0, PV_N - 1);
  add(PX, 1, 0, PV_N);
  piece(5, 1, -1);
  for (int sp = 2; sp < 4; ++sp) {
    piece(4, sp, -1);
    piece(5, sp, -1);
  }
  for (int k = 2; k < 8; ++k) add(PB + k, 1, 0, PV_N + 48 * PV_X);
  int placed = 0, idx = 0, nw = 0, nds = 0;
  c.ok = true;
  for (int g = 0; g < PV_N + 48 * PV_X; ++g) {
    int k = 0, used = 0;
    auto put = [&](int op, int w) {
      c.op[g][k++] = op;
      placed += w;
      used += w;
    };
    if (g == 1) {
      put(PWL + 0, 0);
      put(PWV + PF_WO, 0);
    }
    for (int i = 0; i < 16; ++i)
      if (g == 2 + i * 12 / 16) put(PVR + i, 1);
    if (g == 17) put(PWL + 0, 0);
    for (int i = 0; i < 24; ++i)
      if (g == 81 + i * 80 / 24) {
        put(PW + i, i % 8 ? 2 : 3);
        ++nw;
      }
    const int cs = (g - PV_N) / 48, cf = (g - PV_N) % 48 / 6, ctt = (g - PV_N) % 6;
    const int cap = g < PV_N ? 99 : (ctt == 4 || ((ctt == 3 || ctt == 5) && 2 * cf + (ctt >> 2) >= c_n(cs)) ? 2 : 0);
    while (idx < n && rdy[idx] <= g && used + cost[idx] <= cap && (g >= PV_N || placed * 64 < PV_R * (g + 1))) {
      const int op = it[idx];
      if (g >= dl[idx]) c.ok = false;
      if (op >= PR && op < PP) nds = 0;
      else if (op >= PO && op < PW && (op - PO) % 64 > 0 && ((op - PO) % 64 - 1) % 13 == 12) ++nds;
      if (op == PWV + 99 && nw < 8) c.ok = false;
      put(op == PWV + 99 ? PWV + nw - 8 : op, cost[idx]);
      ++idx;
    }
    if (g == PV_N - 1) put(PWL + nds, 0);
    if (k > 11) c.ok = false;
  }
  c.ok = c.ok && idx == n && nw == 24;
  return c;
}
constexpr PPlan P_P = p_make();
static_assert(P_P.ok, "PV schedule");

DI unsigned aperm(const u32x4 (&W)[8], int f, int p) {
  return (f & 1) ? perm_hi(W[2 * p][f >> 1], W[2 * p + 1][f >> 1]) : perm_lo(W[2 * p][f >> 1], W[2 * p + 1][f >> 1]);
}

__global__ __launch_bounds__(256, 1) void k1_fused(Ptrs p, u32x4* part
) {
  extern __shared__ __attribute__((aligned(16))) char lds[];
  asm volatile("" ::: "a0", "a255");
  const int bid = blockIdx.x;
  const unsigned short *px, *pk, *pv, *pq, *po;
  asm volatile(
      "s_load_dwordx2 %0, %6, %7 offset:0x30\n\ts_load_dwordx2 %1, %6, %7 offset:0x0\n\ts_load_dwordx2 %2, %6, %7 offset:0x10\n\t"
      "s_load_dwordx2 %3, %6, %7 offset:0x20\n\ts_load_dwordx2 %4, %6, %7 offset:0x40\n\ts_load_dwordx2 %5, %6, 0x50"
      : "=&s"(pq), "=&s"(px), "=&s"(pk), "=&s"(pv), "=&s"(po), "=&s"(part)
      : "s"(__builtin_amdgcn_kernarg_segment_ptr()), "s"((bid & 4) * 2));
  SCHED;
  const int xcd = bid & 7, idx = bid >> 3;
  const int br = xcd >> 2, hg = (xcd & 3) * 2 + (idx & 1), b = idx >> 1;
  const int tid = threadIdx.x, lane = tid & 63;
  const int w = __builtin_amdgcn_readfirstlane(tid >> 6);
  const int q = lane & 15, g = lane >> 4;
  const int cchunk = q;
  const int lb = (int)(unsigned)(size_t)(LDSP char*)lds;
  STAMP(0);

  const int vk = lane * 16;
  u32x4 pfd = u32x4{0, 0, 0, 0};
  {
    unsigned long long pc;
    int len;
    asm volatile("s_getpc_b64 %0\n.Lk1_pc:\n\ts_mov_b32 %1, .Lk2_end-.Lk1_pc" : "=s"(pc), "=s"(len));
    const rsrc_t rc = __builtin_amdgcn_make_buffer_rsrc((void*)pc, 0, len, RSRC_W3);
    nop<4>();
    ld_pfv(pfd, rc, vk, (idx * 4 + w) * 1024);
  }
  const int head = hg * 4 + w;
  const size_t kvoff = ((size_t)b * NH + head) * SQ * DH;

  f32x4 accv[20];
  u32x4 W[4][8];
  u32x4 A[2];

  auto gmf = [&](auto FI, auto TI, auto BI, auto ZI, const u32x4& Af) {
    constexpr int f = FI, t = TI, bq = A_B + 24 * BI + 4 * t;
    constexpr bool z = ZI;
    if constexpr (tile_v(f, t)) mf_v_va<bq, z>(accv[tile_vi(f, t)], Af);
    else mf_a_va<tile_a(f, t), bq, z>(Af);
  };
  auto gpk = [&](auto PI, auto TI, auto RI) -> unsigned {
    constexpr int pp = PI, t = TI, r = RI;
    if constexpr (tile_v(2 * pp, t)) return pk_bf16(accv[tile_vi(2 * pp, t)][r], accv[tile_vi(2 * pp + 1, t)][r]);
    else return pk_acc<tile_a(2 * pp, t) + r, tile_a(2 * pp + 1, t) + r>();
  };

  int vx = g * 1024 + ((q ^ g ^ (4 * w)) << 4);
  int vwq = (8 * g) * 8192 + (hg * 512 + w * 128 + 8 * cchunk) * 2;
  int sxb = lb + w * 1024, sxo[6];
#pragma unroll
  for (int i = 0; i < 6; ++i) sxo[i] = (4 * i + w) * 4096 + b * (SQ * DM * 2);
  int vpf = ((lane >> 5) * 32 + 2 * b + (w >> 1)) * 8192 + hg * 1024 + (w & 1) * 512 + (lane & 31) * 16;
  asm volatile("" : "+s"(sxb), "+s"(sxo[0]), "+s"(sxo[1]), "+s"(sxo[2]), "+s"(sxo[3]), "+s"(sxo[4]), "+s"(sxo[5]), "+v"(vx), "+v"(vwq),
               "+v"(vpf));
  nop<4>();
  SCHED;
  asm volatile("s_waitcnt lgkmcnt(0)" : "+s"(pq), "+s"(px), "+s"(pk), "+s"(pv), "+s"(po), "+s"(part));
  const rsrc_t rq = __builtin_amdgcn_make_buffer_rsrc((void*)pq, 0, DM * NH * DH * 2, RSRC_W3);
  const rsrc_t rx = __builtin_amdgcn_make_buffer_rsrc((void*)px, 0, NBATCH * SQ * DM * 2, RSRC_W3);
  const rsrc_t rk = __builtin_amdgcn_make_buffer_rsrc((void*)(pk + kvoff), 0, SQ * DH * 2, RSRC_W3);
  const rsrc_t ro = __builtin_amdgcn_make_buffer_rsrc((void*)(po + (size_t)hg * 512 * DM), 0, 512 * DM * 2, RSRC_W3);
  auto xm0 = [&](auto II) { set_m0<(II / 6) * (XQ_BYTES - 256) + (II % 6) * 4096>(sxb); };
  auto xdma = [&](auto II) { dma_x<(II / 6) * 256>(rx, vx, sxo[II % 6]); };
  STAMP(8);
  ld_pfv(pfd, rq, vpf, 0);
  ld_pfv(pfd, rq, vpf, 64 * 8192);
  sfor<6>([&](auto I) {
    xm0(I);
    nop<0>();
    xdma(I);
  });
  sfor<PRO_OP>([&](auto I) { ld_pfv(pfd, rq, vpf, (2 + I) * 64 * 8192); });
  STAMP(9);
  wait_vm<7 + PRO_OP>();
  STAMP(10);
  sfor<16>([&](auto I) { ld_w<0>(W[I >> 3][I & 7], rq, vwq, (32 * (I >> 3) + (I & 7)) * 8192); });
  int xa[2][4];
#pragma unroll
  for (int h = 0; h < 2; ++h)
#pragma unroll
    for (int j = 0; j < 4; ++j) xa[h][j] = lb + h * 2 * XQ_BYTES + q * 256 + (((4 * j + g) ^ q) << 4);
  const int kst = lb + V1_BASE + w * 16384 + 16384 - 16 * 272;
  const int kwa = kst + g * 272 + q * 16, kra = kst + q * 272 + g * 64;
  const int vv0 = g * 256 + ((q ^ (((g >> 1) << 2) | (g & 1))) << 4);
  const int vv1 = vv0 ^ 128;
  int svb[2] = {lb + V1_BASE + w * 16384, lb + w * 8192};
  const rsrc_t rv0 = __builtin_amdgcn_make_buffer_rsrc((void*)((const char*)(pv + kvoff) - svb[0]), 0, HALFX(svb[0] + 16384), RSRC_W3);
  const rsrc_t rv1 = __builtin_amdgcn_make_buffer_rsrc((void*)((const char*)(pv + kvoff) + 16384 - svb[1]), 0, HALFX(svb[1] + 8192), RSRC_W3);
  asm volatile("" : "+s"(svb[0]), "+s"(svb[1]));
  auto vm0 = [&](auto JI) {
    constexpr int j = JI;
    if constexpr (v_jb(j) == j) set_m0<(j & 15) * 1024>(svb[j >> 4]);
  };
  auto vdma = [&](auto JI) {
    constexpr int j = JI;
    dma_v<(j - v_jb(j)) * 1024>(j < 16 ? rv0 : rv1, (j & 1) ? vv1 : vv0);
  };
  int sob[4][2];
#pragma unroll
  for (int h = 1; h < 4; ++h) {
    sob[h][0] = lb + V1_BASE + ((w + h) & 3) * 16384;
    sob[h][1] = lb + ((w + h) & 3) * 8192;
    asm volatile("" : "+s"(sob[h][0]), "+s"(sob[h][1]));
  }
  STAMP(11);
  SCHED;
  if (b & 1) __builtin_amdgcn_s_sleep(STAG);
  wait_vm<A_NP - 9>();
  barrier();
  STAMP(1);

  constexpr int TG[3][2] = {{4, 5}, {2, 3}, {0, 1}};
  u32x4 AT[3][8];
  auto tperm = [&](auto KI) {
    constexpr int k = KI, m = k >> 1, d = 2 * (k & 1);
    AT[m >> 3][m & 7][d] = aperm(W[(13 + (m >> 3)) & 3], m & 7, d);
    AT[m >> 3][m & 7][d + 1] = aperm(W[(13 + (m >> 3)) & 3], m & 7, d + 1);
  };
  auto tmf = [&](auto GI, auto UI) {
    constexpr int G = GI, u = UI, sp = u >> 4, f = (u >> 1) & 7, tt = u & 1, t = TG[G][tt];
    constexpr int bq = A_B + 24 * ((G & 1) ^ 1) + 4 * (2 * sp + tt);
    if constexpr (tile_v(f, t)) mf_v_va<bq, false>(accv[tile_vi(f, t)], AT[sp][f]);
    else mf_a_va<tile_a(f, t), bq, false>(AT[sp][f]);
  };
  u32x4 Qf[6][4];
  float ta[2][2];
  auto qp = [&](auto II, auto JI) {
    constexpr int qi = II, t = TG[qi >> 1][qi & 1], j = JI, pp = (j & 15) >> 2, s = j & 3, i = j > 0 ? j - 1 : 0;
    if constexpr (j < 16) {
      if constexpr (tile_v(2 * pp, t)) Qf[qi][s][pp] = gpk(IC<pp>{}, IC<t>{}, IC<s>{});
      else acc_rd2<tile_a(2 * pp, t) + s, tile_a(2 * pp + 1, t) + s>(ta[j & 1][0], ta[j & 1][1]);
    }
    if constexpr (j > 0 && !tile_v(2 * (i >> 2), t)) Qf[qi][i & 3][i >> 2] = pk_bf16(ta[i & 1][0], ta[i & 1][1]);
  };

  auto xrdx = [&](auto SI, auto TI, auto DI_) {
    constexpr int s = SI, t = TI;
    lds_rd_a<DI_, ((s >> 2) & 1) * XQ_BYTES + t * 4096>(xa[s >> 3][s & 3]);
  };
  auto xrd = [&](auto SI, auto TI) { xrdx(SI, TI, IC<A_B + 24 * (SI & 1) + 4 * TI>{}); };
  auto txrd = [&](auto GI, auto JI) {
    constexpr int G = GI, jj = JI;
    xrdx(IC<13 + jj / 2>{}, IC<TG[G][jj & 1]>{}, IC<A_B + 24 * ((G & 1) ^ 1) + 4 * jj>{});
  };
  int so[2];
  auto aprep = [&](auto OI, auto PI) {
    constexpr int op = OI, j = op % 1000, pi = PI;
    if constexpr (op >= OV && op < OW) vm0(IC<j>{});
    else if constexpr (op >= OK_) {
      so[pi] = op >= OW ? (32 * (j >> 3) + (j & 7)) * 8192 : op >= OP ? j * 64 * 8192 : (j >> 2) * 4096;
      asm volatile("" : "+s"(so[pi]));
    } else if constexpr (op >= OX) xm0(IC<j>{});
  };
  auto aop = [&](auto OI, auto PI) {
    constexpr int op = OI, j = op % 1000, pi = PI;
    if constexpr (op >= OW) ld_w<0>(W[(j >> 3) & 3][j & 7], rq, vwq, so[pi]);
    else if constexpr (op >= OV) vdma(IC<j>{});
    else if constexpr (op >= OP) ld_pfv(pfd, rq, vpf, so[pi]);
    else if constexpr (op >= OK_) ld_k<A_K + 4 * j, (j & 3) * 1024>(rk, vk, so[pi]);
    else if constexpr (op >= OX) xdma(IC<j>{});
  };
  sfor<6>([&](auto T) { xrd(IC<0>{}, T); });
  wait_vm<8>();
  SCHED;
#pragma unroll
  for (int pp = 0; pp < 4; ++pp) A[0][pp] = aperm(W[0], 0, pp);
  SCHED;
  wait_lgkm0();
  nop<0>();
  SCHED;
  sfor<104>([&](auto NI) {
    constexpr int n = NI, s = n >> 3, f = n & 7;
    constexpr int kt = a_kt(s), ktc = kt < 0 ? 0 : kt;
    if constexpr (f == 0 && A_S.bar[s]) {
      barrier();
      SCHED;
    }
    sfor<6>([&](auto TI) {
      constexpr int t = TI;
      gmf(IC<f>{}, TI, IC<(s & 1)>{}, std::bool_constant<s == 0>{}, A[n & 1]);
      SCHED;
      constexpr int opb = A_S.op[s][2 * f + 1];
      constexpr bool dmb = (opb >= OX && opb < OK_) || (opb >= OV && opb < OW);
      if constexpr (t == 0 && f == 7) wait_vm<a_wait(s)>();
      if constexpr (t == 0) aprep(IC<A_S.op[s][2 * f]>{}, IC<0>{});
      if constexpr (t == (dmb ? 4 : 0)) aprep(IC<opb>{}, IC<1>{});
      if constexpr (t == 4 && f < 6) {
        if constexpr (s < 12) xrd(IC<s + 1>{}, IC<f>{});
        else txrd(IC<0>{}, IC<f>{});
      }
      if constexpr (t == 3 || t == 5) aop(IC<A_S.op[s][2 * f + t / 4]>{}, IC<t / 4>{});
      if constexpr (t == 1 || t == 2) {
        if constexpr (n < 103) {
          A[(n + 1) & 1][2 * t - 2] = aperm(W[((n + 1) >> 3) & 3], (n + 1) & 7, 2 * t - 2);
          A[(n + 1) & 1][2 * t - 1] = aperm(W[((n + 1) >> 3) & 3], (n + 1) & 7, 2 * t - 1);
        } else tperm(IC<t - 1>{});
      }
      if constexpr (n == 103 && t == 4) tperm(IC<2>{});
      if constexpr (t == 4 && kt >= 0) {
        if constexpr (f < 4) lds_wr_a<A_K + 16 * ktc + 4 * f, f * 4 * 272>(kwa);
        else lds_rd_a<A_K + 16 * ktc + 4 * (f - 4), (f - 4) * 16>(kra);
      }
      if constexpr (t == 5 && f == 7) wait_lgkm<(kt >= 0 ? 2 : 0)>();
      SCHED;
    });
  });
  sfor<3>([&](auto GI) {
    constexpr int G = GI, s = 13 + G;
    constexpr int kt = a_kt(s), ktc = kt < 0 ? 0 : kt;
    sfor<48>([&](auto UI) {
      constexpr int u = UI;
      tmf(GI, UI);
      SCHED;
      if constexpr (u == 42 && s < 15) wait_vm<a_wait(s)>();
      if constexpr (G == 0 && u == 13) wait_vm<a_wait_w(14, 13, 6)>();
      if constexpr (G == 0 && u == 29) wait_vm<a_wait_w(15, 13, 14)>();
      if constexpr (u & 1) aop(IC<A_S.op[s][u / 2]>{}, IC<0>{});
      else aprep(IC<A_S.op[s][u / 2]>{}, IC<0>{});
      if constexpr (G == 0 && u + 3 < 48) tperm(IC<u + 3>{});
      if constexpr (G == 0 && u == 45) nop<0>();
      if constexpr (G < 2 && u % 6 == 5 && u / 6 < 6) txrd(IC<(G < 2 ? G + 1 : 0)>{}, IC<u / 6>{});
      if constexpr (kt >= 0 && u % 6 == 3) {
        if constexpr (u / 6 < 4) lds_wr_a<A_K + 16 * ktc + 4 * (u / 6), (u / 6) * 4 * 272>(kwa);
        else if constexpr (u / 6 < 7) lds_rd_a<A_K + 16 * ktc + 4 * (u / 6 - 4), (u / 6 - 4) * 16>(kra);
      }
      if constexpr (kt >= 0 && u == 41) lds_rd_a<A_K + 16 * ktc + 12, 48>(kra);
      if constexpr (u == 47) wait_lgkm0();
      if constexpr (G > 0 && u >= 2) {
        constexpr int lo = (u - 2) * 34 / 45 < 34 ? (u - 2) * 34 / 45 : 34, hi = (u - 1) * 34 / 45 < 34 ? (u - 1) * 34 / 45 : 34;
        sfor<hi - lo>([&](auto KI) { qp(IC<2 * (G > 0 ? G - 1 : 0) + (lo + KI) / 17>{}, IC<(lo + KI) % 17>{}); });
      }
      SCHED;
    });
  });
  asm volatile("" ::"v"(pfd));
  STAMP(2);

  f32x4 sacc[2][6];
  u32x4 Pf[6][3];
  f32x4 oacc[2][8];
  f32x4 lacc[2];
  float mA = 0.f, mB = 0.f, li[2] = {0.f, 0.f};
  float ev[24];
  const float c = 0.08838834764831845f * 1.4426950408889634f;

  auto sm = [&](auto TI, auto JI) {
    constexpr int t = TI, j = JI;
    const f32x4(&S)[6] = sacc[t & 1];
    auto v = [&](int i) -> float { return S[i >> 2][i & 3]; };
    if constexpr (j == 0) {
      mA = max3(v(0), v(1), v(2));
      mB = max3(v(3), v(4), v(5));
    } else if constexpr (j < 5) {
      mA = max3(mA, v(4 * j + 2), v(4 * j + 3));
      mB = max3(mB, v(4 * j + 4), v(4 * j + 5));
    } else if constexpr (j == 5) {
      mA = __builtin_elementwise_maximum(max3(mA, v(22), v(23)), mB);
      xmax16(mA);
    } else if constexpr (j == 6) xmax32s(mA, -c);
    else if constexpr (j < 7 + 24) {
      constexpr int i = j - 7;
      ev[i] = __builtin_amdgcn_exp2f(__builtin_fmaf(v(i), c, mA));
    } else {
      constexpr int i = j - 31;
      Pf[t][i >> 2][i & 3] = pk_bf16(ev[2 * i], ev[2 * i + 1]);
    }
  };
  const int vo = lane * 16;
  const int oa1 = lb + V1_BASE + w * 16384 + vo, oa2 = lb + w * 8192 + vo;
  u32x4 ot;
  float om[8];
  auto oe = [&](auto TI, auto KI) {
    constexpr int t = TI, k = KI;
    if constexpr (k == 0) {
      asm volatile("" : "+v"(lacc[t & 1]));
      li[t & 1] = __builtin_amdgcn_rcpf(lacc[t & 1][0]);
    } else {
      constexpr int sp = (k - 1) / 13, e = (k - 1) % 13, r = E_RD[e], pk = E_PK[e];
      if constexpr (r >= 0) asm volatile("v_mul_f32 %0, %1, %2" : "=v"(om[r]) : "v"(oacc[t & 1][2 * sp + (r >> 2)][r & 3]), "v"(li[t & 1]));
      else if constexpr (pk >= 0) ot[pk] = pk_bf16(om[2 * pk], om[2 * pk + 1]);
      else {
        constexpr int fi = sp * 6 + TG[t >> 1][t & 1];
        if constexpr (fi < 16) lds_wr<fi * 1024>(oa1, ot);
        else lds_wr<(fi - 16) * 1024>(oa2, ot);
      }
    }
  };
  const int vtr = (4 * g + ((lane >> 2) & 3)) * 256 +
                  (((lane & 3) ^ (((g & 1) << 3) | (((lane >> 3) & 1) << 2) | ((lane >> 2) & 1))) << 4) + 8 * (lane & 1);
  const int vt1 = lb + V1_BASE + w * 16384 + vtr, vt2 = lb + w * 8192 + vtr;
  auto vrd = [&](auto T3, auto JI) {
    constexpr int t3 = T3, j = JI, f = j >> 1, hh = j & 1, sp = f >> 1, h = f & 1;
    constexpr int x = (sp << 6) ^ (h << 3), d = A_V + 4 * (t3 * 8 + f) + 2 * hh;
    if constexpr (t3 < 2) lds_rdtr_a<d, (32 * t3 + 16 * hh) * 256>(vt1 ^ x);
    else lds_rdtr_a<d, 16 * hh * 256>(vt2 ^ x);
  };
  const int vwoA = (8 * g + 4 * (g & 1)) * 1024 + (w * 128 + 8 * cchunk) * 2;
  const int vwoB = (8 * g - 4 * (g & 1) + 4) * 1024 + (w * 128 + 8 * cchunk) * 2;
  u32x4 WC[16][8];
  int wso[16];
  auto wsof = [&](auto SI) {
    constexpr int S = SI;
    wso[S] = (((w + (S >> 2)) & 3) * 128 + 32 * (S & 3)) * 1024;
    asm volatile("" : "+s"(wso[S]));
  };
  auto wold = [&](auto SI, auto II) {
    constexpr int S = SI, i = II;
    if constexpr (S < 3 && i == 0) wsof(SI);
    ld_w<(i & 3) * 1024>(WC[S][i], ro, i < 4 ? vwoA : vwoB, wso[S]);
  };

  int ob[4][2] = {{oa1, oa2}};
  auto ordx = [&](auto SI, auto TI, auto DI_) {
    constexpr int S = SI, t = TI, d = DI_, fi = (S & 3) * 6 + t;
    if constexpr (fi < 16) lds_rd_a<d, fi * 1024>(ob[S >> 2][0]);
    else lds_rd_a<d, (fi - 16) * 1024>(ob[S >> 2][1]);
  };
  auto ord = [&](auto SI, auto TI) { ordx(SI, TI, IC<C_B + 24 * (SI & 1) + 4 * TI>{}); };

  sfor<6>([&](auto TI) {
    constexpr int t = TI;
    constexpr int NQ = t == 0 ? 34 : 0, NS = t > 0 ? SM_N : 0;
    constexpr int NF = NQ + NS, M0 = t == 0 ? 2 : 0, MN = 24 - M0;
    BSTAMP(t);
    if constexpr (t == 4) wait_vm<B_NV>();
    if constexpr (t == 5) {
      wait_lgkm0();
      wait_vm<B_NV - 5>();
    }
    if constexpr (t == 5) BSTAMP(6);
    sfor<24>([&](auto MI) {
      constexpr int m = MI, s = m / 6, kt = m % 6;
      mf_v_av<A_K + 4 * (kt * 4 + s), s == 0>(sacc[t & 1][kt], Qf[t][s]);
      SCHED;
      if constexpr (m % 8 == 1 && t < 4 && 3 * t + m / 8 < B_NV) vm0(IC<B_V[(3 * t + m / 8) < B_NV ? 3 * t + m / 8 : 0]>{});
      if constexpr (m % 8 == 2 && t < 4 && 3 * t + m / 8 < B_NV) vdma(IC<B_V[(3 * t + m / 8) < B_NV ? 3 * t + m / 8 : 0]>{});
      if constexpr (t == 5 && m % 3 == 0 && m / 3 < PF_WO) ld_pf<A_B + 4 * (m / 3)>(ro, vk, (32 * b + 8 * w + m / 3) * 1024);
      if constexpr (t >= 4 && m % 3 != 0) vrd(IC<(t == 4 ? 0 : 2)>{}, IC<m - m / 3 - 1>{});
      constexpr int m0 = m < M0 ? 0 : m - M0, m1 = m + 1 < M0 ? 0 : m + 1 - M0;
      constexpr int lo = m0 * NF / MN, hi = m1 * NF / MN;
      sfor<hi - lo>([&](auto KI) {
        constexpr int k = lo + KI;
        if constexpr (k < NS) sm(IC<t - 1>{}, IC<k>{});
        else qp(IC<4 + (k - NS) / 17>{}, IC<(k - NS) % 17>{});
      });
      SCHED;
    });
  });
  STAMP(3);
  u32x4 ones = u32x4{0x3F803F80u, 0x3F803F80u, 0x3F803F80u, 0x3F803F80u};
  asm volatile("" : "+v"(ones));
  nop<1>();
  auto pop = [&](auto UI) {
    sfor<12>([&](auto KI) {
      constexpr int op = P_P.op[UI][KI], j = op % 1000;
      if constexpr (op >= PWL) wait_lgkm<j>();
      else if constexpr (op >= PWV) wait_vm<j>();
      else if constexpr (op >= PB) ob[(j - 500) >> 1][j & 1] = sob[(j - 500) >> 1][j & 1] + vo;
      else if constexpr (op >= PX) wsof(IC<3>{});
      else if constexpr (op >= PP) A[0][j] = aperm(WC[0], 0, j);
      else if constexpr (op >= PR) ord(IC<0>{}, IC<j>{});
      else if constexpr (op >= PW) wold(IC<j / 8>{}, IC<j % 8>{});
      else if constexpr (op >= PO) oe(IC<j / 64>{}, IC<j % 64>{});
      else if constexpr (op >= PVR) vrd(IC<1>{}, IC<j>{});
      else if constexpr (op >= PS) sm(IC<5>{}, IC<j>{});
      if constexpr (op > 0) SCHED;
    });
  };
  sfor<PV_N>([&](auto UI) {
    constexpr int u = UI, t = u / 27, m = u % 27, t3 = pv_t3(t, m), f = pv_f(t, m);
    if constexpr (m == 0) BSTAMP(7 + t);
    if constexpr (f > 0) mf_v_av<A_V + 4 * (t3 * 8 + f - 1), pv_z(t, m)>(oacc[t & 1][f - 1], Pf[t][t3]);
    else mf_v_vv<pv_z(t, m)>(lacc[t & 1], ones, Pf[t][t3]);
    SCHED;
    pop(UI);
  });
  BSTAMP(13);
  STAMP(4);

  auto tord = [&](auto PI) {
    constexpr int p = PI;
    ordx(IC<C_NS + C_P.bs[p]>{}, IC<C_P.bt[p]>{}, IC<C_B + 24 * ((C_NS + p / 6) & 1) + 4 * (p % 6)>{});
  };
  u32x4 CA[CT][8];
  auto cperm = [&](auto SI, auto KI) {
    constexpr int S = SI, k = KI;
    CA[S - C_NS][k >> 2][k & 3] = aperm(WC[S], k >> 2, k & 3);
  };
  sfor<8 * C_NS>([&](auto NI) {
    constexpr int n = NI, s = n >> 3, f = n & 7;
    constexpr bool cv = s >= C_S0;
    if constexpr (n == 8) BSTAMP(14);
    if constexpr (n == 16) BSTAMP(15);
    if constexpr (n == 24) {
      barrier();
      SCHED;
    }
    sfor<6>([&](auto TI) {
      constexpr int t = TI;
      mf_a_va<4 * (8 * t + f), C_B + 24 * (s & 1) + 4 * t, s == 0>(A[n & 1]);
      SCHED;
      constexpr int v = 2 * f + (t >> 2), li = c_cum(s) + v, ln = c_cum(s) + 2 * f + 2;
      constexpr bool ld = (t == 3 || t == 5) && v < c_n(s);
      if constexpr (ld) wold(IC<li / 8>{}, IC<li % 8>{});
      if constexpr (t == 0) {
        if constexpr (c_rdt(s, f) >= 0 && f < 7) {
          if constexpr (s + 1 < C_NS) ord(IC<s + 1>{}, IC<c_rdt(s, f)>{});
          else tord(IC<f>{});
        }
        if constexpr (f < 7 && ln % 8 == 0 && ln <= c_cum(s + 1)) wsof(IC<ln / 8>{});
        if constexpr (f == 1 && cv) wait_vm<2>();
        if constexpr (f == 7) {
          if constexpr (s + 1 < C_NS) wait_vm<c_cum(s + 1) - 8 * (s + 2)>();
          wait_lgkm0();
        }
      }
      if constexpr (n + 1 < 8 * C_NS && (t == 1 || t == 2)) {
        A[(n + 1) & 1][2 * t - 2] = aperm(WC[(n + 1) >> 3], (n + 1) & 7, 2 * t - 2);
        A[(n + 1) & 1][2 * t - 1] = aperm(WC[(n + 1) >> 3], (n + 1) & 7, 2 * t - 1);
      }
      if constexpr (s < PV_X) pop(IC<PV_N + 6 * n + t>{});
      constexpr int cg = f == 0 ? -1 : f < 4 ? (t == 4 ? f - 1 : -1) : t >= 3 ? 3 * (f - 3) + t - 3 : (f == 6 && t == 0 ? 15 : -1);
      if constexpr (cv && cg >= 0) {
        cperm(IC<(cv ? s + CT - 2 : C_NS)>{}, IC<2 * (cg < 0 ? 0 : cg)>{});
        cperm(IC<(cv ? s + CT - 2 : C_NS)>{}, IC<2 * (cg < 0 ? 0 : cg) + 1>{});
      }
      SCHED;
    });
  });
  const rsrc_t rp = __builtin_amdgcn_make_buffer_rsrc((void*)(part + (size_t)(((br * NBATCH + b) * 8 + hg) * 4 + w) * 24 * 64), 0,
                                                      24 * 1024, RSRC_W3);
  u32x4 sd[2];
  float tb[8];
  auto stp = [&](auto MI) {
    constexpr int m = MI, t = C_P.ct[m / 52], r = (m % 52) / 13, e = m % 13;
    constexpr int rd[13] = {0, 1, 2, 3, -1, 4, 5, -1, 6, 7, -1, -1, -1}, pk[13] = {-1, -1, -1, -1, 0, -1, -1, 1, -1, -1, 2, 3, -1};
    if constexpr (rd[e] >= 0) asm volatile("v_accvgpr_read_b32 %0, a[%c1]" : "=v"(tb[rd[e]]) : "i"(4 * (8 * t + rd[e]) + r));
    else if constexpr (pk[e] >= 0) sd[r & 1][pk[e]] = pk_bf16(tb[2 * pk[e]], tb[2 * pk[e] + 1]);
    else __builtin_amdgcn_raw_buffer_store_b128(sd[r & 1], rp, vo + r * 1024, t * 4096, AUX_PST);
    if constexpr (e == 2 && m >= 13) asm volatile("" ::"v"(sd[(r & 1) ^ 1]));
    SCHED;
  };
  sfor<48 * CT>([&](auto UI) {
    constexpr int u = UI, p = u >> 3, f = u & 7;
    mf_a_va<4 * (8 * C_P.bt[p] + f), C_B + 24 * ((C_NS + p / 6) & 1) + 4 * (p % 6), false>(CA[C_P.bs[p]][f]);
    SCHED;
    sfor<3>([&](auto KI) {
      constexpr int op = C_P.op[u][KI], j = op % 1000;
      if constexpr (op >= TWL) wait_lgkm0();
      else if constexpr (op >= TWV) wait_vm<j>();
      else if constexpr (op >= TS) stp(IC<j>{});
      else if constexpr (op >= TB) tord(IC<j>{});
      else if constexpr (op >= TP) cperm(IC<14 + j / 32>{}, IC<j % 32>{});
      else if constexpr (op >= TL) wold(IC<15>{}, IC<j>{});
      SCHED;
    });
  });
  STAMP(5);
  nop<7>();
  sfor<6 * 52 - C_P.nst>([&](auto KI) { stp(IC<C_P.nst + KI>{}); });
  STAMP(6);
}

__global__ __launch_bounds__(256) void k2_reduce(const u32x4* __restrict__ part, unsigned short* __restrict__ out) {
  const int bb = blockIdx.x / 24, j = blockIdx.x % 24;
  const int tid = threadIdx.x;
  int vi = (bb * 8 * 6144 + j * 256 + tid) * 16;
  asm volatile("" : "+v"(vi));
  const rsrc_t rp = __builtin_amdgcn_make_buffer_rsrc((void*)part, 0, 2 * NBATCH * 8 * 6144 * 16, RSRC_W3);
  u32x4 v[8];
#pragma unroll
  for (int h = 0; h < 8; ++h) v[h] = __builtin_amdgcn_raw_buffer_load_b128(rp, vi, h * 6144 * 16, AUX_PLD);
  SCHED;
  const int t = j % 6, w = j / 6, br = bb >> 4, b = bb & 15;
  unsigned short* po = out + ((size_t)b * SQ + 16 * t) * 1024 + br * 512 + 128 * w;
  int vo = (tid & 15) * 2048 + (tid & 48) * 4 + (tid >> 6) * 16;
  unsigned hi = 0xffff0000u;
  asm volatile("" : "+s"(po), "+v"(vo), "+s"(hi));
  const rsrc_t ro = __builtin_amdgcn_make_buffer_rsrc((void*)po, 0, 15 * 2048 + 256, RSRC_W3);
  f32x2 s[4];
#pragma unroll
  for (int h = 0; h < 8; ++h) {
#pragma unroll
    for (int i = 0; i < 4; ++i) {
      const f32x2 x = f32x2{__builtin_bit_cast(float, v[h][i] << 16), __builtin_bit_cast(float, v[h][i] & hi)};
      s[i] = h ? s[i] + x : x;
    }
    asm volatile("" : "+v"(s[0]), "+v"(s[1]), "+v"(s[2]), "+v"(s[3]));
  }
  u32x4 o;
#pragma unroll
  for (int i = 0; i < 4; ++i) o[i] = pk_bf16(s[i][0], s[i][1]);
  __builtin_amdgcn_raw_buffer_store_b128(o, ro, vo, 0, 0);
  asm volatile(".Lk2_end:");
}

extern "C" void kernel_launch(void* const* d_in, const int* in_sizes, int n_in, void* d_out, int out_size, void* d_ws,
                              size_t ws_size, hipStream_t stream) {
  Ptrs p;
  for (int br = 0; br < 2; ++br) {
    p.x[br] = (const unsigned short*)d_in[5 * br + 0];
    p.k[br] = (const unsigned short*)d_in[5 * br + 1];
    p.v[br] = (const unsigned short*)d_in[5 * br + 2];
    p.wq[br] = (const unsigned short*)d_in[5 * br + 3];
    p.wo[br] = (const unsigned short*)d_in[5 * br + 4];
  }
  u32x4* part = (u32x4*)d_ws;
  (void)hipFuncSetAttribute((const void*)k1_fused, hipFuncAttributeMaxDynamicSharedMemorySize, LDS_BYTES);
  k1_fused<<<256, 256, LDS_BYTES, stream>>>(p, part
  );
  k2_reduce<<<2 * 16 * 96 * 64 / 256, 256, 0, stream>>>(part, (unsigned short*)d_out);
}
```

```cpp
#include <hip/hip_runtime.h>
#include <hip/hip_bf16.h>
#include <utility>

typedef unsigned u32x2 __attribute__((ext_vector_type(2)));
typedef unsigned u32x4 __attribute__((ext_vector_type(4)));
typedef float f32x4 __attribute__((ext_vector_type(4)));
#define LDSP __attribute__((address_space(3)))
#define DI __device__ __forceinline__
typedef __amdgpu_buffer_rsrc_t rsrc_t;

constexpr int SQ = 96, DM = 512, NH = 32, DH = 128, NBATCH = 16;
constexpr int RSRC_W3 = 0x00020000;
#define POL_KV " nt"
#define POL_W ""
#define POL_PF ""
#define POL_X ""
#define AUX_PST 16
#define AUX_PLD 0

template <class F, int... I>
DI void sfor_impl(F&& f, std::integer_sequence<int, I...>) {
  (f(std::integral_constant<int, I>{}), ...);
}
template <int N, class F>
DI void sfor(F&& f) {
  sfor_impl(f, std::make_integer_sequence<int, N>{});
}
template <int N>
using IC = std::integral_constant<int, N>;

typedef float f32x2 __attribute__((ext_vector_type(2)));
typedef __bf16 bf16x2 __attribute__((ext_vector_type(2)));
DI unsigned pk_bf16(float lo, float hi) {
  return __builtin_bit_cast(unsigned, __builtin_convertvector(f32x2{lo, hi}, bf16x2));
}
DI unsigned perm_lo(unsigned e, unsigned o) { return __builtin_amdgcn_perm(o, e, 0x05040100u); }
DI unsigned perm_hi(unsigned e, unsigned o) { return __builtin_amdgcn_perm(o, e, 0x07060302u); }
#define SCHED __builtin_amdgcn_sched_barrier(0)

#define MF "v_mfma_f32_16x16x32_bf16 "
template <int D, int Bq, bool Z>
DI void mf_a_va(const u32x4& A) {
  if constexpr (Z) asm volatile(MF "a[%c0:%c1], %2, a[%c3:%c4], 0" ::"i"(D), "i"(D + 3), "v"(A), "i"(Bq), "i"(Bq + 3));
  else asm volatile(MF "a[%c0:%c1], %2, a[%c3:%c4], a[%c0:%c1]" ::"i"(D), "i"(D + 3), "v"(A), "i"(Bq), "i"(Bq + 3));
}
template <int Bq, bool Z>
DI void mf_v_va(f32x4& C, const u32x4& A) {
  if constexpr (Z) asm volatile(MF "%0, %1, a[%c2:%c3], 0" : "=&v"(C) : "v"(A), "i"(Bq), "i"(Bq + 3));
  else asm volatile(MF "%0, %1, a[%c2:%c3], %0" : "+v"(C) : "v"(A), "i"(Bq), "i"(Bq + 3));
}
template <int Aq, bool Z>
DI void mf_v_av(f32x4& C, const u32x4& Bv) {
  if constexpr (Z) asm volatile(MF "%0, a[%c2:%c3], %1, 0" : "=&v"(C) : "v"(Bv), "i"(Aq), "i"(Aq + 3));
  else asm volatile(MF "%0, a[%c2:%c3], %1, %0" : "+v"(C) : "v"(Bv), "i"(Aq), "i"(Aq + 3));
}
template <bool Z>
DI void mf_v_vv(f32x4& C, const u32x4& A, const u32x4& Bv) {
  if constexpr (Z) asm volatile(MF "%0, %1, %2, 0" : "=&v"(C) : "v"(A), "v"(Bv));
  else asm volatile(MF "%0, %1, %2, %0" : "+v"(C) : "v"(A), "v"(Bv));
}
template <int X, int Y>
DI unsigned pk_acc() {
  unsigned r, t0, t1;
  asm volatile("v_accvgpr_read_b32 %1, a[%c3]\n\tv_accvgpr_read_b32 %2, a[%c4]\n\tv_cvt_pk_bf16_f32 %0, %1, %2"
               : "=v"(r), "=&v"(t0), "=&v"(t1)
               : "i"(X), "i"(Y));
  return r;
}
template <int IMM>
DI void ld_w(u32x4& d, rsrc_t r, int voff, int soff) {
  asm volatile("buffer_load_dwordx4 %0, %1, %2, %3 offen offset:%c4" POL_W : "=v"(d) : "v"(voff), "s"(r), "s"(soff), "i"(IMM));
}
template <int D, int IMM>
DI void ld_k(rsrc_t r, int voff, int soff) {
  asm volatile("buffer_load_dwordx4 a[%c0:%c1], %2, %3, %4 offen offset:%c5" POL_KV ::"i"(D), "i"(D + 3), "v"(voff), "s"(r),
               "s"(soff), "i"(IMM));
}
template <int D>
DI void ld_pf(rsrc_t r, int voff, int soff) {
  asm volatile("buffer_load_dwordx4 a[%c0:%c1], %2, %3, %4 offen" POL_PF ::"i"(D), "i"(D + 3), "v"(voff), "s"(r), "s"(soff));
}
DI void ld_pfv(u32x4& d, rsrc_t r, int voff, int soff) {
  asm volatile("buffer_load_dwordx4 %0, %1, %2, %3 offen" POL_PF : "+v"(d) : "v"(voff), "s"(r), "s"(soff));
}
template <int C>
DI void set_m0(int base) {
  asm volatile("s_add_i32 m0, %0, %c1" ::"s"(base), "i"(C) : "scc", "memory");
}
template <int IMM>
DI void dma_x(rsrc_t r, int voff, int soff) {
  asm volatile("buffer_load_dwordx4 %0, %1, %2 offen offset:%c3" POL_X " lds" ::"v"(voff), "s"(r), "s"(soff), "i"(IMM) : "memory");
}
template <int IMM>
DI void dma_v(rsrc_t r, int voff) {
  asm volatile("buffer_load_dwordx4 %0, %1, m0 offen offset:%c2" POL_KV " lds" ::"v"(voff), "s"(r), "i"(IMM) : "memory");
}
template <int D, int IMM>
DI void lds_rd_a(int addr) {
  asm volatile("ds_read_b128 a[%c0:%c1], %2 offset:%c3" ::"i"(D), "i"(D + 3), "v"(addr), "i"(IMM) : "memory");
}
template <int D, int IMM>
DI void lds_rdtr_a(int addr) {
  asm volatile("ds_read_b64_tr_b16 a[%c0:%c1], %2 offset:%c3" ::"i"(D), "i"(D + 1), "v"(addr), "i"(IMM) : "memory");
}
template <int D, int IMM>
DI void lds_wr_a(int addr) {
  asm volatile("ds_write_b128 %2, a[%c0:%c1] offset:%c3" ::"i"(D), "i"(D + 3), "v"(addr), "i"(IMM) : "memory");
}
template <int IMM>
DI void lds_wr(int addr, const u32x4& d) {
  asm volatile("ds_write_b128 %0, %1 offset:%c2" ::"v"(addr), "v"(d), "i"(IMM) : "memory");
}
template <int N>
DI void wait_vm() {
  static_assert(N >= 0 && N < 64, "vmcnt");
  asm volatile("s_waitcnt vmcnt(%c0)" ::"i"(N) : "memory");
}
DI void wait_lgkm0() { asm volatile("s_waitcnt lgkmcnt(0)" ::: "memory"); }
template <int N>
DI void wait_lgkm() {
  static_assert(N >= 0 && N < 16, "lgkmcnt");
  asm volatile("s_waitcnt lgkmcnt(%c0)" ::"i"(N) : "memory");
}
DI void barrier() { asm volatile("s_barrier" ::: "memory"); }
template <int N>
DI void nop() {
  asm volatile("s_nop %c0" ::"i"(N));
}
DI float max3(float a, float b, float c) { return __builtin_elementwise_maximum(__builtin_elementwise_maximum(a, b), c); }
DI void xmax16(float& a) {
  float t;
  asm volatile("v_mov_b32 %1, %0\n\ts_nop 1\n\tv_permlane16_swap_b32 %0, %1\n\tv_max_f32 %0, %0, %1" : "+v"(a), "=&v"(t));
}
DI void xmax32s(float& a, float s) {
  float t;
  asm volatile("v_mov_b32 %1, %0\n\ts_nop 1\n\tv_permlane32_swap_b32 %0, %1\n\tv_max_f32 %0, %0, %1\n\tv_mul_f32 %0, %2, %0"
               : "+v"(a), "=&v"(t)
               : "s"(s));
}
template <int X, int Y>
DI void acc_rd2(float& a, float& b) {
  asm volatile("v_accvgpr_read_b32 %0, a[%c2]\n\tv_accvgpr_read_b32 %1, a[%c3]" : "=v"(a), "=v"(b) : "i"(X), "i"(Y));
}

#define STAMP(i)

#define BSTAMP(i)
#define HALFX(n) (n)
struct Ptrs {
  const unsigned short* x[2];
  const unsigned short* k[2];
  const unsigned short* v[2];
  const unsigned short* wq[2];
  const unsigned short* wo[2];
};

constexpr int LDS_BYTES = 163840;
constexpr int XQ_BYTES = 24576, V1_BASE = 98304;

constexpr int A_B = 112, A_K = 160, A_V = 0;
constexpr bool tile_v(int f, int t) { return t < 2 || (t == 2 && f < 4); }
constexpr int tile_vi(int f, int t) { return t * 8 + f; }
constexpr int tile_a(int f, int t) { return 4 * (t >= 3 ? (t - 3) * 8 + f : 24 + (f - 4)); }

#define PF_WO 8
enum { OX = 1000, OK_ = 2000, OP = 3000, OV = 4000, OW = 5000 };
struct ASched {
  int op[16][24];
  bool bar[16];
};
#define SCHV 5
#define PFPV 6
#define STAG 16
#define X6(q) OX + 6 * q, OX + 6 * q + 1, OX + 6 * q + 2, OX + 6 * q + 3, OX + 6 * q + 4, OX + 6 * q + 5
#define K4(t) OK_ + 4 * t, OK_ + 4 * t + 1, OK_ + 4 * t + 2, OK_ + 4 * t + 3
#define V4(j) OV + j, OV + j + 1, OV + j + 2, OV + j + 3
#define PRO_OP 2
constexpr int A_BURST[6][12] = {{X6(1), OP + 4}, {X6(2), OP + 5}, {OP + 6, K4(0), OK_ + 4, OK_ + 5},
                                {X6(3), OP + 7}, {OK_ + 6, OK_ + 7, K4(2), OK_ + 12}, {OK_ + 13, OK_ + 14, OK_ + 15, K4(4)}};
constexpr int A_TAIL[3][8] = {{K4(5), OV + 0, OV + 1, OV + 2}, {OV + 3, V4(4), OV + 8}, {OV + 9, OV + 10, OV + 16, OV + 17, OV + 18}};
constexpr int A_KST[6] = {8, 11, 12, 13, 14, 15};
constexpr int B_V[] = {19, 20, 21, 22, 23, 11, 12, 13, 14, 15};
constexpr int B_NV = 10, B_PFT = 4;
constexpr ASched a_make() {
  ASched a{};
  for (int s = 0; s <= 12; s += 2)
    for (int i = 0; i < 16; ++i) a.op[s][i] = OW + 8 * (s + 2) + i;
  for (int s = 1; s <= 11; s += 2) {
    a.bar[s] = true;
    for (int i = 0; i < (int)(sizeof(A_BURST[0]) / sizeof(int)); ++i) a.op[s][i] = A_BURST[s >> 1][i];
  }
  for (int s = 13; s < 16; ++s) {
    int n = 0;
    while (n < 8 && A_TAIL[s - 13][n]) ++n;
    for (int i = 0; i < n; ++i) a.op[s][3 * (i * 8 / n)] = A_TAIL[s - 13][i];
  }
  return a;
}
constexpr ASched A_S = a_make();
constexpr int A_NP = 3 + 6 + PRO_OP + 16;
constexpr int a_seq(int op) {
  if (op >= OW && op < OW + 16) return 9 + PRO_OP + op - OW;
  int c = A_NP;
  for (int s = 0; s < 16; ++s)
    for (int k = 0; k < 24; ++k)
      if (A_S.op[s][k]) {
        if (A_S.op[s][k] == op) return c;
        ++c;
      }
  return -1;
}
constexpr int a_issued(int s, int k) {
  int c = A_NP;
  for (int i = 0; i < 16; ++i)
    for (int j = 0; j < 24; ++j)
      if (A_S.op[i][j] && (i < s || (i == s && j < k))) ++c;
  return c;
}
constexpr int a_kt(int s) {
  for (int k = 0; k < 6; ++k)
    if (A_KST[k] == s) return k;
  return -1;
}
constexpr int imax(int a, int b) { return a > b ? a : b; }
constexpr int a_need(int s) {
  int n = 0;
  for (int i = 0; i < 8; ++i) n = imax(n, a_seq(OW + 8 * (s + 1) + i));
  if (s == 2 || s == 6 || s == 10)
    for (int i = 0; i < 6; ++i) n = imax(n, a_seq(OX + 6 * ((s + 2) / 4) + i));
  if (a_kt(s + 1) >= 0)
    for (int i = 0; i < 4; ++i) n = imax(n, a_seq(OK_ + 4 * a_kt(s + 1) + i));
  if (s == 14)
    for (int i = 2; i < 8; ++i) n = imax(n, a_seq(OP + i));
  return n;
}
constexpr int a_wait(int s) { return a_issued(s, s < 13 ? 14 : 21) - a_need(s) - 1; }
constexpr int v_jb(int j) {
  int jb = -99;
  for (int i = 0; i < 16 * 24 + B_NV; ++i) {
    const int op = i < 16 * 24 ? A_S.op[i / 24][i % 24] : OV + B_V[i - 16 * 24];
    if (op >= OX && op < OK_) jb = -99;
    if (op < OV || op >= OW) continue;
    const int v = op - OV;
    if (!(v >= jb && v - jb < 4 && (v < 16) == (jb < 16))) jb = v;
    if (v == j) return jb;
  }
  return -1;
}
constexpr int a_wait_w(int ws, int s, int k) {
  int n = 0;
  for (int i = 0; i < 8; ++i) n = imax(n, a_seq(OW + 8 * ws + i));
  return a_issued(s, k) - n - 1;
}
constexpr bool a_check() {
  for (int i = 0; i < 24; ++i)
    if (a_seq(OK_ + i) < 0) return false;
  for (int i = 6; i < 24; ++i)
    if (a_seq(OX + i) < 0) return false;
  for (int i = 0; i < 128; ++i)
    if (a_seq(OW + i) < 0 || (i >= 32 && a_seq(OW + i) < a_issued(i / 8 - 3, 0))) return false;
  int nv = B_NV;
  for (int i = 0; i < 24; ++i)
    if (a_seq(OV + i) >= 0) {
      ++nv;
      if (i >= 11 && i < 16 && a_seq(OV + i) < a_issued(A_KST[5] + 1, 0)) return false;
      if (i >= 16 && a_seq(OV + i) < a_issued(7, 0)) return false;
    }
  for (int s = 0; s < 15; ++s)
    if (a_wait(s) < 0 || a_wait(s) > 63) return false;
  return nv == 24 && A_S.bar[3] && A_S.bar[7] && A_S.bar[11];
}
static_assert(a_check(), "phase A schedule");

#define CT 6
constexpr int C_NS = 16 - CT, C_S0 = C_NS - CT + 2, C_B = 192;
constexpr int c_n(int s) { return s < C_S0 && 4 * s < 8 * (CT - 4) ? 12 : 8; }
constexpr int c_cum(int s) {
  int c = 24;
  for (int i = 0; i < s; ++i) c += c_n(i);
  return c;
}
static_assert(CT >= 4 && CT <= 6 && c_cum(C_NS) == 120, "phase C schedule");
enum { TL = 1000, TP = 2000, TB = 3000, TS = 4000, TWV = 5000, TWL = 6000 };
struct CPlan {
  int bt[6 * CT], bs[6 * CT];
  int ct[6];
  int op[48 * CT][3];
  int nst;
};
constexpr CPlan c_make() {
  CPlan c{};
  int nx[6] = {0, 0, 0, 0, 0, 0}, done[6] = {0, 0, 0, 0, 0, 0}, nc = 0;
  for (int p = 0; p < 6 * CT; ++p)
    for (int t = 0; t < 6; ++t)
      if (nx[t] < CT && (nx[t] < CT - 2 || (nx[t] == CT - 2 && p >= 3) || p >= 6)) {
        c.bt[p] = t;
        c.bs[p] = nx[t]++;
        if (nx[t] == CT) {
          c.ct[nc] = t;
          done[nc++] = 8 * p + 7;
        }
        break;
      }
  int used[48 * CT] = {};
  auto put = [&](int u, int op, int w) {
    int k = 0;
    while (c.op[u][k]) ++k;
    c.op[u][k] = op;
    used[u] += w;
  };
  for (int i = 0; i < 8; ++i) put(i, TL + i, 2);
  put(8, TWV + 8, 0);
  put(32, TWV + 0, 0);
  for (int k = 0; k < 32; ++k) {
    put(8 + k / 2, TP + k, 1);
    put(32 + k / 2, TP + 32 + k, 1);
  }
  for (int r = 0; r + 1 < CT; ++r) {
    for (int j = 0; j < 6; ++j) put(r ? 48 * r + 6 * j : 24 + j, TB + 6 * (r + 1) + j, 1);
    put(48 * r + 46, TWL, 0);
  }
  int u = 0, m = 0;
  for (; m < 6 * 52; ++m) {
    const int w = m < 260 ? (m % 13 == 12 ? 2 : 1) : (m >= 308 ? 2 : 1);
    const int rdy = m < 260 ? done[m / 52] + 2 : 48 * CT - 48 + 12 * (m < 308 ? (m - 260) / 12 : 3) + 13;
    if (u < rdy) u = rdy;
    while (u < 48 * CT && used[u] + w > 2) ++u;
    if (u >= 48 * CT) break;
    put(u, TS + m, w);
  }
  c.nst = m;
  return c;
}
constexpr CPlan C_P = c_make();
constexpr bool c_check() {
  for (int p = 6 * CT - 6; p < 6 * CT; ++p)
    if (C_P.bt[p] != C_P.ct[5]) return false;
  return CT == 6;
}
static_assert(c_check(), "phase C tail");

constexpr int pv_t3(int t, int m) { return t == 0 ? (m < 9 ? 0 : m < 18 ? 2 : 1) : (m < 9 ? m / 3 : ((m - 9) % 6) / 2); }
constexpr int pv_f(int t, int m) { return t == 0 ? m % 9 : (m < 9 ? m % 3 : 3 + 2 * ((m - 9) / 6) + (m - 9) % 2); }
constexpr bool pv_z(int t, int m) { return t == 0 ? m < 9 : pv_t3(t, m) == 0; }
constexpr int pv_first(int t, int f) {
  for (int m = 0; m < 27; ++m)
    if (pv_f(t, m) == f) return 27 * t + m;
  return -1;
}
constexpr int pv_last(int t, int f) {
  for (int m = 26; m >= 0; --m)
    if (pv_f(t, m) == f) return 27 * t + m;
  return -1;
}
#define PV_R 177
constexpr int SM_N = 43, OE_N = 53, PV_N = 162;
constexpr int E_RD[13] = {0, 1, 2, 3, -1, 4, 5, -1, 6, 7, -1, -1, -1}, E_PK[13] = {-1, -1, -1, -1, 0, -1, -1, 1, -1, -1, 2, 3, -1};
enum { PS = 1000, PVR = 2000, PO = 3000, PW = 4000, PR = 5000, PP = 6000, PX = 7000, PB = 7500, PWV = 8000, PWL = 9000 };
constexpr int PV_X = 3;
constexpr int c_rdt(int s, int f) { return s < PV_X ? (f < 4 ? f + 2 : f - 5) : (f < 6 ? f : -1); }
struct PPlan {
  int op[PV_N + 48 * PV_X][12];
  bool ok;
};
constexpr PPlan p_make() {
  PPlan c{};
  int it[512] = {}, cost[512] = {}, rdy[512] = {}, dl[512] = {}, n = 0;
  auto add = [&](int op, int w, int r, int d) {
    it[n] = op;
    cost[n] = w;
    rdy[n] = r;
    dl[n++] = d;
  };
  auto piece = [&](int t, int sp, int nt) {
    for (int e = 0; e < 13; ++e) {
      const int f = 1 + 2 * sp + (E_RD[e] >> 2);
      if (e == 12 && nt >= 0) add(PO + 64 * nt, 2, pv_last(nt, 0) + 3, nt < 4 ? pv_first(nt + 2, 0) : 999);
      add(PO + 64 * t + 1 + 13 * sp + e, 1, E_RD[e] >= 0 ? pv_last(t, f) + 3 : 0,
          t < 4 ? (E_RD[e] >= 0 ? pv_first(t + 2, f) : PV_N) : (sp ? PV_N + 48 * (sp - 1) + 6 * (t + 1) : PV_N));
    }
  };
  for (int j = 0; j < SM_N; ++j) {
    if (j == SM_N - 1) add(PO, 2, pv_last(0, 0) + 3, pv_first(2, 0));
    add(PS + j, j < 5 ? 2 : j < 7 ? 6 : j < 31 ? 3 : 1, 0, 27 * 5 - 1);
  }
  for (int t = 0; t < 4; ++t)
    for (int sp = 0; sp < 4; ++sp) piece(t, sp, sp == 3 ? t + 1 : -1);
  piece(4, 0, -1);
  piece(4, 1, 5);
  piece(5, 0, -1);
  for (int k = 0; k < 6; ++k) add(PR + (k + 2) % 6, 1, 0, PV_N - 3);
  add(PWV + 99, 0, 0, PV_N - 1);
  for (int k = 0; k < 4; ++k) add(PP + k, 1, 0, PV_N - 1);
  add(PX, 1, 0, PV_N);
  piece(5, 1, -1);
  for (int sp = 2; sp < 4; ++sp) {
    piece(4, sp, -1);
    piece(5, sp, -1);
  }
  for (int k = 2; k < 8; ++k) add(PB + k, 1, 0, PV_N + 48 * PV_X);
  int placed = 0, idx = 0, nw = 0, nds = 0;
  c.ok = true;
  for (int g = 0; g < PV_N + 48 * PV_X; ++g) {
    int k = 0, used = 0;
    auto put = [&](int op, int w) {
      c.op[g][k++] = op;
      placed += w;
      used += w;
    };
    if (g == 1) {
      put(PWL + 0, 0);
      put(PWV + PF_WO, 0);
    }
    for (int i = 0; i < 16; ++i)
      if (g == 2 + i * 12 / 16) put(PVR + i, 1);
    if (g == 17) put(PWL + 0, 0);
    for (int i = 0; i < 24; ++i)
      if (g == 81 + i * 80 / 24) {
        put(PW + i, i % 8 ? 2 : 3);
        ++nw;
      }
    const int cs = (g - PV_N) / 48, cf = (g - PV_N) % 48 / 6, ctt = (g - PV_N) % 6;
    const int cap = g < PV_N ? 99 : (ctt == 4 || ((ctt == 3 || ctt == 5) && 2 * cf + (ctt >> 2) >= c_n(cs)) ? 2 : 0);
    while (idx < n && rdy[idx] <= g && used + cost[idx] <= cap && (g >= PV_N || placed * 64 < PV_R * (g + 1))) {
      const int op = it[idx];
      if (g >= dl[idx]) c.ok = false;
      if (op >= PR && op < PP) nds = 0;
      else if (op >= PO && op < PW && (op - PO) % 64 > 0 && ((op - PO) % 64 - 1) % 13 == 12) ++nds;
      if (op == PWV + 99 && nw < 8) c.ok = false;
      put(op == PWV + 99 ? PWV + nw - 8 : op, cost[idx]);
      ++idx;
    }
    if (g == PV_N - 1) put(PWL + nds, 0);
    if (k > 11) c.ok = false;
  }
  c.ok = c.ok && idx == n && nw == 24;
  return c;
}
constexpr PPlan P_P = p_make();
static_assert(P_P.ok, "PV schedule");

DI unsigned aperm(const u32x4 (&W)[8], int f, int p) {
  return (f & 1) ? perm_hi(W[2 * p][f >> 1], W[2 * p + 1][f >> 1]) : perm_lo(W[2 * p][f >> 1], W[2 * p + 1][f >> 1]);
}

__global__ __launch_bounds__(256, 1) void k1_fused(Ptrs p, u32x4* part
) {
  extern __shared__ __attribute__((aligned(16))) char lds[];
  asm volatile("" ::: "a0", "a255");
  const int bid = blockIdx.x;
  const unsigned short *px, *pk, *pv, *pq, *po;
  asm volatile(
      "s_load_dwordx2 %0, %6, %7 offset:0x30\n\ts_load_dwordx2 %1, %6, %7 offset:0x0\n\ts_load_dwordx2 %2, %6, %7 offset:0x10\n\t"
      "s_load_dwordx2 %3, %6, %7 offset:0x20\n\ts_load_dwordx2 %4, %6, %7 offset:0x40\n\ts_load_dwordx2 %5, %6, 0x50"
      : "=&s"(pq), "=&s"(px), "=&s"(pk), "=&s"(pv), "=&s"(po), "=&s"(part)
      : "s"(__builtin_amdgcn_kernarg_segment_ptr()), "s"((bid & 4) * 2));
  SCHED;
  const int xcd = bid & 7, idx = bid >> 3;
  const int br = xcd >> 2, hg = (xcd & 3) * 2 + (idx & 1), b = idx >> 1;
  const int tid = threadIdx.x, lane = tid & 63;
  const int w = __builtin_amdgcn_readfirstlane(tid >> 6);
  const int q = lane & 15, g = lane >> 4;
  const int cchunk = q;
  const int lb = (int)(unsigned)(size_t)(LDSP char*)lds;
  STAMP(0);

  const int vk = lane * 16;
  u32x4 pfd = u32x4{0, 0, 0, 0};
  {
    unsigned long long pc;
    int len;
    asm volatile("s_getpc_b64 %0\n.Lk1_pc:\n\ts_mov_b32 %1, .Lk2_end-.Lk1_pc" : "=s"(pc), "=s"(len));
    const rsrc_t rc = __builtin_amdgcn_make_buffer_rsrc((void*)pc, 0, len, RSRC_W3);
    nop<4>();
    ld_pfv(pfd, rc, vk, (idx * 4 + w) * 1024);
  }
  const int head = hg * 4 + w;
  const size_t kvoff = ((size_t)b * NH + head) * SQ * DH;

  f32x4 accv[20];
  u32x4 W[4][8];
  u32x4 A[2];

  auto gmf = [&](auto FI, auto TI, auto BI, auto ZI, const u32x4& Af) {
    constexpr int f = FI, t = TI, bq = A_B + 24 * BI + 4 * t;
    constexpr bool z = ZI;
    if constexpr (tile_v(f, t)) mf_v_va<bq, z>(accv[tile_vi(f, t)], Af);
    else mf_a_va<tile_a(f, t), bq, z>(Af);
  };
  auto gpk = [&](auto PI, auto TI, auto RI) -> unsigned {
    constexpr int pp = PI, t = TI, r = RI;
    if constexpr (tile_v(2 * pp, t)) return pk_bf16(accv[tile_vi(2 * pp, t)][r], accv[tile_vi(2 * pp + 1, t)][r]);
    else return pk_acc<tile_a(2 * pp, t) + r, tile_a(2 * pp + 1, t) + r>();
  };

  int vx = g * 1024 + ((q ^ g ^ (4 * w)) << 4);
  int vwq = (8 * g) * 8192 + (hg * 512 + w * 128 + 8 * cchunk) * 2;
  int sxb = lb + w * 1024, sxo[6];
#pragma unroll
  for (int i = 0; i < 6; ++i) sxo[i] = (4 * i + w) * 4096 + b * (SQ * DM * 2);
  int vpf = ((lane >> 5) * 32 + 2 * b + (w >> 1)) * 8192 + hg * 1024 + (w & 1) * 512 + (lane & 31) * 16;
  asm volatile("" : "+s"(sxb), "+s"(sxo[0]), "+s"(sxo[1]), "+s"(sxo[2]), "+s"(sxo[3]), "+s"(sxo[4]), "+s"(sxo[5]), "+v"(vx), "+v"(vwq),
               "+v"(vpf));
  nop<4>();
  SCHED;
  asm volatile("s_waitcnt lgkmcnt(0)" : "+s"(pq), "+s"(px), "+s"(pk), "+s"(pv), "+s"(po), "+s"(part));
  const rsrc_t rq = __builtin_amdgcn_make_buffer_rsrc((void*)pq, 0, DM * NH * DH * 2, RSRC_W3);
  const rsrc_t rx = __builtin_amdgcn_make_buffer_rsrc((void*)px, 0, NBATCH * SQ * DM * 2, RSRC_W3);
  const rsrc_t rk = __builtin_amdgcn_make_buffer_rsrc((void*)(pk + kvoff), 0, SQ * DH * 2, RSRC_W3);
  const rsrc_t ro = __builtin_amdgcn_make_buffer_rsrc((void*)(po + (size_t)hg * 512 * DM), 0, 512 * DM * 2, RSRC_W3);
  auto xm0 = [&](auto II) { set_m0<(II / 6) * (XQ_BYTES - 256) + (II % 6) * 4096>(sxb); };
  auto xdma = [&](auto II) { dma_x<(II / 6) * 256>(rx, vx, sxo[II % 6]); };
  STAMP(8);
  ld_pfv(pfd, rq, vpf, 0);
  ld_pfv(pfd, rq, vpf, 64 * 8192);
  sfor<6>([&](auto I) {
    xm0(I);
    nop<0>();
    xdma(I);
  });
  sfor<PRO_OP>([&](auto I) { ld_pfv(pfd, rq, vpf, (2 + I) * 64 * 8192); });
  STAMP(9);
  wait_vm<7 + PRO_OP>();
  STAMP(10);
  sfor<16>([&](auto I) { ld_w<0>(W[I >> 3][I & 7], rq, vwq, (32 * (I >> 3) + (I & 7)) * 8192); });
  int xa[2][4];
#pragma unroll
  for (int h = 0; h < 2; ++h)
#pragma unroll
    for (int j = 0; j < 4; ++j) xa[h][j] = lb + h * 2 * XQ_BYTES + q * 256 + (((4 * j + g) ^ q) << 4);
  const int kst = lb + V1_BASE + w * 16384 + 16384 - 16 * 272;
  const int kwa = kst + g * 272 + q * 16, kra = kst + q * 272 + g * 64;
  const int vv0 = g * 256 + ((q ^ (((g >> 1) << 2) | (g & 1))) << 4);
  const int vv1 = vv0 ^ 128;
  int svb[2] = {lb + V1_BASE + w * 16384, lb + w * 8192};
  const rsrc_t rv0 = __builtin_amdgcn_make_buffer_rsrc((void*)((const char*)(pv + kvoff) - svb[0]), 0, HALFX(svb[0] + 16384), RSRC_W3);
  const rsrc_t rv1 = __builtin_amdgcn_make_buffer_rsrc((void*)((const char*)(pv + kvoff) + 16384 - svb[1]), 0, HALFX(svb[1] + 8192), RSRC_W3);
  asm volatile("" : "+s"(svb[0]), "+s"(svb[1]));
  auto vm0 = [&](auto JI) {
    constexpr int j = JI;
    if constexpr (v_jb(j) == j) set_m0<(j & 15) * 1024>(svb[j >> 4]);
  };
  auto vdma = [&](auto JI) {
    constexpr int j = JI;
    dma_v<(j - v_jb(j)) * 1024>(j < 16 ? rv0 : rv1, (j & 1) ? vv1 : vv0);
  };
  int sob[4][2];
#pragma unroll
  for (int h = 1; h < 4; ++h) {
    sob[h][0] = lb + V1_BASE + ((w + h) & 3) * 16384;
    sob[h][1] = lb + ((w + h) & 3) * 8192;
    asm volatile("" : "+s"(sob[h][0]), "+s"(sob[h][1]));
  }
  STAMP(11);
  SCHED;
  if (b & 1) __builtin_amdgcn_s_sleep(STAG);
  wait_vm<A_NP - 9>();
  barrier();
  STAMP(1);

  constexpr int TG[3][2] = {{4, 5}, {2, 3}, {0, 1}};
  u32x4 AT[3][8];
  auto tperm = [&](auto KI) {
    constexpr int k = KI, m = k >> 1, d = 2 * (k & 1);
    AT[m >> 3][m & 7][d] = aperm(W[(13 + (m >> 3)) & 3], m & 7, d);
    AT[m >> 3][m & 7][d + 1] = aperm(W[(13 + (m >> 3)) & 3], m & 7, d + 1);
  };
  auto tmf = [&](auto GI, auto UI) {
    constexpr int G = GI, u = UI, sp = u >> 4, f = (u >> 1) & 7, tt = u & 1, t = TG[G][tt];
    constexpr int bq = A_B + 24 * ((G & 1) ^ 1) + 4 * (2 * sp + tt);
    if constexpr (tile_v(f, t)) mf_v_va<bq, false>(accv[tile_vi(f, t)], AT[sp][f]);
    else mf_a_va<tile_a(f, t), bq, false>(AT[sp][f]);
  };
  u32x4 Qf[6][4];
  float ta[2][2];
  auto qp = [&](auto II, auto JI) {
    constexpr int qi = II, t = TG[qi >> 1][qi & 1], j = JI, pp = (j & 15) >> 2, s = j & 3, i = j > 0 ? j - 1 : 0;
    if constexpr (j < 16) {
      if constexpr (tile_v(2 * pp, t)) Qf[qi][s][pp] = gpk(IC<pp>{}, IC<t>{}, IC<s>{});
      else acc_rd2<tile_a(2 * pp, t) + s, tile_a(2 * pp + 1, t) + s>(ta[j & 1][0], ta[j & 1][1]);
    }
    if constexpr (j > 0 && !tile_v(2 * (i >> 2), t)) Qf[qi][i & 3][i >> 2] = pk_bf16(ta[i & 1][0], ta[i & 1][1]);
  };

  auto xrdx = [&](auto SI, auto TI, auto DI_) {
    constexpr int s = SI, t = TI;
    lds_rd_a<DI_, ((s >> 2) & 1) * XQ_BYTES + t * 4096>(xa[s >> 3][s & 3]);
  };
  auto xrd = [&](auto SI, auto TI) { xrdx(SI, TI, IC<A_B + 24 * (SI & 1) + 4 * TI>{}); };
  auto txrd = [&](auto GI, auto JI) {
    constexpr int G = GI, jj = JI;
    xrdx(IC<13 + jj / 2>{}, IC<TG[G][jj & 1]>{}, IC<A_B + 24 * ((G & 1) ^ 1) + 4 * jj>{});
  };
  int so[2];
  auto aprep = [&](auto OI, auto PI) {
    constexpr int op = OI, j = op % 1000, pi = PI;
    if constexpr (op >= OV && op < OW) vm0(IC<j>{});
    else if constexpr (op >= OK_) {
      so[pi] = op >= OW ? (32 * (j >> 3) + (j & 7)) * 8192 : op >= OP ? j * 64 * 8192 : (j >> 2) * 4096;
      asm volatile("" : "+s"(so[pi]));
    } else if constexpr (op >= OX) xm0(IC<j>{});
  };
  auto aop = [&](auto OI, auto PI) {
    constexpr int op = OI, j = op % 1000, pi = PI;
    if constexpr (op >= OW) ld_w<0>(W[(j >> 3) & 3][j & 7], rq, vwq, so[pi]);
    else if constexpr (op >= OV) vdma(IC<j>{});
    else if constexpr (op >= OP) ld_pfv(pfd, rq, vpf, so[pi]);
    else if constexpr (op >= OK_) ld_k<A_K + 4 * j, (j & 3) * 1024>(rk, vk, so[pi]);
    else if constexpr (op >= OX) xdma(IC<j>{});
  };
  sfor<6>([&](auto T) { xrd(IC<0>{}, T); });
  wait_vm<8>();
  SCHED;
#pragma unroll
  for (int pp = 0; pp < 4; ++pp) A[0][pp] = aperm(W[0], 0, pp);
  SCHED;
  wait_lgkm0();
  nop<0>();
  SCHED;
  sfor<104>([&](auto NI) {
    constexpr int n = NI, s = n >> 3, f = n & 7;
    constexpr int kt = a_kt(s), ktc = kt < 0 ? 0 : kt;
    if constexpr (f == 0 && A_S.bar[s]) {
      barrier();
      SCHED;
    }
    sfor<6>([&](auto TI) {
      constexpr int t = TI;
      gmf(IC<f>{}, TI, IC<(s & 1)>{}, std::bool_constant<s == 0>{}, A[n & 1]);
      SCHED;
      constexpr int opb = A_S.op[s][2 * f + 1];
      constexpr bool dmb = (opb >= OX && opb < OK_) || (opb >= OV && opb < OW);
      if constexpr (t == 0 && f == 7) wait_vm<a_wait(s)>();
      if constexpr (t == 0) aprep(IC<A_S.op[s][2 * f]>{}, IC<0>{});
      if constexpr (t == (dmb ? 4 : 0)) aprep(IC<opb>{}, IC<1>{});
      if constexpr (t == 4 && f < 6) {
        if constexpr (s < 12) xrd(IC<s + 1>{}, IC<f>{});
        else txrd(IC<0>{}, IC<f>{});
      }
      if constexpr (t == 3 || t == 5) aop(IC<A_S.op[s][2 * f + t / 4]>{}, IC<t / 4>{});
      if constexpr (t == 1 || t == 2) {
        if constexpr (n < 103) {
          A[(n + 1) & 1][2 * t - 2] = aperm(W[((n + 1) >> 3) & 3], (n + 1) & 7, 2 * t - 2);
          A[(n + 1) & 1][2 * t - 1] = aperm(W[((n + 1) >> 3) & 3], (n + 1) & 7, 2 * t - 1);
        } else tperm(IC<t - 1>{});
      }
      if constexpr (n == 103 && t == 4) tperm(IC<2>{});
      if constexpr (t == 4 && kt >= 0) {
        if constexpr (f < 4) lds_wr_a<A_K + 16 * ktc + 4 * f, f * 4 * 272>(kwa);
        else lds_rd_a<A_K + 16 * ktc + 4 * (f - 4), (f - 4) * 16>(kra);
      }
      if constexpr (t == 5 && f == 7) wait_lgkm<(kt >= 0 ? 2 : 0)>();
      SCHED;
    });
  });
  sfor<3>([&](auto GI) {
    constexpr int G = GI, s = 13 + G;
    constexpr int kt = a_kt(s), ktc = kt < 0 ? 0 : kt;
    sfor<48>([&](auto UI) {
      constexpr int u = UI;
      tmf(GI, UI);
      SCHED;
      if constexpr (u == 42 && s < 15) wait_vm<a_wait(s)>();
      if constexpr (G == 0 && u == 13) wait_vm<a_wait_w(14, 13, 6)>();
      if constexpr (G == 0 && u == 29) wait_vm<a_wait_w(15, 13, 14)>();
      if constexpr (u & 1) aop(IC<A_S.op[s][u / 2]>{}, IC<0>{});
      else aprep(IC<A_S.op[s][u / 2]>{}, IC<0>{});
      if constexpr (G == 0 && u + 3 < 48) tperm(IC<u + 3>{});
      if constexpr (G == 0 && u == 45) nop<0>();
      if constexpr (G < 2 && u % 6 == 5 && u / 6 < 6) txrd(IC<(G < 2 ? G + 1 : 0)>{}, IC<u / 6>{});
      if constexpr (kt >= 0 && u % 6 == 3) {
        if constexpr (u / 6 < 4) lds_wr_a<A_K + 16 * ktc + 4 * (u / 6), (u / 6) * 4 * 272>(kwa);
        else if constexpr (u / 6 < 7) lds_rd_a<A_K + 16 * ktc + 4 * (u / 6 - 4), (u / 6 - 4) * 16>(kra);
      }
      if constexpr (kt >= 0 && u == 41) lds_rd_a<A_K + 16 * ktc + 12, 48>(kra);
      if constexpr (u == 47) wait_lgkm0();
      if constexpr (G > 0 && u >= 2) {
        constexpr int lo = (u - 2) * 34 / 45 < 34 ? (u - 2) * 34 / 45 : 34, hi = (u - 1) * 34 / 45 < 34 ? (u - 1) * 34 / 45 : 34;
        sfor<hi - lo>([&](auto KI) { qp(IC<2 * (G > 0 ? G - 1 : 0) + (lo + KI) / 17>{}, IC<(lo + KI) % 17>{}); });
      }
      SCHED;
    });
  });
  asm volatile("" ::"v"(pfd));
  STAMP(2);

  f32x4 sacc[2][6];
  u32x4 Pf[6][3];
  f32x4 oacc[2][8];
  f32x4 lacc[2];
  float mA = 0.f, mB = 0.f, li[2] = {0.f, 0.f};
  float ev[24];
  const float c = 0.08838834764831845f * 1.4426950408889634f;

  auto sm = [&](auto TI, auto JI) {
    constexpr int t = TI, j = JI;
    const f32x4(&S)[6] = sacc[t & 1];
    auto v = [&](int i) -> float { return S[i >> 2][i & 3]; };
    if constexpr (j == 0) {
      mA = max3(v(0), v(1), v(2));
      mB = max3(v(3), v(4), v(5));
    } else if constexpr (j < 5) {
      mA = max3(mA, v(4 * j + 2), v(4 * j + 3));
      mB = max3(mB, v(4 * j + 4), v(4 * j + 5));
    } else if constexpr (j == 5) {
      mA = __builtin_elementwise_maximum(max3(mA, v(22), v(23)), mB);
      xmax16(mA);
    } else if constexpr (j == 6) xmax32s(mA, -c);
    else if constexpr (j < 7 + 24) {
      constexpr int i = j - 7;
      ev[i] = __builtin_amdgcn_exp2f(__builtin_fmaf(v(i), c, mA));
    } else {
      constexpr int i = j - 31;
      Pf[t][i >> 2][i & 3] = pk_bf16(ev[2 * i], ev[2 * i + 1]);
    }
  };
  const int vo = lane * 16;
  const int oa1 = lb + V1_BASE + w * 16384 + vo, oa2 = lb + w * 8192 + vo;
  u32x4 ot;
  float om[8];
  auto oe = [&](auto TI, auto KI) {
    constexpr int t = TI, k = KI;
    if constexpr (k == 0) {
      asm volatile("" : "+v"(lacc[t & 1]));
      li[t & 1] = __builtin_amdgcn_rcpf(lacc[t & 1][0]);
    } else {
      constexpr int sp = (k - 1) / 13, e = (k - 1) % 13, r = E_RD[e], pk = E_PK[e];
      if constexpr (r >= 0) asm volatile("v_mul_f32 %0, %1, %2" : "=v"(om[r]) : "v"(oacc[t & 1][2 * sp + (r >> 2)][r & 3]), "v"(li[t & 1]));
      else if constexpr (pk >= 0) ot[pk] = pk_bf16(om[2 * pk], om[2 * pk + 1]);
      else {
        constexpr int fi = sp * 6 + TG[t >> 1][t & 1];
        if constexpr (fi < 16) lds_wr<fi * 1024>(oa1, ot);
        else lds_wr<(fi - 16) * 1024>(oa2, ot);
      }
    }
  };
  const int vtr = (4 * g + ((lane >> 2) & 3)) * 256 +
                  (((lane & 3) ^ (((g & 1) << 3) | (((lane >> 3) & 1) << 2) | ((lane >> 2) & 1))) << 4) + 8 * (lane & 1);
  const int vt1 = lb + V1_BASE + w * 16384 + vtr, vt2 = lb + w * 8192 + vtr;
  auto vrd = [&](auto T3, auto JI) {
    constexpr int t3 = T3, j = JI, f = j >> 1, hh = j & 1, sp = f >> 1, h = f & 1;
    constexpr int x = (sp << 6) ^ (h << 3), d = A_V + 4 * (t3 * 8 + f) + 2 * hh;
    if constexpr (t3 < 2) lds_rdtr_a<d, (32 * t3 + 16 * hh) * 256>(vt1 ^ x);
    else lds_rdtr_a<d, 16 * hh * 256>(vt2 ^ x);
  };
  const int vwoA = (8 * g + 4 * (g & 1)) * 1024 + (w * 128 + 8 * cchunk) * 2;
  const int vwoB = (8 * g - 4 * (g & 1) + 4) * 1024 + (w * 128 + 8 * cchunk) * 2;
  u32x4 WC[16][8];
  int wso[16];
  auto wsof = [&](auto SI) {
    constexpr int S = SI;
    wso[S] = (((w + (S >> 2)) & 3) * 128 + 32 * (S & 3)) * 1024;
    asm volatile("" : "+s"(wso[S]));
  };
  auto wold = [&](auto SI, auto II) {
    constexpr int S = SI, i = II;
    if constexpr (S < 3 && i == 0) wsof(SI);
    ld_w<(i & 3) * 1024>(WC[S][i], ro, i < 4 ? vwoA : vwoB, wso[S]);
  };

  int ob[4][2] = {{oa1, oa2}};
  auto ordx = [&](auto SI, auto TI, auto DI_) {
    constexpr int S = SI, t = TI, d = DI_, fi = (S & 3) * 6 + t;
    if constexpr (fi < 16) lds_rd_a<d, fi * 1024>(ob[S >> 2][0]);
    else lds_rd_a<d, (fi - 16) * 1024>(ob[S >> 2][1]);
  };
  auto ord = [&](auto SI, auto TI) { ordx(SI, TI, IC<C_B + 24 * (SI & 1) + 4 * TI>{}); };

  sfor<6>([&](auto TI) {
    constexpr int t = TI;
    constexpr int NQ = t == 0 ? 34 : 0, NS = t > 0 ? SM_N : 0;
    constexpr int NF = NQ + NS, M0 = t == 0 ? 2 : 0, MN = 24 - M0;
    BSTAMP(t);
    if constexpr (t == 4) wait_vm<B_NV>();
    if constexpr (t == 5) {
      wait_lgkm0();
      wait_vm<B_NV - 5>();
    }
    if constexpr (t == 5) BSTAMP(6);
    sfor<24>([&](auto MI) {
      constexpr int m = MI, s = m / 6, kt = m % 6;
      mf_v_av<A_K + 4 * (kt * 4 + s), s == 0>(sacc[t & 1][kt], Qf[t][s]);
      SCHED;
      if constexpr (m % 8 == 1 && t < 4 && 3 * t + m / 8 < B_NV) vm0(IC<B_V[(3 * t + m / 8) < B_NV ? 3 * t + m / 8 : 0]>{});
      if constexpr (m % 8 == 2 && t < 4 && 3 * t + m / 8 < B_NV) vdma(IC<B_V[(3 * t + m / 8) < B_NV ? 3 * t + m / 8 : 0]>{});
      if constexpr (t == 5 && m % 3 == 0 && m / 3 < PF_WO) ld_pf<A_B + 4 * (m / 3)>(ro, vk, (32 * b + 8 * w + m / 3) * 1024);
      if constexpr (t >= 4 && m % 3 != 0) vrd(IC<(t == 4 ? 0 : 2)>{}, IC<m - m / 3 - 1>{});
      constexpr int m0 = m < M0 ? 0 : m - M0, m1 = m + 1 < M0 ? 0 : m + 1 - M0;
      constexpr int lo = m0 * NF / MN, hi = m1 * NF / MN;
      sfor<hi - lo>([&](auto KI) {
        constexpr int k = lo + KI;
        if constexpr (k < NS) sm(IC<t - 1>{}, IC<k>{});
        else qp(IC<4 + (k - NS) / 17>{}, IC<(k - NS) % 17>{});
      });
      SCHED;
    });
  });
  STAMP(3);
  u32x4 ones = u32x4{0x3F803F80u, 0x3F803F80u, 0x3F803F80u, 0x3F803F80u};
  asm volatile("" : "+v"(ones));
  nop<1>();
  auto pop = [&](auto UI) {
    sfor<12>([&](auto KI) {
      constexpr int op = P_P.op[UI][KI], j = op % 1000;
      if constexpr (op >= PWL) wait_lgkm<j>();
      else if constexpr (op >= PWV) wait_vm<j>();
      else if constexpr (op >= PB) ob[(j - 500) >> 1][j & 1] = sob[(j - 500) >> 1][j & 1] + vo;
      else if constexpr (op >= PX) wsof(IC<3>{});
      else if constexpr (op >= PP) A[0][j] = aperm(WC[0], 0, j);
      else if constexpr (op >= PR) ord(IC<0>{}, IC<j>{});
      else if constexpr (op >= PW) wold(IC<j / 8>{}, IC<j % 8>{});
      else if constexpr (op >= PO) oe(IC<j / 64>{}, IC<j % 64>{});
      else if constexpr (op >= PVR) vrd(IC<1>{}, IC<j>{});
      else if constexpr (op >= PS) sm(IC<5>{}, IC<j>{});
      if constexpr (op > 0) SCHED;
    });
  };
  sfor<PV_N>([&](auto UI) {
    constexpr int u = UI, t = u / 27, m = u % 27, t3 = pv_t3(t, m), f = pv_f(t, m);
    if constexpr (m == 0) BSTAMP(7 + t);
    if constexpr (f > 0) mf_v_av<A_V + 4 * (t3 * 8 + f - 1), pv_z(t, m)>(oacc[t & 1][f - 1], Pf[t][t3]);
    else mf_v_vv<pv_z(t, m)>(lacc[t & 1], ones, Pf[t][t3]);
    SCHED;
    pop(UI);
  });
  BSTAMP(13);
  STAMP(4);

  auto tord = [&](auto PI) {
    constexpr int p = PI;
    ordx(IC<C_NS + C_P.bs[p]>{}, IC<C_P.bt[p]>{}, IC<C_B + 24 * ((C_NS + p / 6) & 1) + 4 * (p % 6)>{});
  };
  u32x4 CA[CT][8];
  auto cperm = [&](auto SI, auto KI) {
    constexpr int S = SI, k = KI;
    CA[S - C_NS][k >> 2][k & 3] = aperm(WC[S], k >> 2, k & 3);
  };
  sfor<8 * C_NS>([&](auto NI) {
    constexpr int n = NI, s = n >> 3, f = n & 7;
    constexpr bool cv = s >= C_S0;
    if constexpr (n == 8) BSTAMP(14);
    if constexpr (n == 16) BSTAMP(15);
    if constexpr (n == 24) {
      barrier();
      SCHED;
    }
    sfor<6>([&](auto TI) {
      constexpr int t = TI;
      mf_a_va<4 * (8 * t + f), C_B + 24 * (s & 1) + 4 * t, s == 0>(A[n & 1]);
      SCHED;
      constexpr int v = 2 * f + (t >> 2), li = c_cum(s) + v, ln = c_cum(s) + 2 * f + 2;
      constexpr bool ld = (t == 3 || t == 5) && v < c_n(s);
      if constexpr (ld) wold(IC<li / 8>{}, IC<li % 8>{});
      if constexpr (t == 0) {
        if constexpr (c_rdt(s, f) >= 0 && f < 7) {
          if constexpr (s + 1 < C_NS) ord(IC<s + 1>{}, IC<c_rdt(s, f)>{});
          else tord(IC<f>{});
        }
        if constexpr (f < 7 && ln % 8 == 0 && ln <= c_cum(s + 1)) wsof(IC<ln / 8>{});
        if constexpr (f == 1 && cv) wait_vm<2>();
        if constexpr (f == 7) {
          if constexpr (s + 1 < C_NS) wait_vm<c_cum(s + 1) - 8 * (s + 2)>();
          wait_lgkm0();
        }
      }
      if constexpr (n + 1 < 8 * C_NS && (t == 1 || t == 2)) {
        A[(n + 1) & 1][2 * t - 2] = aperm(WC[(n + 1) >> 3], (n + 1) & 7, 2 * t - 2);
        A[(n + 1) & 1][2 * t - 1] = aperm(WC[(n + 1) >> 3], (n + 1) & 7, 2 * t - 1);
      }
      if constexpr (s < PV_X) pop(IC<PV_N + 6 * n + t>{});
      constexpr int cg = f == 0 ? -1 : f < 4 ? (t == 4 ? f - 1 : -1) : t >= 3 ? 3 * (f - 3) + t - 3 : (f == 6 && t == 0 ? 15 : -1);
      if constexpr (cv && cg >= 0) {
        cperm(IC<(cv ? s + CT - 2 : C_NS)>{}, IC<2 * (cg < 0 ? 0 : cg)>{});
        cperm(IC<(cv ? s + CT - 2 : C_NS)>{}, IC<2 * (cg < 0 ? 0 : cg) + 1>{});
      }
      SCHED;
    });
  });
  const rsrc_t rp = __builtin_amdgcn_make_buffer_rsrc((void*)(part + (size_t)(((br * NBATCH + b) * 8 + hg) * 4 + w) * 24 * 64), 0,
                                                      24 * 1024, RSRC_W3);
  u32x4 sd[2];
  float tb[8];
  u32x4 sl[4];
  auto stp = [&](auto MI) {
    constexpr int m = MI, t = C_P.ct[m / 52], r = (m % 52) / 13, e = m % 13;
    if constexpr (m >= 308) __builtin_amdgcn_raw_buffer_store_b128(sl[m - 308], rp, vo + (m - 308) * 1024, t * 4096, AUX_PST);
    else if constexpr (m >= 260) {
      constexpr int pp = (m - 260) / 12, i = E_RD[(m - 260) % 12], k = E_PK[(m - 260) % 12];
      if constexpr (i >= 0) asm volatile("v_accvgpr_read_b32 %0, a[%c1]" : "=v"(tb[i]) : "i"(4 * (8 * t + 2 * pp + (i & 1)) + (i >> 1)));
      else sl[k][pp] = pk_bf16(tb[2 * k], tb[2 * k + 1]);
    } else if constexpr (E_RD[e] >= 0) asm volatile("v_accvgpr_read_b32 %0, a[%c1]" : "=v"(tb[E_RD[e]]) : "i"(4 * (8 * t + E_RD[e]) + r));
    else if constexpr (E_PK[e] >= 0) sd[r & 1][E_PK[e]] = pk_bf16(tb[2 * E_PK[e]], tb[2 * E_PK[e] + 1]);
    else __builtin_amdgcn_raw_buffer_store_b128(sd[r & 1], rp, vo + r * 1024, t * 4096, AUX_PST);
    if constexpr (e == 2 && m >= 13 && m < 273) asm volatile("" ::"v"(sd[(r & 1) ^ 1]));
    SCHED;
  };
  sfor<48 * CT>([&](auto UI) {
    constexpr int u = UI, v = u - (48 * CT - 48);
    constexpr int p = v < 0 ? u >> 3 : 6 * CT - 6 + v % 12 / 2, f = v < 0 ? u & 7 : 2 * (v / 12) + (v & 1);
    mf_a_va<4 * (8 * C_P.bt[p] + f), C_B + 24 * ((C_NS + p / 6) & 1) + 4 * (p % 6), false>(CA[C_P.bs[p]][f]);
    SCHED;
    sfor<3>([&](auto KI) {
      constexpr int op = C_P.op[u][KI], j = op % 1000;
      if constexpr (op >= TWL) wait_lgkm0();
      else if constexpr (op >= TWV) wait_vm<j>();
      else if constexpr (op >= TS) stp(IC<j>{});
      else if constexpr (op >= TB) tord(IC<j>{});
      else if constexpr (op >= TP) cperm(IC<14 + j / 32>{}, IC<j % 32>{});
      else if constexpr (op >= TL) wold(IC<15>{}, IC<j>{});
      SCHED;
    });
  });
  STAMP(5);
  nop<7>();
  sfor<6 * 52 - C_P.nst>([&](auto KI) { stp(IC<C_P.nst + KI>{}); });
  STAMP(6);
}

__global__ __launch_bounds__(256) void k2_reduce(const u32x4* __restrict__ part, unsigned short* __restrict__ out) {
  const int bb = blockIdx.x / 24, j = blockIdx.x % 24;
  const int tid = threadIdx.x;
  int vi = (bb * 8 * 6144 + j * 256 + tid) * 16;
  asm volatile("" : "+v"(vi));
  const rsrc_t rp = __builtin_amdgcn_make_buffer_rsrc((void*)part, 0, 2 * NBATCH * 8 * 6144 * 16, RSRC_W3);
  u32x4 v[8];
#pragma unroll
  for (int h = 0; h < 8; ++h) v[h] = __builtin_amdgcn_raw_buffer_load_b128(rp, vi, h * 6144 * 16, AUX_PLD);
  SCHED;
  const int t = j % 6, w = j / 6, br = bb >> 4, b = bb & 15;
  unsigned short* po = out + ((size_t)b * SQ + 16 * t) * 1024 + br * 512 + 128 * w;
  int vo = (tid & 15) * 2048 + (tid & 48) * 4 + (tid >> 6) * 16;
  unsigned hi = 0xffff0000u;
  asm volatile("" : "+s"(po), "+v"(vo), "+s"(hi));
  const rsrc_t ro = __builtin_amdgcn_make_buffer_rsrc((void*)po, 0, 15 * 2048 + 256, RSRC_W3);
  f32x2 s[4];
#pragma unroll
  for (int h = 0; h < 8; ++h) {
#pragma unroll
    for (int i = 0; i < 4; ++i) {
      const f32x2 x = f32x2{__builtin_bit_cast(float, v[h][i] << 16), __builtin_bit_cast(float, v[h][i] & hi)};
      s[i] = h ? s[i] + x : x;
    }
    asm volatile("" : "+v"(s[0]), "+v"(s[1]), "+v"(s[2]), "+v"(s[3]));
  }
  u32x4 o;
#pragma unroll
  for (int i = 0; i < 4; ++i) o[i] = pk_bf16(s[i][0], s[i][1]);
  __builtin_amdgcn_raw_buffer_store_b128(o, ro, vo, 0, 0);
  asm volatile(".Lk2_end:");
}

extern "C" void kernel_launch(void* const* d_in, const int* in_sizes, int n_in, void* d_out, int out_size, void* d_ws,
                              size_t ws_size, hipStream_t stream) {
  Ptrs p;
  for (int br = 0; br < 2; ++br) {
    p.x[br] = (const unsigned short*)d_in[5 * br + 0];
    p.k[br] = (const unsigned short*)d_in[5 * br + 1];
    p.v[br] = (const unsigned short*)d_in[5 * br + 2];
    p.wq[br] = (const unsigned short*)d_in[5 * br + 3];
    p.wo[br] = (const unsigned short*)d_in[5 * br + 4];
  }
  u32x4* part = (u32x4*)d_ws;
  (void)hipFuncSetAttribute((const void*)k1_fused, hipFuncAttributeMaxDynamicSharedMemorySize, LDS_BYTES);
  k1_fused<<<256, 256, LDS_BYTES, stream>>>(p, part
  );
  k2_reduce<<<2 * 16 * 96 * 64 / 256, 256, 0, stream>>>(part, (unsigned short*)d_out);
}
```
